```python
import torch

def _softmax(x, axis=-1):
    return torch.softmax(x, dim=axis)
def _gelu(x):
    return torch.nn.functional.gelu(x, approximate="tanh")
def _silu(x):
    return torch.nn.functional.silu(x)
def _sigmoid(x):
    return torch.sigmoid(x)
def _layernorm(x, axis=-1, eps=1e-5):
    m = x.mean(dim=axis, keepdim=True)
    v = x.var(dim=axis, keepdim=True, unbiased=False)
    return (x - m) / torch.sqrt(v + eps)
def _scatter_add(tgt, idx, src, axis=0):
    return tgt.clone().index_add_(axis, idx, src)
def _segment_sum(src, seg, n):
    out = torch.zeros((n,) + src.shape[1:], dtype=src.dtype)
    return out.index_add_(0, seg, src)
def _pad(x, pw):
    flat = tuple(v for pair in reversed(pw) for v in pair)
    return torch.nn.functional.pad(x, flat)

kernel_name = 'parallel_sum__spectral_gfnet__glu_gated_reglu__prompt'

def setup_inputs():

    _w = (lambda a: (lambda t: t.to(torch.bfloat16) if t.is_floating_point() else t)(torch.as_tensor(a)))
    def _A_setup():
        B, S, D = 1, 2048, 448
        Df = D // 2 + 1
        return {
            "x": _w((lambda *s: torch.randn(*s, dtype=torch.bfloat16) * 0.05)(B, S, D)),
            "w_r": _w((lambda *s: torch.randn(*s, dtype=torch.bfloat16) * 0.4)(S, Df) * 0.02),
            "w_i": _w((lambda *s: torch.randn(*s, dtype=torch.bfloat16) * 0.4)(S, Df) * 0.02),
        }
    _A_in = _A_setup()

    def _B_setup():
        B, S, D, F = 1, 2048, 448, 1792
        return {
            "x": _w((lambda *s: torch.randn(*s, dtype=torch.bfloat16) * 0.05)(B, S, D)),
            "W_gate": _w((lambda *s: torch.randn(*s, dtype=torch.bfloat16) * 0.2)(D, F) * (2.0 / D) ** 0.5),
            "W_up": _w((lambda *s: torch.randn(*s, dtype=torch.bfloat16) * 0.2)(D, F) * (2.0 / D) ** 0.5),
            "W_down": _w((lambda *s: torch.randn(*s, dtype=torch.bfloat16) * 0.2)(F, D) * (1.0 / F) ** 0.5),
        }
    _B_in = _B_setup()

    return {
        "A_x": _A_in["x"],
        "A_w_r": _A_in["w_r"],
        "A_w_i": _A_in["w_i"],
        "B_x": _B_in["x"],
        "B_W_gate": _B_in["W_gate"],
        "B_W_up": _B_in["W_up"],
        "B_W_down": _B_in["W_down"],
    }

def reference(A_x, A_w_r, A_w_i, B_x, B_W_gate, B_W_up, B_W_down):
    def _A_ref(x, w_r, w_i):
        B, S, D = x.shape
        X = (lambda x, axes=(-2,-1): torch.fft.rfft2(x.float(), dim=axes))(x, axes=(-2, -1))
        W = w_r + 1j * w_i
        X = X * W
        y = (lambda x, s, axes=(-2,-1): torch.fft.irfft2(x, s=s, dim=axes).to(torch.bfloat16))(X, (S, D), axes=(-2, -1))
        return y

    def _B_ref(x, W_gate, W_up, W_down):
        gate = torch.einsum("bsd,df->bsf", x, W_gate)
        up = torch.einsum("bsd,df->bsf", x, W_up)
        gate = gate * (lambda x, dt: x.to(dt))(gate > 0, gate.dtype)
        h = gate * up
        return torch.einsum("bsf,fd->bsd", h, W_down)

    x = A_x
    xn = _layernorm(x, axis=-1)
    return x + _A_ref(xn, A_w_r, A_w_i) + _B_ref(xn, B_W_gate, B_W_up, B_W_down)
```

```cpp
#include <hip/hip_runtime.h>
#include <hip/hip_bf16.h>
#include <stdint.h>
#include <utility>

typedef __attribute__((ext_vector_type(8))) short s16x8;
typedef __attribute__((ext_vector_type(4))) short s16x4;
typedef __attribute__((ext_vector_type(8))) __bf16 bf16x8;
typedef __attribute__((ext_vector_type(2))) __bf16 bf16x2;
typedef __attribute__((ext_vector_type(4))) float f32x4;
typedef __attribute__((ext_vector_type(4))) unsigned u32x4;
typedef __attribute__((address_space(3))) s16x4* lds_s16x4_p;
typedef __attribute__((address_space(3))) unsigned* lds_u32_p;
typedef __attribute__((address_space(1))) const unsigned* g_u32_p;
#define DI __device__ __forceinline__

constexpr int S_ = 2048, D_ = 448, F_ = 1792, DF_ = 225;

namespace tab {
constexpr double kPi = 3.14159265358979323846264338327950288;
constexpr double ccos(double x) {
  double t = 1, s = 1, x2 = x * x;
  for (int i = 1; i < 14; ++i) { t *= -x2 / ((2 * i - 1) * (2 * i)); s += t; }
  return s;
}
constexpr double csin(double x) {
  double t = x, s = x, x2 = x * x;
  for (int i = 1; i < 14; ++i) { t *= -x2 / ((2 * i) * (2 * i + 1)); s += t; }
  return s;
}
constexpr uint16_t f2bf(float f) {
  uint32_t u = __builtin_bit_cast(uint32_t, f);
  u += 0x7fffu + ((u >> 16) & 1u);
  return (uint16_t)(u >> 16);
}
struct Base { uint16_t v[448]; };
constexpr Base make_base() {
  Base b{};
  for (int i = 0; i < 448; ++i) {
    int k = i; if (k > 224) k = 448 - k;
    bool neg = false; if (k > 112) { k = 224 - k; neg = true; }
    double val = (k <= 56) ? ccos(2 * kPi * k / 448) : csin(2 * kPi * (112 - k) / 448);
    if (neg) val = -val;
    b.v[i] = f2bf((float)val);
  }
  return b;
}
constexpr Base kBase = make_base();
struct Blk { uint16_t v[16][448]; };
constexpr Blk make_blk(int jb) {
  Blk r{};
  for (int jj = 0; jj < 16; ++jj) {
    int j = jb * 16 + jj;
    int c = (j == 1) ? 224 : (j >> 1);
    int idx = ((j & 1) && j != 1) ? 112 : 0;
    for (int d = 0; d < 448; ++d) { r.v[jj][d] = kBase.v[idx]; idx += c; if (idx >= 448) idx -= 448; }
  }
  return r;
}
template <int J> constexpr Blk kBlk = make_blk(J);
struct Tab { Blk b[28]; };
}
__device__ const __attribute__((aligned(128))) tab::Tab g_tab = {{
  tab::kBlk<0>, tab::kBlk<1>, tab::kBlk<2>, tab::kBlk<3>, tab::kBlk<4>, tab::kBlk<5>, tab::kBlk<6>,
  tab::kBlk<7>, tab::kBlk<8>, tab::kBlk<9>, tab::kBlk<10>, tab::kBlk<11>, tab::kBlk<12>, tab::kBlk<13>,
  tab::kBlk<14>, tab::kBlk<15>, tab::kBlk<16>, tab::kBlk<17>, tab::kBlk<18>, tab::kBlk<19>, tab::kBlk<20>,
  tab::kBlk<21>, tab::kBlk<22>, tab::kBlk<23>, tab::kBlk<24>, tab::kBlk<25>, tab::kBlk<26>, tab::kBlk<27>}};

template <class Fn, int... I> DI void sfor_impl(Fn&& f, std::integer_sequence<int, I...>) { (f(std::integral_constant<int, I>{}), ...); }
template <int N, class Fn> DI void sfor(Fn&& f) { sfor_impl(f, std::make_integer_sequence<int, N>{}); }

DI f32x4 mfma16(u32x4 a, u32x4 b, f32x4 c) {
  return __builtin_amdgcn_mfma_f32_16x16x32_bf16(__builtin_bit_cast(bf16x8, a), __builtin_bit_cast(bf16x8, b), c, 0, 0, 0);
}
DI unsigned pk2(float lo, float hi) { bf16x2 p; p[0] = (__bf16)lo; p[1] = (__bf16)hi; return __builtin_bit_cast(unsigned, p); }
DI float bflo(unsigned u) { return __builtin_bit_cast(float, u << 16); }
DI float bfhi(unsigned u) { return __builtin_bit_cast(float, u & 0xffff0000u); }
DI float rbf(float f) { return (float)(__bf16)f; }
DI u32x4 tr_frag(const char* p0, const char* p1) {
  s16x4 a = __builtin_amdgcn_ds_read_tr16_b64_v4i16((lds_s16x4_p)p0);
  s16x4 b = __builtin_amdgcn_ds_read_tr16_b64_v4i16((lds_s16x4_p)p1);
  return __builtin_bit_cast(u32x4, __builtin_shufflevector(a, b, 0, 1, 2, 3, 4, 5, 6, 7));
}
DI void glds16(unsigned voff, const void* sbase, unsigned lds) {
  asm volatile("s_mov_b32 m0, %2\n\ts_nop 0\n\tglobal_load_lds_dwordx4 %0, %1" ::"v"(voff), "s"(sbase), "s"(lds) : "memory");
}
DI unsigned lds_addr(const void* p) { return (unsigned)(uintptr_t)(__attribute__((address_space(3))) const char*)p; }
DI float dot2(unsigned a, unsigned b, float c) { return __builtin_amdgcn_fdot2_f32_bf16(__builtin_bit_cast(bf16x2, a), __builtin_bit_cast(bf16x2, b), c, false); }
template <int OFF> DI void gload(u32x4& dst, const void* p) { asm volatile("global_load_dwordx4 %0, %1, off offset:%2" : "=v"(dst) : "v"(p), "n"(OFF) : "memory"); }
template <int N> DI void wait_vm() { asm volatile("s_waitcnt vmcnt(%0)" ::"n"(N) : "memory"); }
DI void bar() { asm volatile("s_waitcnt lgkmcnt(0)" ::: "memory"); __builtin_amdgcn_s_barrier(); asm volatile("" ::: "memory"); }

DI void stamp_to(unsigned long long& t) { __builtin_amdgcn_sched_barrier(0); asm volatile("s_memrealtime %0\n\ts_waitcnt lgkmcnt(0)" : "=s"(t) :: "memory"); __builtin_amdgcn_sched_barrier(0); }
#define STAMP_BEGIN(kid)
#define ST(kid, i)
#define STL(i)
#define STAMP_END(kid)
#define STAMP_ARG
#define STAMP_PASS

template <int NT>
DI void g1_stage(const char* sbase, const int (&aoff)[4], const u32x4 (&xf)[2][14], int ks0, f32x4 (&ag)[4][2], f32x4 (&au)[4][2]) {}

__global__ __launch_bounds__(512, 2) void k1(const uint16_t* __restrict__ x, const uint16_t* __restrict__ wr, const uint16_t* __restrict__ wi,
                                             const uint16_t* __restrict__ wg, const uint16_t* __restrict__ wu,
                                             u32x4* __restrict__ H, u32x4* __restrict__ Zc, u32x4* __restrict__ Wt STAMP_ARG) {
  extern __shared__ __attribute__((aligned(16))) char smem[];
  STAMP_BEGIN(0)
  const int tid = threadIdx.x, l = tid & 63, v = l & 15, q = l >> 4;
  const int w = __builtin_amdgcn_readfirstlane(tid >> 6), ws = w & 3, wn = w >> 2;
  const int b = blockIdx.x, xcd = b & 7, idx = b >> 3;
  const int rb = (xcd & 1) * 8 + (idx & 7), ct = (xcd >> 1) * 4 + (idx >> 3);
  const int pr = ct >> 1, ytype = ct & 1;
  const int f0 = 224 * pr + (ytype ? 128 : 0);

  u32x4 xf[2][14];
  {
    const uint16_t* p0 = x + (size_t)(128 * rb + 32 * (v & 3) + 4 * (2 * ws) + (v >> 2)) * D_ + 8 * q;
    const uint16_t* p1 = p0 + 4 * D_;
    sfor<14>([&](auto I) { constexpr int ks = I;
      gload<ks * 64>(xf[0][ks], p0);
      gload<ks * 64>(xf[1][ks], p1);
    });
  }
  const int gk = q | (((w >> 1) & 1) << 2);
  const int sch = v ^ (gk << 1);
  const bool sact = (!ytype) || (sch < 12);
  const unsigned soff = (unsigned)((4 * w + q) * F_ + f0 + sch * 8) * 2;
  const unsigned lds0 = lds_addr(smem) + w * 1024;
  auto stage = [&](int st, int slot) {
    if (sact) {
      const uint16_t* g0 = wg + (size_t)st * 64 * F_;
      const uint16_t* u0 = wu + (size_t)st * 64 * F_;
      const unsigned d = lds0 + slot * 32768;
      glds16(soff, g0, d);
      glds16(soff, g0 + 32 * F_, d + 8192);
      glds16(soff, u0, d + 16384);
      glds16(soff, u0 + 32 * F_, d + 24576);
    }
  };
  stage(0, 0); stage(1, 1); stage(2, 2); stage(3, 3); stage(4, 4);
  asm volatile("s_waitcnt vmcnt(20)"
               : "+v"(xf[0][0]), "+v"(xf[0][1]), "+v"(xf[0][2]), "+v"(xf[0][3]), "+v"(xf[0][4]), "+v"(xf[0][5]), "+v"(xf[0][6]),
                 "+v"(xf[0][7]), "+v"(xf[0][8]), "+v"(xf[0][9]), "+v"(xf[0][10]), "+v"(xf[0][11]), "+v"(xf[0][12]), "+v"(xf[0][13]) :: "memory");
  asm volatile(""
               : "+v"(xf[1][0]), "+v"(xf[1][1]), "+v"(xf[1][2]), "+v"(xf[1][3]), "+v"(xf[1][4]), "+v"(xf[1][5]), "+v"(xf[1][6]),
                 "+v"(xf[1][7]), "+v"(xf[1][8]), "+v"(xf[1][9]), "+v"(xf[1][10]), "+v"(xf[1][11]), "+v"(xf[1][12]), "+v"(xf[1][13]) :: "memory");
  ST(0, 1)
#pragma unroll
  for (int u = 0; u < 2; ++u) {
    float s = 0.f, ss = 0.f;
#pragma unroll
    for (int ks = 0; ks < 14; ++ks)
#pragma unroll
      for (int e = 0; e < 4; ++e) { s = dot2(xf[u][ks][e], 0x3f803f80u, s); ss = dot2(xf[u][ks][e], xf[u][ks][e], ss); }
    s += __shfl_xor(s, 16); s += __shfl_xor(s, 32);
    ss += __shfl_xor(ss, 16); ss += __shfl_xor(ss, 32);
    const float mean = s * (1.0f / D_);
    ss = fmaxf(ss - s * mean, 0.f);
    const float mb = rbf(mean);
    const float sd = rbf(sqrtf(rbf(rbf(ss * (1.0f / D_)) + 1e-5f)));
    const float rs = 1.0f / sd;
#pragma unroll
    for (int ks = 0; ks < 14; ++ks)
#pragma unroll
      for (int e = 0; e < 4; ++e) {
        unsigned d = pk2(bflo(xf[u][ks][e]) - mb, bfhi(xf[u][ks][e]) - mb);
        xf[u][ks][e] = pk2(bflo(d) * rs, bfhi(d) * rs);
      }
  }
  ST(0, 2)
  const bool qrole = ytype && wn;
  if (qrole && pr < 7) {
    const uint16_t* T = (const uint16_t*)&g_tab;
#pragma unroll
    for (int cti = 0; cti < 2; ++cti) {
      const int c = 32 * pr + 16 * cti + v;
      const uint16_t* tp = T + (size_t)(2 * c) * D_ + 8 * q;
      f32x4 zr[2] = {}, zi[2] = {};
      u32x4 tr_[14], ti_[14];
      sfor<14>([&](auto I) { constexpr int ks = I;
        gload<ks * 64>(tr_[ks], tp);
      });
      asm volatile("s_waitcnt vmcnt(0)"
               : "+v"(tr_[0]), "+v"(tr_[1]), "+v"(tr_[2]), "+v"(tr_[3]), "+v"(tr_[4]), "+v"(tr_[5]), "+v"(tr_[6]),
                 "+v"(tr_[7]), "+v"(tr_[8]), "+v"(tr_[9]), "+v"(tr_[10]), "+v"(tr_[11]), "+v"(tr_[12]), "+v"(tr_[13]) :: "memory");
#pragma unroll
      for (int ks = 0; ks < 14; ++ks) { zr[0] = mfma16(xf[0][ks], tr_[ks], zr[0]); zr[1] = mfma16(xf[1][ks], tr_[ks], zr[1]); }
      sfor<14>([&](auto I) { constexpr int ks = I;
        gload<D_ * 2 + ks * 64>(ti_[ks], tp);
      });
      asm volatile("s_waitcnt vmcnt(0)"
               : "+v"(ti_[0]), "+v"(ti_[1]), "+v"(ti_[2]), "+v"(ti_[3]), "+v"(ti_[4]), "+v"(ti_[5]), "+v"(ti_[6]),
                 "+v"(ti_[7]), "+v"(ti_[8]), "+v"(ti_[9]), "+v"(ti_[10]), "+v"(ti_[11]), "+v"(ti_[12]), "+v"(ti_[13]) :: "memory");
#pragma unroll
      for (int ks = 0; ks < 14; ++ks) { zi[0] = mfma16(xf[0][ks], ti_[ks], zi[0]); zi[1] = mfma16(xf[1][ks], ti_[ks], zi[1]); }
#pragma unroll
      for (int uu = 0; uu < 2; ++uu) {
        const int u = 2 * ws + uu;
        u32x4 o; for (int e = 0; e < 4; ++e) o[e] = pk2(zr[uu][e], zi[uu][e]);
        Zc[c * 512 + (u >> 2) * 256 + rb * 16 + (u & 3) * 4 + q] = o;
      }
    }
  }
  ST(0, 3)
  f32x4 ag[4][2] = {}, au[4][2] = {};
  const int ta = (l & 15) >> 2, tp_ = l & 3;
  const int rg = ta | ((q & 1) << 2);
  int aoff[4];
#pragma unroll
  for (int i = 0; i < 4; ++i) aoff[i] = (8 * q + ta) * 256 + (((4 * wn + i) ^ rg) * 32) + 8 * tp_;
  sfor<7>([&](auto I) { constexpr int st = I;
    if constexpr (st == 0) wait_vm<16>(); else if constexpr (st == 1) wait_vm<12>(); else if constexpr (st == 2) wait_vm<12>();
    else if constexpr (st == 3) wait_vm<12>(); else if constexpr (st == 4) wait_vm<8>(); else if constexpr (st == 5) wait_vm<4>(); else wait_vm<0>();
    bar();
    STL(4 + st)
    if constexpr (st == 1 || st == 2) stage(st + 4, st - 1);
    const char* sb = smem + (st % 5) * 32768;
#pragma unroll
    for (int kk = 0; kk < 2; ++kk) {
#pragma unroll
      for (int i = 0; i < 4; ++i) {
        if (i < 2 || !qrole) {
          const char* p = sb + aoff[i] + kk * 8192;
          u32x4 fg = tr_frag(p, p + 1024);
          u32x4 fu = tr_frag(p + 16384, p + 16384 + 1024);
          ag[i][0] = mfma16(fg, xf[0][2 * st + kk], ag[i][0]);
          ag[i][1] = mfma16(fg, xf[1][2 * st + kk], ag[i][1]);
          au[i][0] = mfma16(fu, xf[0][2 * st + kk], au[i][0]);
          au[i][1] = mfma16(fu, xf[1][2 * st + kk], au[i][1]);
        }
      }
    }
  });
  ST(0, 11)
#pragma unroll
  for (int a = 0; a < 2; ++a) {
    if (a == 0 || !qrole) {
#pragma unroll
      for (int uu = 0; uu < 2; ++uu) {
        float hv[8];
#pragma unroll
        for (int j = 0; j < 8; ++j) {
          float g = rbf(ag[2 * a + (j >> 2)][uu][j & 3]), up = rbf(au[2 * a + (j >> 2)][uu][j & 3]);
          hv[j] = (g > 0.f ? g : 0.f * g) * up;
        }
        u32x4 o; for (int e = 0; e < 4; ++e) o[e] = pk2(hv[2 * e], hv[2 * e + 1]);
        const int fs = (f0 >> 5) + 2 * wn + a;
        H[((size_t)(rb * 8 + 2 * ws + uu) * 56 + fs) * 64 + l] = o;
      }
    }
  }
  ST(0, 12)
  if (!ytype) {
    const int c = 32 * pr + (tid & 31), vv = tid >> 5;
    if (c < DF_) {
      const int wb = rb >> 2, qb = rb & 3;
#pragma unroll
      for (int t = 0; t < 2; ++t) {
        u32x4 o;
#pragma unroll
        for (int r = 0; r < 4; ++r) {
          const int m = 16 * wb + vv + 64 * (16 * t + 4 * qb + r);
          o[r] = (unsigned)wr[m * DF_ + c] | ((unsigned)wi[m * DF_ + c] << 16);
        }
        Wt[c * 512 + wb * 128 + t * 64 + 16 * qb + vv] = o;
      }
    }
  }
  STAMP_END(0)
}

__global__ __launch_bounds__(256) void k2(const u32x4* __restrict__ Zc, const u32x4* __restrict__ Wt, uint16_t* __restrict__ Yt STAMP_ARG) {
  __shared__ __attribute__((aligned(16))) char R[32 * 256];
  STAMP_BEGIN(1)
  const int tid = threadIdx.x, l = tid & 63, v = l & 15, q = l >> 4;
  const int w = __builtin_amdgcn_readfirstlane(tid >> 6);
  const int c = blockIdx.x, zc = (c == 224) ? 0 : c;
  u32x4 z[2][4], wv[2];
#pragma unroll
  for (int t = 0; t < 2; ++t)
#pragma unroll
    for (int ks = 0; ks < 4; ++ks) z[t][ks] = Zc[zc * 512 + t * 256 + ks * 64 + l];
#pragma unroll
  for (int t = 0; t < 2; ++t) wv[t] = Wt[c * 512 + w * 128 + t * 64 + l];
  u32x4 M1[4], M2[4], C32[2], S32[2], N32[2];
  const int k1 = 16 * w + v;
#pragma unroll
  for (int ks = 0; ks < 4; ++ks)
#pragma unroll
    for (int e = 0; e < 4; ++e) {
      const int n1 = 16 * ks + 4 * q + e;
      const float a = (float)((n1 * k1) & 63) * (1.0f / 64);
      const float cs = __builtin_amdgcn_cosf(a), sn = __builtin_amdgcn_sinf(a);
      M1[ks][e] = pk2(cs, sn); M2[ks][e] = pk2(-sn, cs);
    }
#pragma unroll
  for (int t = 0; t < 2; ++t)
#pragma unroll
    for (int e = 0; e < 4; ++e) {
      float cs[2], sn[2];
#pragma unroll
      for (int h = 0; h < 2; ++h) {
        const int j = 2 * e + h, bb = 16 * (j >> 2) + 4 * q + (j & 3);
        const float a = (float)(((16 * t + v) * bb) & 31) * (1.0f / 32);
        cs[h] = __builtin_amdgcn_cosf(a); sn[h] = __builtin_amdgcn_sinf(a);
      }
      C32[t][e] = pk2(cs[0], cs[1]); S32[t][e] = pk2(sn[0], sn[1]); N32[t][e] = pk2(-sn[0], -sn[1]);
    }
  float t1c[2][4], t1s[2][4], t2c[2][4], t2s[2][4];
#pragma unroll
  for (int t = 0; t < 2; ++t)
#pragma unroll
    for (int r = 0; r < 4; ++r) {
      const float a1 = (float)((16 * t + 4 * q + r) * k1) * (1.0f / 2048);
      t1c[t][r] = __builtin_amdgcn_cosf(a1); t1s[t][r] = __builtin_amdgcn_sinf(a1);
      const float a2 = (float)((16 * t + v) * (16 * w + 4 * q + r)) * (1.0f / 2048);
      t2c[t][r] = __builtin_amdgcn_cosf(a2); t2s[t][r] = __builtin_amdgcn_sinf(a2);
    }
  ST(1, 1)
  const bool special = (c == 0) || (c == 224);
  if (special) {
    const int sh = (c == 224) ? 16 : 0;
#pragma unroll
    for (int t = 0; t < 2; ++t)
#pragma unroll
      for (int ks = 0; ks < 4; ++ks)
#pragma unroll
        for (int e = 0; e < 4; ++e) z[t][ks][e] = (z[t][ks][e] >> sh) & 0xffffu;
  }
  const float scale = (special ? 1.0f : 2.0f) / (448.0f * 2048.0f);
  f32x4 pr[2] = {}, pi[2] = {};
#pragma unroll
  for (int ks = 0; ks < 4; ++ks)
#pragma unroll
    for (int t = 0; t < 2; ++t) { pr[t] = mfma16(z[t][ks], M1[ks], pr[t]); pi[t] = mfma16(z[t][ks], M2[ks], pi[t]); }
  u32x4 fr, fi;
  {
    float a[8], bq[8];
#pragma unroll
    for (int t = 0; t < 2; ++t)
#pragma unroll
      for (int r = 0; r < 4; ++r) {
        a[4 * t + r] = pr[t][r] * t1c[t][r] + pi[t][r] * t1s[t][r];
        bq[4 * t + r] = pi[t][r] * t1c[t][r] - pr[t][r] * t1s[t][r];
      }
#pragma unroll
    for (int e = 0; e < 4; ++e) { fr[e] = pk2(a[2 * e], a[2 * e + 1]); fi[e] = pk2(bq[2 * e], bq[2 * e + 1]); }
  }
  ST(1, 2)
  f32x4 qr[2], qi[2];
  const f32x4 zero = {0.f, 0.f, 0.f, 0.f};
#pragma unroll
  for (int t = 0; t < 2; ++t) {
    qr[t] = mfma16(C32[t], fr, zero); qi[t] = mfma16(C32[t], fi, zero);
    qr[t] = mfma16(S32[t], fi, qr[t]); qi[t] = mfma16(N32[t], fr, qi[t]);
  }
  {
    float a[8], bq[8];
#pragma unroll
    for (int t = 0; t < 2; ++t)
#pragma unroll
      for (int r = 0; r < 4; ++r) {
        const float wre = bflo(wv[t][r]) * scale, wim = bfhi(wv[t][r]) * scale;
        a[4 * t + r] = qr[t][r] * wre - qi[t][r] * wim;
        bq[4 * t + r] = qr[t][r] * wim + qi[t][r] * wre;
      }
#pragma unroll
    for (int e = 0; e < 4; ++e) { fr[e] = pk2(a[2 * e], a[2 * e + 1]); fi[e] = pk2(bq[2 * e], bq[2 * e + 1]); }
  }
#pragma unroll
  for (int t = 0; t < 2; ++t) {
    f32x4 rr = mfma16(fr, C32[t], zero), ri = mfma16(fi, C32[t], zero);
    rr = mfma16(fi, N32[t], rr); ri = mfma16(fr, S32[t], ri);
    u32x4 o;
#pragma unroll
    for (int r = 0; r < 4; ++r) {
      const float a = rr[r] * t2c[t][r] - ri[r] * t2s[t][r];
      const float bq = rr[r] * t2s[t][r] + ri[r] * t2c[t][r];
      o[r] = pk2(a, -bq);
    }
    const int n2 = 16 * t + v;
    *(u32x4*)(R + n2 * 256 + (((4 * w + q) ^ (n2 >> 1)) * 16)) = o;
  }
  __syncthreads();
  ST(1, 3)
  f32x4 yr[2] = {}, yi[2] = {};
#pragma unroll
  for (int ks = 0; ks < 4; ++ks)
#pragma unroll
    for (int t = 0; t < 2; ++t) {
      const u32x4 rf = *(const u32x4*)(R + (2 * v + t) * 256 + (((4 * ks + q) ^ v) * 16));
      yr[t] = mfma16(M1[ks], rf, yr[t]); yi[t] = mfma16(M2[ks], rf, yi[t]);
    }
  const int pos = 128 * (4 * w + q) + 8 * v;
  u32x4 ore, oim;
  ore[0] = pk2(yr[0][0], yr[0][1]); ore[1] = pk2(yr[0][2], yr[0][3]); ore[2] = pk2(yr[1][0], yr[1][1]); ore[3] = pk2(yr[1][2], yr[1][3]);
  oim[0] = pk2(-yi[0][0], -yi[0][1]); oim[1] = pk2(-yi[0][2], -yi[0][3]); oim[2] = pk2(-yi[1][0], -yi[1][1]); oim[3] = pk2(-yi[1][2], -yi[1][3]);
  ST(1, 4)
  const int jr = (c == 224) ? 1 : 2 * c;
  *(u32x4*)(Yt + (size_t)jr * S_ + pos) = ore;
  if (!special) *(u32x4*)(Yt + (size_t)(jr + 1) * S_ + pos) = oim;
  STAMP_END(1)
}

constexpr int NS3 = 8;
__global__ __launch_bounds__(512, 2) void k3(const uint16_t* __restrict__ x, const uint16_t* __restrict__ wd, const u32x4* __restrict__ H,
                                             const uint16_t* __restrict__ Yt, uint16_t* __restrict__ out STAMP_ARG) {
  extern __shared__ __attribute__((aligned(16))) char smem[];
  STAMP_BEGIN(2)
  const int tid = threadIdx.x, l = tid & 63, v = l & 15, q = l >> 4;
  const int w = __builtin_amdgcn_readfirstlane(tid >> 6);
  const int b = blockIdx.x, xcd = b & 7, idx = b >> 3;
  const int rt = 4 * xcd + (idx & 3), dt = idx >> 2;
  const int rb = rt >> 1, u0 = (rt & 1) * 4;
  const int ep = tid >> 3, ech = tid & 7;
  const int pp = (rt & 1) * 64 + ep;
  const int srow = 128 * rb + 32 * (pp & 3) + (pp >> 2);
  u32x4 xres;
  gload<0>(xres, x + (size_t)srow * D_ + dt * 64 + ech * 8);

  const int srcch = (l & 7) ^ (((l >> 4) & 3) << 1);
  const int srow8 = 8 * w + (l >> 3);
  const uint16_t* T = (const uint16_t*)&g_tab;
  const unsigned aoffs = (unsigned)(srow8 * D_ + dt * 64 + srcch * 8) * 2;
  const unsigned hoffs = (unsigned)(((rb * 8 + u0 + (w >> 1)) * 56 + (w & 1)) * 64 + l) * 16;
  const unsigned yoffs = (unsigned)(srow8 * S_ + rt * 64 + srcch * 8) * 2;
  const unsigned lds0 = lds_addr(smem) + w * 1024;
  auto stage = [&](int st) {
    const unsigned d = lds0 + (st % NS3) * 16384;
    if (st < 28) { glds16(aoffs, wd + (size_t)st * 64 * D_, d); glds16(hoffs, H + st * 128, d + 8192); }
    else { glds16(aoffs, T + (size_t)(st - 28) * 64 * D_, d); glds16(yoffs, Yt + (size_t)(st - 28) * 64 * S_, d + 8192); }
  };
  sfor<NS3 - 1>([&](auto I) { stage(I); });
  const int kh = w >> 2, wdh = (w >> 1) & 1, wsx = w & 1;
  const int ta = (l & 15) >> 2, tp_ = l & 3;
  const int sw = ((q & 1) << 1) | (ta >> 1);
  int aoff[2], boffY[2], boffH[2];
#pragma unroll
  for (int i = 0; i < 2; ++i) {
    aoff[i] = (32 * kh + 4 * q + ta) * 128 + (((2 * wdh + i) ^ sw) * 32) + 8 * tp_;
    boffY[i] = 8192 + (32 * kh + 4 * q + ta) * 128 + (((2 * wsx + i) ^ sw) * 32) + 8 * tp_;
    boffH[i] = 8192 + ((2 * wsx + i) * 2 + kh) * 1024 + l * 16;
  }
  f32x4 acc[2][2] = {};
  sfor<35>([&](auto I) { constexpr int st = I;
    constexpr int ahead = (34 - st) < (NS3 - 2) ? (34 - st) : (NS3 - 2);
    wait_vm<2 * ahead>();
    bar();
    if constexpr (st % 4 == 0) { STL(1 + st / 4) }
    if constexpr (st + NS3 - 1 < 35) stage(st + NS3 - 1);
    const char* sb = smem + (st % NS3) * 16384;
    u32x4 fa[2], fb[2];
#pragma unroll
    for (int i = 0; i < 2; ++i) {
      fa[i] = tr_frag(sb + aoff[i], sb + aoff[i] + 2048);
      if constexpr (st < 28) fb[i] = *(const u32x4*)(sb + boffH[i]);
      else fb[i] = tr_frag(sb + boffY[i], sb + boffY[i] + 2048);
    }
#pragma unroll
    for (int i = 0; i < 2; ++i)
#pragma unroll
      for (int j = 0; j < 2; ++j) acc[i][j] = mfma16(fa[i], fb[j], acc[i][j]);
  });
  ST(2, 11)
  char* red = smem + NS3 * 16384 - 2 * 64 * 272;
  asm volatile("" : "+v"(xres));
  bar();
#pragma unroll
  for (int i = 0; i < 2; ++i)
#pragma unroll
    for (int j = 0; j < 2; ++j)
      *(f32x4*)(red + kh * (64 * 272) + ((2 * wsx + j) * 16 + v) * 272 + ((2 * wdh + i) * 16 + 4 * q) * 4) = acc[i][j];
  bar();
  {
    const char* r0 = red + ep * 272 + ech * 32;
    f32x4 a0 = *(const f32x4*)r0, a1 = *(const f32x4*)(r0 + 16);
    f32x4 b0 = *(const f32x4*)(r0 + 64 * 272), b1 = *(const f32x4*)(r0 + 64 * 272 + 16);
    a0 += b0; a1 += b1;
    u32x4 o;
    o[0] = pk2(a0[0] + bflo(xres[0]), a0[1] + bfhi(xres[0]));
    o[1] = pk2(a0[2] + bflo(xres[1]), a0[3] + bfhi(xres[1]));
    o[2] = pk2(a1[0] + bflo(xres[2]), a1[1] + bfhi(xres[2]));
    o[3] = pk2(a1[2] + bflo(xres[3]), a1[3] + bfhi(xres[3]));
    *(u32x4*)(out + (size_t)srow * D_ + dt * 64 + ech * 8) = o;
  }
  STAMP_END(2)
}

extern "C" void kernel_launch(void* const* d_in, const int* in_sizes, int n_in, void* d_out, int out_size, void* d_ws, size_t ws_size,
                              hipStream_t stream) {
  const uint16_t* x = (const uint16_t*)d_in[0];
  const uint16_t* wr = (const uint16_t*)d_in[1];
  const uint16_t* wi = (const uint16_t*)d_in[2];
  const uint16_t* wg = (const uint16_t*)d_in[4];
  const uint16_t* wu = (const uint16_t*)d_in[5];
  const uint16_t* wd = (const uint16_t*)d_in[6];
  char* ws = (char*)d_ws;
  u32x4* H = (u32x4*)ws;
  u32x4* Zc = (u32x4*)(ws + (8u << 20));
  u32x4* Wt = (u32x4*)(ws + (10u << 20));
  uint16_t* Yt = (uint16_t*)(ws + (12u << 20));
  (void)hipFuncSetAttribute((const void*)k1, hipFuncAttributeMaxDynamicSharedMemorySize, 163840);
  (void)hipFuncSetAttribute((const void*)k3, hipFuncAttributeMaxDynamicSharedMemorySize, NS3 * 16384);
  k1<<<256, 512, 163840, stream>>>(x, wr, wi, wg, wu, H, Zc, Wt STAMP_PASS);
  k2<<<225, 256, 0, stream>>>(Zc, Wt, Yt STAMP_PASS);
  k3<<<224, 512, NS3 * 16384, stream>>>(x, wd, H, Yt, (uint16_t*)d_out STAMP_PASS);
}
```

```cpp
#include <hip/hip_runtime.h>
#include <hip/hip_bf16.h>
#include <stdint.h>
#include <utility>

typedef __attribute__((ext_vector_type(8))) short s16x8;
typedef __attribute__((ext_vector_type(4))) short s16x4;
typedef __attribute__((ext_vector_type(8))) __bf16 bf16x8;
typedef __attribute__((ext_vector_type(2))) __bf16 bf16x2;
typedef __attribute__((ext_vector_type(4))) float f32x4;
typedef __attribute__((ext_vector_type(4))) unsigned u32x4;
typedef __attribute__((address_space(3))) s16x4* lds_s16x4_p;
typedef __attribute__((address_space(3))) unsigned* lds_u32_p;
typedef __attribute__((address_space(1))) const unsigned* g_u32_p;
#define DI __device__ __forceinline__

constexpr int S_ = 2048, D_ = 448, F_ = 1792, DF_ = 225;

namespace tab {
constexpr double kPi = 3.14159265358979323846264338327950288;
constexpr double ccos(double x) {
  double t = 1, s = 1, x2 = x * x;
  for (int i = 1; i < 14; ++i) { t *= -x2 / ((2 * i - 1) * (2 * i)); s += t; }
  return s;
}
constexpr double csin(double x) {
  double t = x, s = x, x2 = x * x;
  for (int i = 1; i < 14; ++i) { t *= -x2 / ((2 * i) * (2 * i + 1)); s += t; }
  return s;
}
constexpr uint16_t f2bf(float f) {
  uint32_t u = __builtin_bit_cast(uint32_t, f);
  u += 0x7fffu + ((u >> 16) & 1u);
  return (uint16_t)(u >> 16);
}
struct Base { uint16_t v[448]; };
constexpr Base make_base() {
  Base b{};
  for (int i = 0; i < 448; ++i) {
    int k = i; if (k > 224) k = 448 - k;
    bool neg = false; if (k > 112) { k = 224 - k; neg = true; }
    double val = (k <= 56) ? ccos(2 * kPi * k / 448) : csin(2 * kPi * (112 - k) / 448);
    if (neg) val = -val;
    b.v[i] = f2bf((float)val);
  }
  return b;
}
constexpr Base kBase = make_base();
struct Blk1 { uint16_t v[4096]; };
constexpr Blk1 make_blk1(int id) {
  Blk1 r{};
  const int hb = id / 7, st = id % 7;
  for (int i = 0; i < 4; ++i)
    for (int v = 0; v < 16; ++v) {
      const int cc = 32 * hb + 16 * (i & 1) + v, ri = i >> 1;
      const int c = (cc == 0 && ri) ? 224 : cc;
      int idx = (((ri && cc != 0) ? 112 : 0) + c * (64 * st)) % 448;
      for (int d = 0; d < 64; ++d) {
        r.v[((i * 2 + (d >> 5)) * 64 + ((d >> 3) & 3) * 16 + v) * 8 + (d & 7)] = kBase.v[idx];
        idx += c; if (idx >= 448) idx -= 448;
      }
    }
  return r;
}
template <int J> constexpr Blk1 kBlk1 = make_blk1(J);
struct Tab1 { Blk1 b[49]; };
template <int... J> constexpr Tab1 make_tab1(std::integer_sequence<int, J...>) { return Tab1{{kBlk1<J>...}}; }
constexpr float bf2f(uint16_t u) { return __builtin_bit_cast(float, (uint32_t)u << 16); }
constexpr float eps64() { float a = 0; for (int k = 0; k < 7; ++k) a += bf2f(kBase.v[64 * k]); return a * 64.f; }
constexpr float kEps64 = eps64();
}
__device__ const __attribute__((aligned(128))) tab::Tab1 g_tab1 = tab::make_tab1(std::make_integer_sequence<int, 49>{});

namespace tab {
struct Blk2 { uint16_t v[7168]; };
constexpr Blk2 make_blk2(int dt) {
  Blk2 r{};
  for (int j = 0; j < 448; ++j) {
    const int c = (j == 1) ? 224 : (j >> 1);
    int idx = ((((j & 1) && j != 1) ? 112 : 0) + c * (16 * dt)) % 448;
    const int ks = j >> 5, e = ((j >> 4) & 1) * 4 + (j & 3), q = (j >> 2) & 3;
    for (int v = 0; v < 16; ++v) {
      r.v[(ks * 64 + q * 16 + v) * 8 + e] = kBase.v[idx];
      idx += c; if (idx >= 448) idx -= 448;
    }
  }
  return r;
}
template <int J> constexpr Blk2 kBlk2 = make_blk2(J);
struct Tab2 { Blk2 b[28]; };
template <int... J> constexpr Tab2 make_tab2(std::integer_sequence<int, J...>) { return Tab2{{kBlk2<J>...}}; }
}
__device__ const __attribute__((aligned(128))) tab::Tab2 g_tab2 = tab::make_tab2(std::make_integer_sequence<int, 28>{});

template <class Fn, int... I> DI void sfor_impl(Fn&& f, std::integer_sequence<int, I...>) { (f(std::integral_constant<int, I>{}), ...); }
template <int N, class Fn> DI void sfor(Fn&& f) { sfor_impl(f, std::make_integer_sequence<int, N>{}); }

DI f32x4 mfma16(u32x4 a, u32x4 b, f32x4 c) {
  return __builtin_amdgcn_mfma_f32_16x16x32_bf16(__builtin_bit_cast(bf16x8, a), __builtin_bit_cast(bf16x8, b), c, 0, 0, 0);
}
DI unsigned pk2(float lo, float hi) { bf16x2 p; p[0] = (__bf16)lo; p[1] = (__bf16)hi; return __builtin_bit_cast(unsigned, p); }
DI float bflo(unsigned u) { return __builtin_bit_cast(float, u << 16); }
DI float bfhi(unsigned u) { return __builtin_bit_cast(float, u & 0xffff0000u); }
DI float rbf(float f) { return (float)(__bf16)f; }
DI u32x4 tr_frag(const char* p0, const char* p1) {
  s16x4 a = __builtin_amdgcn_ds_read_tr16_b64_v4i16((lds_s16x4_p)p0);
  s16x4 b = __builtin_amdgcn_ds_read_tr16_b64_v4i16((lds_s16x4_p)p1);
  return __builtin_bit_cast(u32x4, __builtin_shufflevector(a, b, 0, 1, 2, 3, 4, 5, 6, 7));
}
DI void glds16(unsigned voff, const void* sbase, unsigned lds) {
  asm volatile("s_mov_b32 m0, %2\n\ts_nop 0\n\tglobal_load_lds_dwordx4 %0, %1" ::"v"(voff), "s"(sbase), "s"(lds) : "memory");
}
DI unsigned lds_addr(const void* p) { return (unsigned)(uintptr_t)(__attribute__((address_space(3))) const char*)p; }
DI float dot2(unsigned a, unsigned b, float c) { return __builtin_amdgcn_fdot2_f32_bf16(__builtin_bit_cast(bf16x2, a), __builtin_bit_cast(bf16x2, b), c, false); }
template <int OFF> DI void gload(u32x4& dst, const void* p) { asm volatile("global_load_dwordx4 %0, %1, off offset:%2" : "=v"(dst) : "v"(p), "n"(OFF) : "memory"); }
template <int N> DI void wait_vm() { asm volatile("s_waitcnt vmcnt(%0)" ::"n"(N) : "memory"); }
DI void bar() { asm volatile("s_waitcnt lgkmcnt(0)" ::: "memory"); __builtin_amdgcn_s_barrier(); asm volatile("" ::: "memory"); }

#define STINS "s_memrealtime"
DI void stamp_to(unsigned long long& t) { __builtin_amdgcn_sched_barrier(0); asm volatile(STINS " %0\n\ts_waitcnt lgkmcnt(0)" : "=s"(t) :: "memory"); __builtin_amdgcn_sched_barrier(0); }
#define STAMP_BEGIN(kid)
#define ST(kid, i)
#define STL(i)
#define STAMP_END(kid)
#define STAMP_ARG
#define STAMP_PASS

#define SB() __builtin_amdgcn_sched_barrier(0)
constexpr unsigned ONES = 0x3f803f80u;
constexpr int L_MISC = 9 * 16384;
DI u32x4 lds128(const char* p) { return *(const u32x4*)p; }
DI void st_sc1(u32x4 v, void* p) { asm volatile("global_store_dwordx4 %0, %1, off sc1\n\ts_nop 1" ::"v"(p), "v"(v) : "memory"); }

struct G1State {
  u32x4 xf[2][14];
  u32x4 wf[2][4];
  f32x4 acc[4][2];
  f32x4 cs, sm, gr;
};
template <bool AT, bool P1, int N, int C, class GA, class GB>
DI void g1_half(G1State& S, const char* xb, const char* wb, const int (&xo)[2][2], const int (&wo)[4], GA&& ga, GB&& gb) {
  constexpr bool DR = N >= 0, DM = C >= 0;
  constexpr int kk = DR ? (N & 1) : 0, nb = DR ? (N & 1) : 0, cb = DM ? (C & 1) : 0, cc = DM ? C : 0, nn = DR ? N : 0;
  const u32x4 ones = {ONES, ONES, ONES, ONES};
  const f32x4 zero = {0.f, 0.f, 0.f, 0.f};
  auto rdw = [&](int i) {
    if constexpr (DR) {
      if constexpr (AT) S.wf[nb][i] = lds128(wb + wo[i] + kk * 1024);
      else S.wf[nb][i] = tr_frag(wb + wo[i] + kk * 4096, wb + wo[i] + kk * 4096 + 512);
    }
  };
  auto mm = [&](int i) {
    if constexpr (DM) {
#pragma unroll
      for (int uu = 0; uu < 2; ++uu) {
        const f32x4 c0 = (cc == 0) ? zero : S.acc[i][uu];
        if constexpr (AT) S.acc[i][uu] = mfma16(S.xf[uu][cc], S.wf[cb][i], c0);
        else S.acc[i][uu] = mfma16(S.wf[cb][i], S.xf[uu][cc], c0);
      }
    }
  };
  if constexpr (DR && P1) { S.xf[0][nn] = lds128(xb + xo[0][kk]); S.xf[1][nn] = lds128(xb + xo[1][kk]); }
  if constexpr (!P1) rdw(0);
  mm(0); SB();
  if constexpr (P1) rdw(0); else rdw(1);
  mm(1); SB();
  ga(); SB();
  if constexpr (P1) rdw(1); else rdw(2);
  mm(2); SB();
  if constexpr (P1) rdw(2); else rdw(3);
  mm(3); SB();
  if constexpr (P1) rdw(3);
  if constexpr (DM) {
    if constexpr (!AT) S.cs = mfma16(S.wf[cb][0], ones, (cc == 0) ? zero : S.cs);
    if constexpr (P1) {
      S.sm = mfma16(ones, S.xf[0][cc], (cc == 0) ? zero : S.sm);
      S.gr = mfma16(S.xf[0][cc], S.xf[0][cc], (cc == 0) ? zero : S.gr);
    }
  }
  SB();
  gb(); SB();
}

typedef unsigned long long u64;
typedef __attribute__((address_space(1))) u64 gu64;
typedef __attribute__((address_space(3))) unsigned lu32;
typedef __attribute__((ext_vector_type(2))) unsigned u32x2;
#define RLX_AGENT __ATOMIC_RELAXED, __HIP_MEMORY_SCOPE_AGENT
constexpr unsigned OFF_Z = 8u << 20, OFF_W = 10u << 20, OFF_Y = 12u << 20, OFF_C = 14u << 20;
constexpr int FH0 = 512, FZ0 = 1536, FW0 = 2048, FY0 = 3072;
constexpr int SPIN_CAP = 100000;
constexpr int UBASE[4] = {0, 6 * 16384, 2 * 16384, 4 * 16384};
constexpr int P2SLOT[7] = {6, 7, 2, 3, 4, 5, 8};

template <int OFF> DI void ld_sc1(u32x4& d, unsigned voff, const void* sb) { asm volatile("global_load_dwordx4 %0, %1, %2 offset:%3 sc1" : "=v"(d) : "v"(voff), "s"(sb), "n"(OFF) : "memory"); }
template <int OFF> DI void ld_pl(u32x4& d, unsigned voff, const void* sb) { asm volatile("global_load_dwordx4 %0, %1, %2 offset:%3" : "=v"(d) : "v"(voff), "s"(sb), "n"(OFF) : "memory"); }
DI void ld_u16(unsigned& d, unsigned voff, const void* sb) { asm volatile("global_load_ushort %0, %1, %2" : "=v"(d) : "v"(voff), "s"(sb) : "memory"); }
DI void ld_flag(u32x2& d, unsigned voff, const void* sb) { asm volatile("global_load_dwordx2 %0, %1, %2 sc1" : "=v"(d) : "v"(voff), "s"(sb) : "memory"); }
DI void st16_sc1(u32x4 v, unsigned voff, void* sb) { asm volatile("global_store_dwordx4 %0, %1, %2 sc1\n\ts_nop 1" ::"v"(voff), "v"(v), "s"(sb) : "memory"); }
DI void st_flag(u64* p, u64 tag) { __hip_atomic_store((gu64*)p, tag, RLX_AGENT); }
DI void pin(u32x4& a) { asm volatile("" : "+v"(a)); }
DI void pin(u32x2& a) { asm volatile("" : "+v"(a)); }
DI void pin(unsigned& a) { asm volatile("" : "+v"(a)); }
DI void poll_rest(u32x2 fv, unsigned voff, const void* sb, unsigned tlo, unsigned thi) {
  for (int it = 0; it < SPIN_CAP; ++it) {
    if (__all(fv[0] == tlo && fv[1] == thi)) break;
    __builtin_amdgcn_s_sleep(1);
    ld_flag(fv, voff, sb); wait_vm<0>(); pin(fv);
  }
  asm volatile("" ::: "memory");
}
DI unsigned lds_arrive(lu32* c, int l) {
  asm volatile("s_waitcnt lgkmcnt(0)" ::: "memory");
  unsigned old = 0;
  if (l == 0) old = __hip_atomic_fetch_add(c, 1u, __ATOMIC_RELAXED, __HIP_MEMORY_SCOPE_WORKGROUP);
  return __builtin_amdgcn_readfirstlane(old);
}
DI void lds_wait(lu32* c, unsigned n) {
  for (int it = 0; it < (1 << 24); ++it) {
    if (__builtin_amdgcn_readfirstlane(__hip_atomic_load(c, __ATOMIC_RELAXED, __HIP_MEMORY_SCOPE_WORKGROUP)) >= n) break;
  }
  asm volatile("" ::: "memory");
}

template <int MODE>
__global__ __launch_bounds__(512, 2) void fwd(const uint16_t* __restrict__ x, const uint16_t* __restrict__ wr, const uint16_t* __restrict__ wi,
                                              const uint16_t* __restrict__ wg, const uint16_t* __restrict__ wu, const uint16_t* __restrict__ wd,
                                              char* __restrict__ ws_, uint16_t* __restrict__ out STAMP_ARG) {
  extern __shared__ __attribute__((aligned(16))) char smem[];
  STAMP_BEGIN(0)
  const int tid = threadIdx.x, l = tid & 63, v = l & 15, q = l >> 4;
  const int w = __builtin_amdgcn_readfirstlane(tid >> 6), ws = w & 3, wn = w >> 2;
  const int b = blockIdx.x, xcd = b & 7, idx = b >> 3;
  const int r8 = l >> 3, ch = l & 7;
  const int ta = (l & 15) >> 2, tp_ = l & 3;
  const unsigned lds0 = lds_addr(smem);
  u64* ctl = (u64*)(ws_ + OFF_C);
  lu32* cnt = (lu32*)(smem + L_MISC + 2048);
  float* stats = (float*)(smem + L_MISC);
  float* csum = (float*)(smem + L_MISC + 1024);
  u32x2 ep;
  ld_flag(ep, 0u, ctl);
  if (tid < 8) cnt[tid] = 0;
  const bool g2cu = idx < 28;
  const int rt = 4 * xcd + (idx & 3), dt = g2cu ? (idx >> 2) : 6;
  const int rot = 2 * (idx & 3) + dt;
  auto fs_of = [&](int i) { return wn ? 6 * ws + (i + rot) % 6 : 24 + 8 * ws + ((i + rot) & 7); };
  const unsigned sw8 = (unsigned)(ch ^ (((r8 >> 1) & 3) << 1));
  const unsigned dvo = (unsigned)(r8 * D_ + dt * 64 + sw8 * 8) * 2;
  auto gd = [&](int i, int n) { glds16(dvo, wd + (size_t)(32 * fs_of(i) + 8 * n) * D_, lds0 + UBASE[i & 3] + w * 4096 + n * 1024); };
  unsigned tlo, thi;
  auto take_epoch = [&] { pin(ep); const u64 e = (((u64)__builtin_amdgcn_readfirstlane(ep[1]) << 32) | (unsigned)__builtin_amdgcn_readfirstlane(ep[0])) + 1; tlo = (unsigned)e; thi = (unsigned)(e >> 32); };

  if constexpr (MODE <= 1) {
    const int rb = (xcd & 1) * 8 + (idx & 7), g = xcd >> 1, ii = idx >> 3;
    const bool atype = ii == 3;
    const int unit1 = 3 * g + ii, unit2 = 12 + 4 * g + ii;
    const int r0 = (rb + 2 * ii + g) % 7;
    unsigned xvo[2];
#pragma unroll
    for (int a = 0; a < 2; ++a) {
      const int p = 16 * w + 8 * a + r8;
      xvo[a] = (unsigned)((128 * rb + 32 * (p & 3) + (p >> 2)) * D_ + ((ch ^ (4 * a + (r8 >> 1))) * 8)) * 2;
    }
    const unsigned wvo = (unsigned)((8 * w + r8) * F_ + ((ch ^ ((((r8 >> 1) & 1) | ((w & 1) << 1)) << 1)) * 8)) * 2;
    const unsigned tvo = (unsigned)((w & 3) * 2048 + l * 16);
    const int hb = (2 * g + wn) < 6 ? (2 * g + wn) : 6;
    const char* t1p = (const char*)&g_tab1 + hb * (7 * 8192);
    auto kof = [&](int st) { int k = st + r0; return k >= 7 ? k - 7 : k; };
    auto gx = [&](int st, int a) { glds16(xvo[a], x + kof(st) * 64, lds0 + (st % 3) * 32768 + w * 2048 + a * 1024); };
    const unsigned w1vo = atype ? tvo : wvo;
    const char* w1p[2] = {atype ? t1p : (const char*)(wg + unit1 * 64), atype ? t1p + 1024 : (const char*)(wu + unit1 * 64)};
    const unsigned w1ks = atype ? 8192u : 64u * F_ * 2u;
    const unsigned w1d = lds0 + 16384 + (atype ? w * 2048 : w * 1024), w1m = atype ? 1024u : 8192u;
    auto gw1 = [&](int st, int m) { glds16(w1vo, w1p[m] + (size_t)(kof(st) * w1ks), w1d + (st % 3) * 32768 + m * w1m); };
    auto gw2 = [&](int st, int m) { glds16(wvo, (m ? wu : wg) + (size_t)kof(st) * 64 * F_ + unit2 * 64, lds0 + P2SLOT[st] * 16384 + m * 8192 + w * 1024); };
    gx(0, 0); gx(0, 1); gw1(0, 0); gw1(0, 1);
    const int fpr = 2 * g + (ii >> 1), ft = ii & 1;
    const int fc = 32 * fpr + (tid & 31), fvv = tid >> 5;
    unsigned fw[8];
    {
      const unsigned fo = (unsigned)((16 * (rb >> 2) + fvv + 64 * (16 * ft + 4 * (rb & 3))) * DF_ + (fc < DF_ ? fc : DF_ - 1)) * 2;
#pragma unroll
      for (int r = 0; r < 4; ++r) { ld_u16(fw[2 * r], fo, wr + r * 64 * DF_); ld_u16(fw[2 * r + 1], fo, wi + r * 64 * DF_); }
    }
    __builtin_amdgcn_s_barrier();
    gx(1, 0); gx(1, 1); gw1(1, 0); gw1(1, 1);
    int xo[2][2], wo[4], to[4];
#pragma unroll
    for (int uu = 0; uu < 2; ++uu)
#pragma unroll
      for (int kk = 0; kk < 2; ++kk) xo[uu][kk] = (16 * (2 * ws + (uu ^ wn)) + v) * 128 + (((4 * kk + q) ^ (v >> 1)) * 16);
#pragma unroll
    for (int i = 0; i < 4; ++i) {
      const int tile = i ^ ws;
      wo[i] = (tile >> 1) * 8192 + (8 * q + ta) * 128 + (((2 * wn + (tile & 1)) ^ ((ta >> 1) | ((q & 1) << 1))) * 32) + 8 * tp_;
      to[i] = wn * 8192 + i * 2048 + l * 16;
    }
    G1State S;
    auto nop = [] {};
    auto pass1 = [&](auto ATc) { constexpr bool AT = ATc;
      sfor<7>([&](auto I) { constexpr int st = I;
        if constexpr (st == 0) wait_vm<12>(); else if constexpr (st == 2) wait_vm<5>(); else wait_vm<4>();
        bar();
        if constexpr (st == 0) { take_epoch(); STL(1) }
        if constexpr (st == 1) {
#pragma unroll
          for (int r = 0; r < 8; ++r) pin(fw[r]);
          u32x4 o;
#pragma unroll
          for (int r = 0; r < 4; ++r) o[r] = fw[2 * r] | (fw[2 * r + 1] << 16);
          if (fc < DF_) st16_sc1(o, (unsigned)(fc * 512 + (rb >> 2) * 128 + ft * 64 + 16 * (rb & 3) + fvv) * 16, ws_ + OFF_W);
          SB();
        }
        if constexpr (st == 3) { if (tid == 0) st_flag(ctl + FW0 + fpr * 32 + rb * 2 + ft, ((u64)thi << 32) | tlo); SB(); }
        const char* xb = smem + (st % 3) * 32768;
        const char* wb = xb + 16384;
        if constexpr (st < 5) {
          g1_half<AT, true, 2 * st, 2 * st - 1>(S, xb, wb, xo, AT ? to : wo, [&] { gx(st + 2, 0); }, [&] { gx(st + 2, 1); });
          g1_half<AT, true, 2 * st + 1, 2 * st>(S, xb, wb, xo, AT ? to : wo, [&] { gw1(st + 2, 0); }, [&] { gw1(st + 2, 1); });
        } else {
          g1_half<AT, true, 2 * st, 2 * st - 1>(S, xb, wb, xo, AT ? to : wo, [&] { gw2(2 * st - 10, 0); }, [&] { gw2(2 * st - 10, 1); });
          g1_half<AT, true, 2 * st + 1, 2 * st>(S, xb, wb, xo, AT ? to : wo, [&] { gw2(2 * st - 9, 0); }, [&] { gw2(2 * st - 9, 1); });
        }
      });
      g1_half<AT, true, -1, 13>(S, smem, smem, xo, AT ? to : wo, [&] { gw2(4, 0); }, [&] { gw2(4, 1); });
    };
    if (atype) pass1(std::true_type{}); else pass1(std::false_type{});
    ST(0, 2)
    {
      const float s = S.sm[0];
      const float ssq = (v & 2) ? ((v & 1) ? S.gr[3] : S.gr[2]) : ((v & 1) ? S.gr[1] : S.gr[0]);
      const float mean = s * (1.0f / D_);
      const float var = fmaxf(ssq - s * mean, 0.f) * (1.0f / D_);
      const float sd = rbf(__builtin_amdgcn_sqrtf(rbf(rbf(var) + 1e-5f)));
      if (q == (v >> 2)) *(float2*)(stats + 2 * (16 * (2 * ws + wn) + v)) = make_float2(rbf(mean), __builtin_amdgcn_rcpf(sd));
      if (!atype && v == 0) *(f32x4*)(csum + (wn * 4 + ws) * 16 + 4 * q) = S.cs;
    }
    const float lo0 = (ws & 2) ? -__builtin_inff() : 0.f, lo1 = (ws & 2) ? 0.f : -__builtin_inff();
    auto epi_h = [&](const float* cs, int unit) {
      f32x4 c[4];
#pragma unroll
      for (int i = 0; i < 4; ++i) c[i] = *(const f32x4*)(cs + (wn * 4 + (i ^ ws)) * 16 + 4 * q);
#pragma unroll
      for (int uu = 0; uu < 2; ++uu) {
        const int u = 2 * ws + (uu ^ wn);
        const float2 st = *(const float2*)(stats + 2 * (16 * u + v));
        const float r2 = st.y * st.y;
        unsigned o[2][2];
#pragma unroll
        for (int a = 0; a < 2; ++a) {
          float hv[4];
#pragma unroll
          for (int r = 0; r < 4; ++r) {
            const float p0 = S.acc[a][uu][r] - st.x * c[a][r], p1 = S.acc[a + 2][uu][r] - st.x * c[a + 2][r];
            hv[r] = (fmaxf(p0, lo0) * r2) * fmaxf(p1, lo1);
          }
          o[a][0] = pk2(hv[0], hv[1]); o[a][1] = pk2(hv[2], hv[3]);
        }
        u32x4 ov;
        ov[0] = (ws & 1) ? o[1][0] : o[0][0]; ov[1] = (ws & 1) ? o[1][1] : o[0][1];
        ov[2] = (ws & 1) ? o[0][0] : o[1][0]; ov[3] = (ws & 1) ? o[0][1] : o[1][1];
        st16_sc1(ov, (unsigned)l * 16, ws_ + (size_t)((rb * 8 + u) * 56 + 2 * unit + wn) * 1024);
      }
    };
    wait_vm<6>();
    bar();
    ST(0, 3)
    g1_half<false, false, 0, -1>(S, smem, smem + P2SLOT[0] * 16384, xo, wo, [&] { gw2(5, 0); gw2(5, 1); }, [&] { gw2(6, 0); gw2(6, 1); });
    if (atype) {
      char* zs = smem + w * 4096;
#pragma unroll
      for (int uu = 0; uu < 2; ++uu) {
        const int u = 2 * ws + (uu ^ wn);
        const f32x4 s0 = *(const f32x4*)(stats + 2 * (16 * u + 4 * q)), s1 = *(const f32x4*)(stats + 2 * (16 * u + 4 * q) + 4);
        const float mb[4] = {s0[0], s0[2], s1[0], s1[2]}, rs[4] = {s0[1], s0[3], s1[1], s1[3]};
#pragma unroll
        for (int cti = 0; cti < 2; ++cti) {
          const int c = 32 * hb + 16 * cti + v;
          const float ts = c == 0 ? 448.f : ((c & 63) == 0 ? tab::kEps64 : 0.f);
          u32x4 o;
#pragma unroll
          for (int r = 0; r < 4; ++r) o[r] = pk2((S.acc[cti][uu][r] - mb[r] * ts) * rs[r], S.acc[cti + 2][uu][r] * rs[r]);
          *(u32x4*)(zs + (16 * cti + v) * 128 + ((((u & 1) * 4 + q) ^ (v & 7)) * 16)) = o;
        }
      }
      if (2 * g + wn < 7) {
#pragma unroll
        for (int n = 0; n < 4; ++n) {
          const u32x4 o = lds128(zs + (8 * n + r8) * 128 + ((ch ^ r8) * 16));
          st16_sc1(o, (unsigned)(r8 * 512 + ch) * 16, ws_ + OFF_Z + (size_t)((32 * hb + 8 * n) * 512 + (ws >> 1) * 256 + rb * 16 + 8 * (ws & 1)) * 16);
        }
      }
    } else epi_h(csum, unit1);
    SB();
    ST(0, 4)
    auto gdm = [&](int i, int n) { if constexpr (MODE == 0) gd(i, n); };
    constexpr int DM_ = MODE == 0 ? 4 : 0;
    g1_half<false, false, 1, 0>(S, smem, smem + P2SLOT[0] * 16384, xo, wo, nop, nop);
    sfor<6>([&](auto I) { constexpr int st = I + 1;
      if constexpr (st == 2) { wait_vm<6 + DM_>(); bar(); }
      if constexpr (st == 4) {
        wait_vm<2 * DM_>(); bar();
        if (tid == 0) st_flag(atype ? ctl + FZ0 + g * 16 + rb : ctl + FH0 + rb * 32 + unit1, ((u64)thi << 32) | tlo);
        SB();
      }
      if constexpr (st == 6) bar();
      const char* wb = smem + P2SLOT[st] * 16384;
      if constexpr (st == 1 || st == 2 || st == 4 || st == 6) {
        g1_half<false, false, 2 * st, 2 * st - 1>(S, smem, wb, xo, wo, [&] { gdm(st >> 1, 0); }, [&] { gdm(st >> 1, 1); });
        g1_half<false, false, 2 * st + 1, 2 * st>(S, smem, wb, xo, wo, [&] { gdm(st >> 1, 2); }, [&] { gdm(st >> 1, 3); });
      } else {
        g1_half<false, false, 2 * st, 2 * st - 1>(S, smem, wb, xo, wo, nop, nop);
        g1_half<false, false, 2 * st + 1, 2 * st>(S, smem, wb, xo, wo, nop, nop);
      }
    });
    g1_half<false, false, -1, 13>(S, smem, smem, xo, wo, nop, nop);
    ST(0, 5)
    if (v == 0) *(f32x4*)(csum + 128 + (wn * 4 + ws) * 16 + 4 * q) = S.cs;
    bar();
    epi_h(csum + 128, unit2);
    SB();
    ST(0, 6)
    if constexpr (MODE == 1) {
      wait_vm<0>();
      if (lds_arrive(cnt + 0, l) == 7 && l == 0) st_flag(ctl + FH0 + rb * 32 + unit2, ((u64)thi << 32) | tlo);
      STAMP_END(0)
      return;
    }
  } else {
    if constexpr (MODE == 3) { sfor<16>([&](auto I) { gd(I >> 2, I & 3); }); }
    wait_vm<0>();
    bar();
    take_epoch();
  }
  const u64 tag = ((u64)thi << 32) | tlo;
  auto publish_h2 = [&] {
    if constexpr (MODE == 0) {
      const int rb = (xcd & 1) * 8 + (idx & 7);
      if (lds_arrive(cnt + 0, l) == 7 && l == 0) st_flag(ctl + FH0 + rb * 32 + 12 + 4 * (xcd >> 1) + (idx >> 3), tag);
    }
  };
  const int rb2 = rt >> 1;
  const int ep_ = tid >> 3;
  const int pp = (rt & 1) * 64 + ep_;
  const unsigned ovo = (unsigned)((128 * rb2 + 32 * (pp & 3) + (pp >> 2)) * D_ + dt * 64 + ch * 8) * 2;
  u32x4 xres;
  if constexpr (MODE != 2) ld_pl<0>(xres, ovo, x);
  const int sw = ((q & 1) << 1) | (ta >> 1);
  const int rank = g2cu ? 32 + idx * 8 + xcd : (idx - 28) * 8 + xcd;
  const char* hbase = ws_ + (size_t)((rb2 * 8 + (rt & 1) * 4) * 56) * 1024;
  const unsigned lvo = (unsigned)l * 16;
  f32x4 acc[4][4];
  u32x4 hf[8][4];
  auto ldh = [&](auto I) { constexpr int i = I;
    const char* p = hbase + fs_of(i) * 1024;
    ld_sc1<0>(hf[i][0], lvo, p); ld_sc1<0>(hf[i][1], lvo, p + 56 * 1024); ld_sc1<0>(hf[i][2], lvo, p + 112 * 1024); ld_sc1<0>(hf[i][3], lvo, p + 168 * 1024);
  };
  const f32x4 zero = {0.f, 0.f, 0.f, 0.f};
  if (wn == 0) {
    if (MODE != 3 && rank < DF_) {
      const int c = rank, zc = (c == 224) ? 0 : c;
      const unsigned fvo = (unsigned)(l < 16 ? FZ0 + (zc >> 6) * 16 + l : FW0 + (c >> 5) * 32 + (l & 31)) * 8;
      u32x2 fv;
      ld_flag(fv, fvo, ctl);
      u32x4 M1[4], M2[4], C32[2], S32[2], N32[2];
      const int k1 = 16 * w + v;
#pragma unroll
      for (int ks = 0; ks < 4; ++ks)
#pragma unroll
        for (int e = 0; e < 4; ++e) {
          const int n1 = 16 * ks + 4 * q + e;
          const float a = (float)((n1 * k1) & 63) * (1.0f / 64);
          const float cs = __builtin_amdgcn_cosf(a), sn = __builtin_amdgcn_sinf(a);
          M1[ks][e] = pk2(cs, sn); M2[ks][e] = pk2(-sn, cs);
        }
#pragma unroll
      for (int ks = 0; ks < 4; ++ks) { pin(M1[ks]); pin(M2[ks]); }
      wait_vm<0>(); pin(fv);
      publish_h2();
      poll_rest(fv, fvo, ctl, tlo, thi);
      ST(0, 7)
      u32x4 z[2][4], wv[2];
      {
        const char* zp = ws_ + OFF_Z + (size_t)zc * 8192;
        ld_sc1<0>(z[0][0], lvo, zp); ld_sc1<0>(z[1][0], lvo, zp + 4096);
        ld_sc1<1024>(z[0][1], lvo, zp); ld_sc1<1024>(z[1][1], lvo, zp + 4096);
        ld_sc1<2048>(z[0][2], lvo, zp); ld_sc1<2048>(z[1][2], lvo, zp + 4096);
        ld_sc1<3072>(z[0][3], lvo, zp); ld_sc1<3072>(z[1][3], lvo, zp + 4096);
        const char* wp = ws_ + OFF_W + (size_t)c * 8192 + w * 2048;
        ld_sc1<0>(wv[0], lvo, wp); ld_sc1<1024>(wv[1], lvo, wp);
      }
#pragma unroll
      for (int t = 0; t < 2; ++t)
#pragma unroll
        for (int e = 0; e < 4; ++e) {
          float cs[2], sn[2];
#pragma unroll
          for (int h = 0; h < 2; ++h) {
            const int j = 2 * e + h, bb = 16 * (j >> 2) + 4 * q + (j & 3);
            const float a = (float)(((16 * t + v) * bb) & 31) * (1.0f / 32);
            cs[h] = __builtin_amdgcn_cosf(a); sn[h] = __builtin_amdgcn_sinf(a);
          }
          C32[t][e] = pk2(cs[0], cs[1]); S32[t][e] = pk2(sn[0], sn[1]); N32[t][e] = S32[t][e] ^ 0x80008000u;
        }
      float t1c[2][4], t1s[2][4], t2c[2][4], t2s[2][4];
#pragma unroll
      for (int t = 0; t < 2; ++t)
#pragma unroll
        for (int r = 0; r < 4; ++r) {
          const float a1 = (float)((16 * t + 4 * q + r) * k1) * (1.0f / 2048);
          t1c[t][r] = __builtin_amdgcn_cosf(a1); t1s[t][r] = __builtin_amdgcn_sinf(a1);
          const float a2 = (float)((16 * t + v) * (16 * w + 4 * q + r)) * (1.0f / 2048);
          t2c[t][r] = __builtin_amdgcn_cosf(a2); t2s[t][r] = __builtin_amdgcn_sinf(a2);
          asm volatile("" : "+v"(t1c[t][r]), "+v"(t1s[t][r]), "+v"(t2c[t][r]), "+v"(t2s[t][r]));
        }
#pragma unroll
      for (int t = 0; t < 2; ++t) { pin(C32[t]); pin(S32[t]); pin(N32[t]); }
      wait_vm<0>();
#pragma unroll
      for (int t = 0; t < 2; ++t) { pin(wv[t]);
#pragma unroll
        for (int ks = 0; ks < 4; ++ks) pin(z[t][ks]); }
      ST(0, 8)
      const bool special = (c == 0) || (c == 224);
      if (special) {
        const int sh = (c == 224) ? 16 : 0;
#pragma unroll
        for (int t = 0; t < 2; ++t)
#pragma unroll
          for (int ks = 0; ks < 4; ++ks)
#pragma unroll
            for (int e = 0; e < 4; ++e) z[t][ks][e] = (z[t][ks][e] >> sh) & 0xffffu;
      }
      const float scale = (special ? 1.0f : 2.0f) / (448.0f * 2048.0f);
      f32x4 pr[2] = {}, pi[2] = {};
#pragma unroll
      for (int ks = 0; ks < 4; ++ks)
#pragma unroll
        for (int t = 0; t < 2; ++t) { pr[t] = mfma16(z[t][ks], M1[ks], pr[t]); pi[t] = mfma16(z[t][ks], M2[ks], pi[t]); }
      u32x4 fr, fi;
      {
        float a[8], bq[8];
#pragma unroll
        for (int t = 0; t < 2; ++t)
#pragma unroll
          for (int r = 0; r < 4; ++r) {
            a[4 * t + r] = pr[t][r] * t1c[t][r] + pi[t][r] * t1s[t][r];
            bq[4 * t + r] = pi[t][r] * t1c[t][r] - pr[t][r] * t1s[t][r];
          }
#pragma unroll
        for (int e = 0; e < 4; ++e) { fr[e] = pk2(a[2 * e], a[2 * e + 1]); fi[e] = pk2(bq[2 * e], bq[2 * e + 1]); }
      }
      f32x4 qr[2], qi[2];
#pragma unroll
      for (int t = 0; t < 2; ++t) {
        qr[t] = mfma16(C32[t], fr, zero); qi[t] = mfma16(C32[t], fi, zero);
        qr[t] = mfma16(S32[t], fi, qr[t]); qi[t] = mfma16(N32[t], fr, qi[t]);
      }
      {
        float a[8], bq[8];
#pragma unroll
        for (int t = 0; t < 2; ++t)
#pragma unroll
          for (int r = 0; r < 4; ++r) {
            const float wre = bflo(wv[t][r]) * scale, wim = bfhi(wv[t][r]) * scale;
            a[4 * t + r] = qr[t][r] * wre - qi[t][r] * wim;
            bq[4 * t + r] = qr[t][r] * wim + qi[t][r] * wre;
          }
#pragma unroll
        for (int e = 0; e < 4; ++e) { fr[e] = pk2(a[2 * e], a[2 * e + 1]); fi[e] = pk2(bq[2 * e], bq[2 * e + 1]); }
      }
      char* R = smem + L_MISC + 4096;
#pragma unroll
      for (int t = 0; t < 2; ++t) {
        f32x4 rr = mfma16(fr, C32[t], zero), ri = mfma16(fi, C32[t], zero);
        rr = mfma16(fi, N32[t], rr); ri = mfma16(fr, S32[t], ri);
        u32x4 o;
#pragma unroll
        for (int r = 0; r < 4; ++r) {
          const float a = rr[r] * t2c[t][r] - ri[r] * t2s[t][r];
          const float bq = rr[r] * t2s[t][r] + ri[r] * t2c[t][r];
          o[r] = pk2(a, -bq);
        }
        const int n2 = 16 * t + v;
        *(u32x4*)(R + n2 * 256 + (((4 * w + q) ^ (n2 >> 1)) * 16)) = o;
      }
      lds_arrive(cnt + 1, l);
      lds_wait(cnt + 1, 4);
      f32x4 yr[2] = {}, yi[2] = {};
#pragma unroll
      for (int ks = 0; ks < 4; ++ks)
#pragma unroll
        for (int t = 0; t < 2; ++t) {
          const u32x4 rf = lds128(R + (2 * v + t) * 256 + (((4 * ks + q) ^ v) * 16));
          yr[t] = mfma16(M1[ks], rf, yr[t]); yi[t] = mfma16(M2[ks], rf, yi[t]);
        }
      u32x4 ore, oim;
      ore[0] = pk2(yr[0][0], yr[0][1]); ore[1] = pk2(yr[0][2], yr[0][3]); ore[2] = pk2(yr[1][0], yr[1][1]); ore[3] = pk2(yr[1][2], yr[1][3]);
      oim[0] = pk2(-yi[0][0], -yi[0][1]); oim[1] = pk2(-yi[0][2], -yi[0][3]); oim[2] = pk2(-yi[1][0], -yi[1][1]); oim[3] = pk2(-yi[1][2], -yi[1][3]);
      const int jr = (c == 224) ? 1 : 2 * c;
      char* yp = ws_ + OFF_Y + (size_t)jr * (S_ * 2) + w * 1024;
      st16_sc1(ore, lvo, yp);
      if (!special) st16_sc1(oim, lvo, yp + S_ * 2);
      ST(0, 9)
    } else { wait_vm<0>(); publish_h2(); }
    const unsigned hvo = (unsigned)(FH0 + rb2 * 32 + 12 + 4 * ws + (l & 3)) * 8;
    u32x2 fv;
    if (MODE != 2) ld_flag(fv, hvo, ctl);
    wait_vm<0>();
    if (MODE != 3 && rank < DF_) { if (lds_arrive(cnt + 2, l) == 3 && l == 0) st_flag(ctl + FY0 + (rank == 224 ? 0 : rank + 1), tag); }
    if (MODE == 2 || !g2cu) { STAMP_END(0) return; }
    pin(fv);
    poll_rest(fv, hvo, ctl, tlo, thi);
    ST(0, 10)
    sfor<4>([&](auto I) { ldh(I); });
    int ao[4];
#pragma unroll
    for (int i = 0; i < 4; ++i) ao[i] = w * 4096 + (4 * q + ta) * 128 + ((i ^ sw) * 32) + 8 * tp_;
    sfor<8>([&](auto I) { constexpr int i = I;
      wait_vm<(i < 4 ? 4 * (3 - i) + 8 * i : 8 * (7 - i))>();
      u32x4 fa[4];
#pragma unroll
      for (int t = 0; t < 4; ++t) fa[t] = tr_frag(smem + UBASE[i & 3] + ao[t], smem + UBASE[i & 3] + ao[t] + 2048);
#pragma unroll
      for (int t = 0; t < 4; ++t) { pin(hf[i][t]); pin(fa[t]); }
      if constexpr (i < 4) { gd(i + 4, 0); gd(i + 4, 1); gd(i + 4, 2); gd(i + 4, 3); ldh(std::integral_constant<int, i + 4>{}); }
#pragma unroll
      for (int t = 0; t < 4; ++t)
#pragma unroll
        for (int j = 0; j < 4; ++j) acc[t][j] = mfma16(fa[t], hf[i][j], i == 0 ? zero : acc[t][j]);
      SB();
    });
    ST(0, 11)
#pragma unroll
    for (int t = 0; t < 4; ++t)
#pragma unroll
      for (int j = 0; j < 4; ++j) *(f32x4*)(smem + UBASE[t] + w * 4096 + j * 1024 + q * 256 + ((v ^ ((2 * t + (q >> 1)) << 1)) * 16)) = acc[t][j];
  } else {
    if (MODE == 2 || !g2cu) { wait_vm<0>(); if constexpr (MODE != 2) pin(xres); publish_h2(); STAMP_END(0) return; }
    const unsigned hvo = (unsigned)(FH0 + rb2 * 32 + 3 * ws + ((l & 3) == 3 ? 0 : (l & 3))) * 8;
    u32x2 fv;
    ld_flag(fv, hvo, ctl);
    wait_vm<0>(); pin(fv);
    publish_h2();
    poll_rest(fv, hvo, ctl, tlo, thi);
    ST(0, 7)
    sfor<4>([&](auto I) { ldh(I); });
    int ao[4];
#pragma unroll
    for (int i = 0; i < 4; ++i) ao[i] = w * 4096 + (4 * q + ta) * 128 + (((i ^ ws) ^ sw) * 32) + 8 * tp_;
    u32x4 tf[14];
    const char* t2p = (const char*)&g_tab2 + (size_t)(4 * dt + ws) * 14336;
    sfor<6>([&](auto I) { constexpr int i = I;
      constexpr int yg[6] = {12, 16, 20, 23, 22, 14};
      wait_vm<yg[i]>();
      u32x4 fa[4];
#pragma unroll
      for (int t = 0; t < 4; ++t) fa[t] = tr_frag(smem + UBASE[i & 3] + ao[t], smem + UBASE[i & 3] + ao[t] + 2048);
#pragma unroll
      for (int t = 0; t < 4; ++t) { pin(hf[i][t]); pin(fa[t]); }
      if constexpr (i < 2) { gd(i + 4, 0); gd(i + 4, 1); gd(i + 4, 2); gd(i + 4, 3); ldh(std::integral_constant<int, i + 4>{}); }
      if constexpr (i == 2 || i == 3) sfor<7>([&](auto J) { constexpr int k = (i - 2) * 7 + J; ld_pl<(k & 3) * 1024>(tf[k], lvo, t2p + (k >> 2) * 4096); });
#pragma unroll
      for (int t = 0; t < 4; ++t)
#pragma unroll
        for (int j = 0; j < 4; ++j) acc[t][j] = mfma16(fa[t], hf[i][j], i == 0 ? zero : acc[t][j]);
      SB();
    });
    ST(0, 8)
    const unsigned yvo = (unsigned)(FY0 + 56 * ws + 1 + (l < 56 ? l : (ws == 0 ? -1 : 0))) * 8;
    ld_flag(fv, yvo, ctl);
#pragma unroll
    for (int t = 1; t < 4; ++t)
#pragma unroll
      for (int j = 0; j < 4; ++j) *(f32x4*)(smem + UBASE[t] + w * 4096 + j * 1024 + l * 16) = acc[t][j];
    lds_arrive(cnt + 3, l);
    lds_wait(cnt + 3, 4);
#pragma unroll
    for (int t = 1; t < 4; ++t)
#pragma unroll
      for (int j = 0; j < 4; ++j) acc[0][j] += *(const f32x4*)(smem + UBASE[t] + (w ^ t) * 4096 + j * 1024 + l * 16);
#pragma unroll
    for (int j = 0; j < 4; ++j) asm volatile("" : "+v"(acc[0][j]));
    lds_arrive(cnt + 4, l);
    wait_vm<0>(); pin(fv);
#pragma unroll
    for (int k = 0; k < 14; ++k) pin(tf[k]);
    ST(0, 9)
    poll_rest(fv, yvo, ctl, tlo, thi);
    ST(0, 10)
    u32x4 yf[14];
    {
      const unsigned yo = (unsigned)(r8 * S_ + rt * 64 + sw8 * 8) * 2;
      const char* yp = ws_ + OFF_Y + (size_t)(112 * ws) * (S_ * 2);
      sfor<14>([&](auto I) { ld_sc1<0>(yf[I], yo, yp + (size_t)I * (8 * S_ * 2)); });
    }
    lds_wait(cnt + 4, 4);
    sfor<14>([&](auto I) { constexpr int i = I;
      wait_vm<13 - i>(); pin(yf[i]);
      const int m = 14 * ws + i, ks = m >> 2;
      const int ub = (ks & 3) == 0 ? UBASE[0] : (ks & 3) == 1 ? UBASE[1] : (ks & 3) == 2 ? UBASE[2] : UBASE[3];
      *(u32x4*)(smem + ub + (4 + (ks >> 2)) * 4096 + (m & 3) * 1024 + l * 16) = yf[i];
    });
    lds_arrive(cnt + 5, l);
    lds_wait(cnt + 5, 4);
    ST(0, 11)
    int bo[4];
#pragma unroll
    for (int j = 0; j < 4; ++j) bo[j] = (4 * q + ta) * 128 + ((j ^ sw) * 32) + 8 * tp_;
#pragma unroll
    for (int ks = 0; ks < 14; ++ks) {
      const char* yb = smem + UBASE[ks & 3] + (4 + (ks >> 2)) * 4096;
#pragma unroll
      for (int j = 0; j < 4; ++j) acc[0][j] = mfma16(tf[ks], tr_frag(yb + bo[j], yb + bo[j] + 2048), acc[0][j]);
    }
#pragma unroll
    for (int j = 0; j < 4; ++j) *(f32x4*)(smem + 8 * 16384 + ws * 4096 + j * 1024 + q * 256 + ((v ^ ((2 * ws + (q >> 1)) << 1)) * 16)) = acc[0][j];
  }
  ST(0, 12)
  bar();
  ST(0, 13)
  {
    const int tl = ch >> 1;
    const int ro = (ep_ >> 4) * 1024 + (ch & 1) * 512 + (((ep_ & 15) ^ (ch << 1)) * 16);
    const int ub = tl == 0 ? UBASE[0] : tl == 1 ? UBASE[1] : tl == 2 ? UBASE[2] : UBASE[3];
    f32x4 a0 = *(const f32x4*)(smem + 8 * 16384 + tl * 4096 + ro), a1 = *(const f32x4*)(smem + 8 * 16384 + tl * 4096 + ro + 256);
#pragma unroll
    for (int s = 0; s < 4; ++s) { a0 += *(const f32x4*)(smem + ub + s * 4096 + ro); a1 += *(const f32x4*)(smem + ub + s * 4096 + ro + 256); }
    pin(xres);
    u32x4 o;
    o[0] = pk2(a0[0] + bflo(xres[0]), a0[1] + bfhi(xres[0]));
    o[1] = pk2(a0[2] + bflo(xres[1]), a0[3] + bfhi(xres[1]));
    o[2] = pk2(a1[0] + bflo(xres[2]), a1[1] + bfhi(xres[2]));
    o[3] = pk2(a1[2] + bflo(xres[3]), a1[3] + bfhi(xres[3]));
    *(u32x4*)((char*)out + ovo) = o;
  }
  if (b == 0 && tid == 0) st_flag(ctl, tag);
  STAMP_END(0)
}

#define SPLIT 0
extern "C" void kernel_launch(void* const* d_in, const int* in_sizes, int n_in, void* d_out, int out_size, void* d_ws, size_t ws_size,
                              hipStream_t stream) {
  const uint16_t* x = (const uint16_t*)d_in[0];
  const uint16_t* wr = (const uint16_t*)d_in[1];
  const uint16_t* wi = (const uint16_t*)d_in[2];
  const uint16_t* wg = (const uint16_t*)d_in[4];
  const uint16_t* wu = (const uint16_t*)d_in[5];
  const uint16_t* wd = (const uint16_t*)d_in[6];
  (void)hipFuncSetAttribute((const void*)fwd<0>, hipFuncAttributeMaxDynamicSharedMemorySize, 163840);
  fwd<0><<<256, 512, 163840, stream>>>(x, wr, wi, wg, wu, wd, (char*)d_ws, (uint16_t*)d_out STAMP_PASS);
}
```

```cpp
#include <hip/hip_runtime.h>
#include <hip/hip_bf16.h>
#include <stdint.h>
#include <utility>

typedef __attribute__((ext_vector_type(4))) short s16x4;
typedef __attribute__((ext_vector_type(8))) __bf16 bf16x8;
typedef __attribute__((ext_vector_type(2))) __bf16 bf16x2;
typedef __attribute__((ext_vector_type(4))) float f32x4;
typedef __attribute__((ext_vector_type(16))) float f32x16;
typedef __attribute__((ext_vector_type(2))) float f32x2;
typedef __attribute__((ext_vector_type(4))) unsigned u32x4;
typedef __attribute__((ext_vector_type(2))) unsigned u32x2;
typedef __attribute__((address_space(3))) s16x4* lds_s16x4_p;
typedef unsigned long long u64;
typedef __attribute__((address_space(1))) u64 gu64;
typedef __attribute__((address_space(3))) unsigned lu32;
#define DI __device__ __forceinline__
#define SB() __builtin_amdgcn_sched_barrier(0)
template <int N> using ic = std::integral_constant<int, N>;

constexpr int S_ = 2048, D_ = 448, F_ = 1792, DF_ = 225;

template <class Fn, int... I> DI void sfor_impl(Fn&& f, std::integer_sequence<int, I...>) { (f(ic<I>{}), ...); }
template <int N, class Fn> DI void sfor(Fn&& f) { sfor_impl(f, std::make_integer_sequence<int, N>{}); }

DI f32x4 mfma16(u32x4 a, u32x4 b, f32x4 c) {
  return __builtin_amdgcn_mfma_f32_16x16x32_bf16(__builtin_bit_cast(bf16x8, a), __builtin_bit_cast(bf16x8, b), c, 0, 0, 0);
}
DI f32x16 mfma32(u32x4 a, u32x4 b, f32x16 c) {
  return __builtin_amdgcn_mfma_f32_32x32x16_bf16(__builtin_bit_cast(bf16x8, a), __builtin_bit_cast(bf16x8, b), c, 0, 0, 0);
}
DI unsigned pk2(float lo, float hi) { bf16x2 p; p[0] = (__bf16)lo; p[1] = (__bf16)hi; return __builtin_bit_cast(unsigned, p); }
DI float bflo(unsigned u) { return __builtin_bit_cast(float, u << 16); }
DI float bfhi(unsigned u) { return __builtin_bit_cast(float, u & 0xffff0000u); }
DI float rbf(float f) { return (float)(__bf16)f; }
typedef __attribute__((address_space(3))) const char* lds_cp;
typedef __attribute__((address_space(3))) const u32x4* lds_u32x4_p;
template <int O0, int O1> DI u32x4 tr_at(unsigned a) {
  s16x4 x = __builtin_amdgcn_ds_read_tr16_b64_v4i16((lds_s16x4_p)((lds_cp)(uintptr_t)a + O0));
  s16x4 y = __builtin_amdgcn_ds_read_tr16_b64_v4i16((lds_s16x4_p)((lds_cp)(uintptr_t)a + O1));
  return __builtin_bit_cast(u32x4, __builtin_shufflevector(x, y, 0, 1, 2, 3, 4, 5, 6, 7));
}
template <int O> DI u32x4 l128_at(unsigned a) { return *(lds_u32x4_p)((lds_cp)(uintptr_t)a + O); }
DI unsigned lds_addr(const void* p) { return (unsigned)(uintptr_t)(__attribute__((address_space(3))) const char*)p; }
template <int N> DI void wait_vm() { asm volatile("s_waitcnt vmcnt(%0)" ::"n"(N) : "memory"); }
DI void bar() { asm volatile("s_waitcnt lgkmcnt(0)" ::: "memory"); __builtin_amdgcn_s_barrier(); asm volatile("" ::: "memory"); }
DI u32x4 lds128(const char* p) { return *(const u32x4*)p; }
DI int lane_id() { int r; asm volatile("v_mbcnt_lo_u32_b32 %0, -1, 0\n\tv_mbcnt_hi_u32_b32 %0, -1, %0" : "=v"(r)); return r; }
DI float vcos(float a) { return __builtin_amdgcn_cosf(a); }
DI float vsin(float a) { return __builtin_amdgcn_sinf(a); }

#define STINS "s_memrealtime"
#define STAMP_BEGIN
#define ST(i)
#define STAMP_END(kid)
#define STAMP_ARG
#define STAMP_PASS

constexpr unsigned ONES = 0x3f803f80u;
constexpr int SL = 16384;
constexpr int L_T1 = 8 * SL, L_GR = 9 * SL + 12288, L_P = 9 * SL, L_NM = 9 * SL + 8192;
constexpr unsigned OFF_Z = 8u << 20, OFF_W = 10u << 20, OFF_Y = 12u << 20, OFF_C = 14u << 20, OFF_S = 15u << 20;
constexpr int FZ0 = 512, FW0 = 1024, FY0 = 2048, FH0 = 4096;
constexpr int SPIN_CAP = 200000;
constexpr int RA = 6;

enum : int { eX = 0, eW = 8, eD = 24, eDR = 40, eH = 48, eEP = 80, ePF, eT1, ePRE, eA6, eCS, eCS2, eZ, eFWL, eWT, eZF, eWF, eCL, eCL2, eCP, eCP2, eFL1, eP1, eFL2, eP2, eRES,
             mX = 200, mS = 208, mP = 210, mK = 230, mEnd = 500 };
struct Ev { int id, n; };
#define PRO {eX + 0, 2}, {eX + 1, 2}, {eX + 2, 2}, {eEP, 1}, {ePF, 1}, {eT1, 0}, {eX + 3, 2}, {ePRE, 0}, \
  {mX + 0, 0}, {eX + 4, 2}, {mX + 1, 0}, {eX + 5, 2}, {mX + 2, 0}, {eX + 6, 2}, {mX + 3, 0}, {eW + 0, 2}, {mX + 4, 0}, {eW + 1, 2}, \
  {mX + 5, 0}, {eW + 2, 2}, {mX + 6, 0}, {eW + 3, 2}, \
  {mP + 0, 0}, {eA6, 0}, {eCS, 1}, {eW + 4, 4}, {mS, 0}, {eZ, 8}, \
  {mP + 1, 0}, {eFWL, 16}, {eD + 0, 4}, {mP + 2, 0}, {eW + 5, 4}, {eD + 1, 4}, {mP + 3, 0}, {eZF, 0}, {eW + 6, 4}, {eWT, 2}, {eD + 2, 4}
constexpr Ev SEQ_R[] = {PRO,
  {mP + 4, 0}, {eW + 7, 4}, {eCL, 4}, {eD + 3, 4}, {eDR + 0, 0},
  {mP + 5, 0}, {eWF, 0}, {eW + 8, 4}, {eD + 4, 4}, {eCP, 0},
  {mP + 6, 0}, {eW + 9, 4}, {eD + 5, 4}, {eDR + 1, 0},
  {mP + 7, 0}, {eCS2, 1}, {eW + 10, 4}, {eD + 6, 4},
  {mP + 8, 0}, {eW + 11, 4}, {eD + 7, 4}, {eDR + 2, 0},
  {mP + 9, 0}, {eW + 12, 4}, {eD + 8, 4},
  {mP + 10, 0}, {eW + 13, 4}, {eD + 9, 4}, {eDR + 3, 0},
  {mP + 11, 0}, {eCL2, 4}, {eD + 10, 4},
  {mP + 12, 0}, {eD + 11, 4}, {eCP2, 0}, {eFL1, 1},
  {mP + 13, 0}, {eP1, 0}, {eH + 0, 2}, {eH + 1, 2}, {eH + 2, 2}, {eH + 3, 2}, {eH + 4, 2},
  {mK + 0, 0}, {eH + 5, 2}, {eH + 6, 2}, {mK + 1, 0}, {eH + 7, 2}, {eH + 8, 2}, {mK + 2, 0}, {eH + 9, 2}, {eH + 10, 2}, {mK + 3, 0}, {eH + 11, 2}, {eH + 12, 2},
  {mK + 4, 0}, {eH + 13, 2}, {eH + 14, 2}, {mK + 5, 0}, {eH + 15, 2}, {mK + 6, 0}, {mK + 7, 0}, {eDR + 4, 0},
  {mK + 8, 0}, {eD + 12, 4}, {mK + 9, 0}, {eD + 13, 4}, {mK + 10, 0}, {eRES, 2}, {mK + 11, 0}, {eDR + 5, 0},
  {mK + 12, 0}, {mK + 13, 0}, {eFL2, 1}, {mK + 14, 0}, {mK + 15, 0}, {eP2, 0},
  {eH + 16, 2}, {eH + 17, 2}, {eH + 18, 2}, {eH + 19, 2}, {eH + 20, 2}, {eH + 21, 2}, {eH + 22, 2}, {eH + 23, 2}, {eH + 24, 2}, {eH + 25, 2}, {eH + 26, 2}, {eH + 27, 2},
  {mK + 16, 0}, {mK + 17, 0}, {mK + 18, 0}, {mK + 19, 0}, {eDR + 6, 0}, {mK + 20, 0}, {mK + 21, 0}, {mK + 22, 0}, {mK + 23, 0},
  {mK + 24, 0}, {mK + 25, 0}, {mK + 26, 0}, {mK + 27, 0}, {mEnd, 0}};
constexpr Ev SEQ_B[] = {PRO,
  {mP + 4, 0}, {eCL, 4}, {eD + 3, 4}, {mP + 5, 0}, {eWF, 0}, {eCP, 0}, {mP + 6, 0},
  {mP + 13, 0}, {eFL1, 1}, {eDR + 0, 0}, {eDR + 1, 0}, {eD + 4, 4}, {eD + 5, 4}, {eP1, 0},
  {eH + 0, 1}, {eH + 1, 1}, {eH + 2, 1}, {eH + 3, 1}, {eH + 4, 1}, {eH + 5, 1}, {eH + 6, 1}, {eH + 7, 1}, {eD + 6, 4}, {eD + 7, 4},
  {eH + 8, 1}, {eH + 9, 1}, {eH + 10, 1}, {eH + 11, 1}, {eH + 12, 1}, {eH + 13, 1}, {eH + 14, 1}, {eH + 15, 1}, {eDR + 2, 0}, {eD + 8, 4}, {eD + 9, 4},
  {mK + 0, 0}, {mK + 1, 0}, {mK + 2, 0}, {mK + 3, 0}, {mK + 4, 0}, {mK + 5, 0}, {mK + 6, 0}, {mK + 7, 0}, {eDR + 3, 0}, {eD + 10, 4}, {eD + 11, 4}, {eFL2, 1},
  {mK + 8, 0}, {mK + 9, 0}, {mK + 10, 0}, {mK + 11, 0}, {mK + 12, 0}, {mK + 13, 0}, {mK + 14, 0}, {mK + 15, 0}, {eDR + 4, 0}, {eD + 12, 4}, {eD + 13, 4}, {eP2, 0},
  {eH + 16, 1}, {eH + 17, 1}, {eH + 18, 1}, {eH + 19, 1}, {eH + 20, 1}, {eH + 21, 1}, {eH + 22, 1}, {eH + 23, 1}, {eH + 24, 1}, {eH + 25, 1}, {eH + 26, 1}, {eH + 27, 1},
  {eDR + 5, 0}, {eDR + 6, 0},
  {mK + 16, 0}, {mK + 17, 0}, {mK + 18, 0}, {mK + 19, 0}, {mK + 20, 0}, {mK + 21, 0}, {mK + 22, 0}, {mK + 23, 0},
  {mK + 24, 0}, {mK + 25, 0}, {mK + 26, 0}, {mK + 27, 0}, {mEnd, 0}};
template <int R> constexpr Ev seq(int i) { return R ? SEQ_B[i] : SEQ_R[i]; }
template <int R> constexpr int sidx(int id) { int i = 0; while (seq<R>(i).id != id && seq<R>(i).id != mEnd) ++i; return i; }
template <int R> constexpr bool has(int id) { return seq<R>(sidx<R>(id)).id == id; }
template <int R> constexpr int pos(int id) { int p = has<R>(id) ? 0 : 1000; for (int i = 0; i <= sidx<R>(id); ++i) p += seq<R>(i).n; return p; }
template <int R> constexpr int before(int id) { return pos<R>(id) - seq<R>(sidx<R>(id)).n; }
template <int R> constexpr int seglen(int i0) { int n = 0; while (seq<R>(i0 + n).id < mX) ++n; return n; }
template <int R> constexpr bool inper(int id, int n) { return sidx<R>(id) > sidx<R>(mP + n) && sidx<R>(id) < sidx<R>(mP + n + 1); }
constexpr int cmax(int a, int b) { return a > b ? a : b; }
template <int R> constexpr int need(int n) {
  int p = pos<R>(eW + n);
  if (inper<R>(eZF, n)) p = cmax(p, pos<R>(eZ));
  if (inper<R>(eWF, n)) p = cmax(p, pos<R>(eWT));
  return p;
}
constexpr int t_start(int n) { return 116 * (n / 7) + 16 * (n % 7); }
constexpr int t_per(int T) { return 7 * (T / 116) + (T % 116 >= 96 ? 6 : T % 116 / 16); }
constexpr int t_idx(int T) { const int u = T % 116; return u < 96 ? u % 16 : u < 100 ? u - 80 : u - 100; }

typedef __amdgpu_buffer_rsrc_t srd_t;
DI srd_t mk_srd(const void* p, int bytes) { return __builtin_amdgcn_make_buffer_rsrc((void*)p, 0, bytes, 0x00020000); }
template <int L> DI void dma2(unsigned lw, unsigned v0, srd_t r0, unsigned v1, srd_t r1, unsigned so) {
  asm volatile("s_add_u32 m0, %0, %1\n\ts_nop 0\n\tbuffer_load_dwordx4 %2, %3, %6 offen lds\n\tbuffer_load_dwordx4 %4, %5, %6 offen offset:1024 lds"
               ::"s"(lw), "n"(L), "v"(v0), "s"(r0), "v"(v1), "s"(r1), "s"(so) : "memory", "scc");
}
template <int L> DI void dma4(unsigned lw, unsigned v0, unsigned v1, unsigned v2, unsigned v3, srd_t r0, srd_t r1, unsigned so) {
  asm volatile("s_add_u32 m0, %0, %1\n\ts_nop 0\n\tbuffer_load_dwordx4 %2, %6, %8 offen lds\n\tbuffer_load_dwordx4 %3, %7, %8 offen offset:1024 lds\n\t"
               "buffer_load_dwordx4 %4, %6, %8 offen offset:2048 lds\n\tbuffer_load_dwordx4 %5, %7, %8 offen offset:3072 lds"
               ::"s"(lw), "n"(L), "v"(v0), "v"(v1), "v"(v2), "v"(v3), "s"(r0), "s"(r1), "s"(so) : "memory", "scc");
}
template <int L> DI void dma4_sc1(unsigned lw, unsigned v, srd_t r, unsigned so) {
  asm volatile("s_add_u32 m0, %0, %1\n\ts_nop 0\n\tbuffer_load_dwordx4 %2, %3, %4 offen sc1 lds\n\tbuffer_load_dwordx4 %2, %3, %4 offen offset:1024 sc1 lds\n\t"
               "buffer_load_dwordx4 %2, %3, %4 offen offset:2048 sc1 lds\n\tbuffer_load_dwordx4 %2, %3, %4 offen offset:3072 sc1 lds"
               ::"s"(lw), "n"(L), "v"(v), "s"(r), "s"(so) : "memory", "scc");
}
template <int OFF> DI void ld_sc1(u32x4& d, unsigned voff, srd_t r, unsigned so) { asm volatile("buffer_load_dwordx4 %0, %1, %2, %3 offen offset:%4 sc1" : "=v"(d) : "v"(voff), "s"(r), "s"(so), "n"(OFF) : "memory"); }
template <int OFF> DI void ld_sc1_over(u32x4& d, unsigned voff, srd_t r, unsigned so) { asm volatile("buffer_load_dwordx4 %0, %1, %2, %3 offen offset:%4 sc1" : "+v"(d) : "v"(voff), "s"(r), "s"(so), "n"(OFF) : "memory"); }
DI void ld_pl(u32x4& d, unsigned voff, const void* sb) { asm volatile("global_load_dwordx4 %0, %1, %2" : "=v"(d) : "v"(voff), "s"(sb) : "memory"); }
template <int OFF> DI void ld_u16(unsigned& d, unsigned voff, srd_t r, unsigned so) { asm volatile("buffer_load_ushort %0, %1, %2, %3 offen offset:%4" : "=v"(d) : "v"(voff), "s"(r), "s"(so), "n"(OFF) : "memory"); }
DI void ld_u32(unsigned& d, unsigned voff, const void* sb) { asm volatile("global_load_dword %0, %1, %2" : "=v"(d) : "v"(voff), "s"(sb) : "memory"); }
DI void ld_u32_sc1(unsigned& d, unsigned voff, srd_t r, unsigned so) { asm volatile("buffer_load_dword %0, %1, %2, %3 offen sc1" : "=v"(d) : "v"(voff), "s"(r), "s"(so) : "memory"); }
DI void ld_flag(u64& d, unsigned voff, const void* sb) { asm volatile("global_load_dwordx2 %0, %1, %2 sc1" : "=v"(d) : "v"(voff), "s"(sb) : "memory"); }
template <int OFF = 0> DI void st16_sc1(u32x4 v, unsigned voff, srd_t r, unsigned so) {
  asm volatile("buffer_store_dwordx4 %0, %1, %2, %3 offen offset:%4 sc1\n\ts_nop 1" ::"v"(v), "v"(voff), "s"(r), "s"(so), "n"(OFF) : "memory");
}
DI void st4_sc1(unsigned v, unsigned voff, srd_t r, unsigned so) { asm volatile("buffer_store_dword %0, %1, %2, %3 offen sc1" ::"v"(v), "v"(voff), "s"(r), "s"(so) : "memory"); }
DI void st_flag1(srd_t r, unsigned so, u64 tag) {
  asm volatile("s_mov_b64 exec, 1\n\tbuffer_store_dwordx2 %0, off, %1, %2 sc1\n\ts_mov_b64 exec, -1" ::"v"(tag), "s"(r), "s"(so) : "memory");
}
template <class T> DI void pin(T& a) { asm volatile("" : "+v"(a)); }
template <class T> DI void pins(T& a) { asm volatile("" : "+s"(a)); }
template <class T> DI void opq(T& a) { asm("" : "+v"(a)); }
DI unsigned selm_n(u64 m, unsigned a, unsigned b) { unsigned r; asm("v_cndmask_b32_e64 %0, %2, %1, %3\n\ts_nop 1" : "=v"(r) : "v"(a), "v"(b), "s"(m)); return r; }
DI unsigned selm(u64 m, unsigned a, unsigned b) { unsigned r; asm("v_cndmask_b32_e64 %0, %2, %1, %3" : "=v"(r) : "v"(a), "v"(b), "s"(m)); return r; }
template <int N> DI void vbar() {
  static_assert(N >= 0, "wait");
  asm volatile("s_waitcnt vmcnt(%0) lgkmcnt(0)\n\ts_barrier" ::"n"(N > 63 ? 63 : N) : "memory");
}
template <int N> DI void wvm() { static_assert(N >= 0, "wait"); wait_vm<(N > 63 ? 63 : N)>(); }
DI void poll(u64& fv, u64 mask, unsigned voff, const void* sb, u64 tag) {
  if ((~__builtin_amdgcn_ballot_w64(fv == tag) & mask) != 0) {
#pragma unroll 1
    for (int it = 0; it < SPIN_CAP; ++it) {
      __builtin_amdgcn_s_sleep(1);
      ld_flag(fv, voff, sb); wait_vm<0>(); pin(fv);
      if ((~__builtin_amdgcn_ballot_w64(fv == tag) & mask) == 0) break;
    }
  }
  asm volatile("" ::: "memory");
}
typedef __attribute__((address_space(3))) char* lds_p;
template <class T, int O> DI T lds_ld(unsigned a) { return *(__attribute__((address_space(3))) const T*)((lds_cp)(uintptr_t)a + O); }
template <class T, int O> DI void lds_st(unsigned a, T v) { *(__attribute__((address_space(3))) T*)((lds_p)(uintptr_t)a + O) = v; }
DI void gram_dump(unsigned a, f32x16 g) {
  asm volatile("s_mov_b32 exec_lo, 0xff\n\ts_mov_b32 exec_hi, 0xff\n\tds_write_b128 %0, %1\n\ts_lshl_b64 exec, exec, 8\n\tds_write_b128 %0, %2\n\t"
               "s_lshl_b64 exec, exec, 8\n\tds_write_b128 %0, %3\n\ts_lshl_b64 exec, exec, 8\n\tds_write_b128 %0, %4\n\ts_mov_b64 exec, -1"
               ::"v"(a), "v"(__builtin_shufflevector(g, g, 0, 1, 2, 3)), "v"(__builtin_shufflevector(g, g, 4, 5, 6, 7)),
                 "v"(__builtin_shufflevector(g, g, 8, 9, 10, 11)), "v"(__builtin_shufflevector(g, g, 12, 13, 14, 15)) : "memory", "scc");
}
DI void put_part(unsigned a, int l, const f32x16& c0, const f32x16& c1) {
  sfor<8>([&](auto K) { constexpr int k = K; const f32x16& c = k < 4 ? c0 : c1;
    lds_st<f32x4, k * 1024>(a + (l >> 5) * 512 + (((l & 31) ^ (2 * k)) * 16), f32x4{c[4 * (k & 3)], c[4 * (k & 3) + 1], c[4 * (k & 3) + 2], c[4 * (k & 3) + 3]});
  });
}

__global__ __launch_bounds__(512, 2) void fwd(const uint16_t* __restrict__ x, const uint16_t* __restrict__ wr, const uint16_t* __restrict__ wi,
                                              const uint16_t* __restrict__ wg, const uint16_t* __restrict__ wu, const uint16_t* __restrict__ wd,
                                              char* __restrict__ ws_, uint16_t* __restrict__ out STAMP_ARG) {
  extern __shared__ __attribute__((aligned(16))) char smem[];
  STAMP_BEGIN
  const int w = __builtin_amdgcn_readfirstlane(threadIdx.x >> 6), ws = w & 3, wn = w >> 2;
  const unsigned b = blockIdx.x;
  const int xcd = b & 7, idx = b >> 3;
  const unsigned lds0 = lds_addr(smem), lw = lds0 + w * 2048;
  const int rb = (xcd & 1) * 8 + (idx & 7), g = xcd >> 1, ii = idx >> 3;
  const bool bcu = ii == 3;
  const int unit1 = 4 * g + ii, unit2 = 16 + 3 * g + ii;
  const int r0 = (unsigned)(rb + 2 * ii + g) % 7;
  auto kof = [&](int st) { int k = st + r0; return k >= 7 ? k - 7 : k; };
  constexpr unsigned CH = 64 * F_ * 2;
  unsigned xvo[2];
  const srd_t rx = mk_srd(x, S_ * D_ * 2), rg = mk_srd(wg, D_ * F_ * 2), ru = mk_srd((const char*)wu - 1024, D_ * F_ * 2 + 1024);
  const srd_t rd = mk_srd(wd, D_ * F_ * 2), rw = mk_srd(ws_, 16 << 20);
  const int rt = 4 * xcd + (idx & 3), dt = bcu ? 6 : (idx >> 2);
  const int rot = (idx & 3) + (idx >> 2), rot3 = (unsigned)rot % 3;
  auto rnd = [&](int i) { if (i < 4) return (i + rot) & 3; const int k = i - 4 + rot3; return 4 + (k >= 3 ? k - 3 : k); };
  const int rtb = 2 * rt + (bcu ? (idx >> 2) & 1 : 0);
  const int gi = (idx & 7) * 8 + xcd;
  const char* ctl = ws_ + OFF_C;
  const f32x4 zero = {0.f, 0.f, 0.f, 0.f};
  const f32x16 zero16 = {};

  u64 ep, fv, tag, pc;
  asm volatile("s_getpc_b64 %0\n.Lpf_here:" : "=s"(pc));
  unsigned pf, lvo, wvo, wo, xo[4], gvo, ovo, r2u;
  u32x4 xres;
  unsigned wv1, dv[4], ao, fl[16];
  u32x4 xa[2][4], ta[2][4], cg[4], wdf[7][8], hf[28][2];
  f32x16 accA, ah[2][2];
  auto res = [&](int R, int u) {
    const int l = lane_id(), pp = 32 * (rtb & 3) + 8 * (R ? ws : w) + (l >> 3);
    ld_u32_sc1(r2u, (unsigned)((pp >> 6) * 4096 + (pp & 63) * 4), rw, OFF_Z + (16 * u + 15) * 8192 + (rt >> 1) * 256);
    ovo = (unsigned)((128 * (rt >> 1) + 32 * (pp & 3) + (pp >> 2)) * D_ + dt * 64 + (l & 7) * 8) * 2;
    ld_pl(xres, ovo, x);
  };
  auto mk_dv = [&] { const int l = lane_id();
    dv[0] = (unsigned)((64 * ws + (l >> 3)) * D_ * 2 + dt * 128 + (((l & 7) ^ ((l >> 2) & 4)) * 16));
#pragma unroll
    for (int i = 1; i < 4; ++i) { dv[i] = dv[0] + i * (8 * D_ * 2 - 1024); pin(dv[i]); }
    ao = lds0 + (5 + ws) * SL + (4 * (l >> 5) + ((l & 15) >> 2)) * 128 + ((l >> 3) & 1) * 64 + ((l >> 4) & 1) * 32 + (l & 3) * 8;
    pin(ao);
  };
  auto ev = [&](auto Rc, auto Ic) { constexpr int R = Rc, id = Ic;
    if constexpr (id >= eX && id < eX + 7) { constexpr int j = id - eX;
      if constexpr (j == 0) {
        const int l = lane_id();
#pragma unroll
        for (int a = 0; a < 2; ++a) {
          const int p = 16 * w + 8 * a + (l >> 3);
          xvo[a] = (unsigned)((128 * rb + 32 * (p & 3) + (p >> 2)) * D_ + (((l & 7) ^ (4 * a + (l >> 4))) * 8)) * 2 - 1024 * a;
        }
      }
      dma2<(j + 1) * SL>(lw, xvo[0], rx, xvo[1], rx, kof(j) * 128);
    } else if constexpr (id >= eW && id < eW + 14) { constexpr int j = id - eW;
      if constexpr (j < 4) dma2<j * SL>(lw, wvo, rg, wvo, ru, kof(j) * CH);
      else dma4<(j % 5) * SL>(lds0 + ws * 4096, wvo, wvo, wv1, wv1, rg, ru, kof(j % 7) * CH + (j >= 7 ? (unit2 - unit1) * 128 : 0));
    } else if constexpr (id >= eD && id < eD + 14) { constexpr int i = id - eD;
      if constexpr (i >= 4 && !(i & 1)) asm volatile("s_waitcnt lgkmcnt(0)" ::: "memory");
      dma4<5 * SL + (i & 3) * 4096>(lds0 + ws * SL, dv[0], dv[1], dv[2], dv[3], rd, rd, rnd(i >> 1) * CH + (i & 1) * (32 * D_ * 2));
    } else if constexpr (id >= eDR && id < eDR + 7) { constexpr int i = id - eDR;
      wvm<pos<R>(id) - pos<R>(eD + 2 * i + 1)>();
      unsigned a1 = ao ^ 64; pin(a1);
      sfor<8>([&](auto K) { constexpr int o = (i & 1) * 8192 + (K >> 1) * 2048; wdf[i][K] = tr_at<o, o + 1024>((K & 1) ? a1 : ao); });
    } else if constexpr (id >= eH && id < eH + 28) { constexpr int s = id - eH;
      const unsigned so = (rtb * 28 + ws) * 4096 + rnd(s >> 2) * 16384;
      if constexpr (R) ld_sc1<(s & 3) * 1024>(hf[s][0], lvo, rw, so);
      else sfor<2>([&](auto C) { constexpr int c = C, k = (s - 5) >> 1;
        if constexpr (s < 5) ld_sc1<(s & 3) * 1024>(hf[s][c], lvo, rw, so + c * 28 * 4096);
        else {
          if constexpr (s >= 26) hf[s][c] = wdf[3][2 * (s - 26) + c];
          else if constexpr (s >= 16) hf[s][c] = hf[s - 10][c];
          else if constexpr (s & 1) hf[s][c] = hf[k][c];
          else hf[s][c] = wdf[k >> 2][2 * (k & 3) + c];
          ld_sc1_over<(s & 3) * 1024>(hf[s][c], lvo, rw, so + c * 28 * 4096);
        }
      });
    } else if constexpr (id == eEP) {
      ld_flag(ep, 0u, ctl);
    } else if constexpr (id == ePF) {
      unsigned len;
      asm volatile("s_mov_b32 %0, .Lpf_end-.Lpf_here" : "=s"(len));
      const unsigned o = (unsigned)((idx * 8 + w) * 4 + (lane_id() & 3)) * 64;
      ld_u32(pf, o < len ? o : 0u, (const void*)pc);
    } else if constexpr (id == eT1) {
      const int l = lane_id(), hh = l >> 5, fi = (l & 31) >> 1, f = fi == 15 ? 0 : 15 * unit1 + fi;
      const float cf_ = (float)f * (1.0f / 448);
      const unsigned sg = (f & 1) ? 0x80008000u : 0u;
#pragma unroll 1
      for (int s = w; s < 14; s += 8) {
        const float a0 = (float)(16 * s + 8 * hh) * cf_ + ((l & 1) ? 0.25f : 0.f);
        u32x4 o;
#pragma unroll
        for (int e = 0; e < 4; ++e) o[e] = pk2(vcos(a0 + (float)(2 * e) * cf_), vcos(a0 + (float)(2 * e + 1) * cf_));
        *(u32x4*)(smem + L_T1 + s * 1024 + l * 16) = o;
        *(u32x4*)(smem + L_T1 + 14336 + s * 1024 + l * 16) = o ^ sg;
      }
    } else if constexpr (id == ePRE) {
      const int l = lane_id(), r8 = l >> 3, ch = l & 7, r32 = l & 31, hh = l >> 5;
      lvo = (unsigned)l * 16;
      wvo = (unsigned)((8 * w + r8) * F_ * 2 + (ch ^ ((r8 & 2) << 1)) * 16 + unit1 * 128);
      wo = lds0 + hh * 2048 + ((l & 15) >> 2) * 128 + (r8 & 1) * 64 + ((l >> 4) & 1) * 32 + (l & 3) * 8;
#pragma unroll
      for (int i = 0; i < 4; ++i) { xo[i] = lds0 + SL + (32 * ws + r32) * 128 + (((2 * i + hh) ^ ((r32 >> 1) & 7)) * 16); pin(xo[i]); }
      pin(lvo); pin(wvo); pin(wo);
    } else if constexpr (id == eA6) {
      sfor<4>([&](auto I) { accA = mfma32(ta[0][I], xa[0][I], accA); });
      wvo += (unsigned)((8 * ws - 32) * F_ * 2); wv1 = wvo + (8 * F_ * 2 - 2048); pin(wvo); pin(wv1);
    } else if constexpr (id == eCS || id == eCS2) {
      constexpr int P = id == eCS2;
      const int l = lane_id();
      const unsigned c = (wo ^ ((ws >> 1) * 64)) + ((ws & 1) * 1024 + P * 2 * SL), sel = (((l >> 2) ^ (l >> 4)) & 1) ? 0u : ONES;
      const u32x4 sl = {sel, sel, sel, sel};
      f32x4 c0 = mfma16(sl, tr_at<0, 512>(c), zero), c1 = mfma16(sl, tr_at<4096, 4608>(c), zero);
      c0 = mfma16(sl, tr_at<8192, 8704>(c), c0); c1 = mfma16(sl, tr_at<12288, 12800>(c), c1);
      st16_sc1(u32x4{__float_as_uint(c0[0] + c1[0]), 0u, (unsigned)tag, (unsigned)(tag >> 32)}, lvo & 511, rw,
               OFF_S + ((P ? unit2 : unit1) * 4 + ws) * 4096 + r0 * 512);
    } else if constexpr (id == eZ) {
      const int l = lane_id(), r32 = l & 31;
      const float sq = lds_ld<float, L_GR - 8 * SL>(lds0 + 8 * SL + ws * 1024 + (r32 + 8 * (r32 & 4)) * 16 + (l & 3) * 4);
      const unsigned su = __float_as_uint(accA[14]);
      const float sm = __uint_as_float(__builtin_amdgcn_permlane32_swap(su, su, false, false)[1]);
      const float mean = sm * (1.0f / D_);
      const unsigned nm = pk2(-mean, -mean);
      lds_st<unsigned, L_NM - 8 * SL>(lds0 + 8 * SL + ws * 256 + l * 4, nm);
      const float rs = __builtin_amdgcn_rcpf(rbf(__builtin_amdgcn_sqrtf(rbf(rbf(fmaxf(sq - sm * mean, 0.f) * (1.0f / D_)) + 1e-5f))));
      accA[0] += __uint_as_float(selm(unit1 == 0 ? 0xffffffffull : 0ull, nm << 16, 0u)) * 448.f;
      const unsigned zv = (unsigned)((l >> 5) * 16384 + r32 * 4), zs = OFF_Z + unit1 * 131072 + (ws >> 1) * 4096 + rb * 256 + (ws & 1) * 128;
      sfor<8>([&](auto E) { constexpr int e = E;
        float p = accA[2 * e] * rs, q = accA[2 * e + 1] * rs; opq(p); opq(q);
        unsigned t = pk2(p, q);
        if constexpr (e == 7) t = selm(0xffffffffull, t, __float_as_uint(rs * rs));
        st4_sc1(t, zv, rw, zs + (4 * (e >> 1) + (e & 1)) * 8192);
      });
    } else if constexpr (id == eFWL) {
      const int l = lane_id();
      const srd_t ra = mk_srd(wr, S_ * DF_ * 2), rc = mk_srd(wi, S_ * DF_ * 2);
      const int fc = 32 * (2 * g + (ii >> 1)) + (l >> 2);
      const unsigned fo[2] = {(unsigned)((l & 3) * DF_ + min(fc, DF_ - 1)) * 2, (unsigned)((l & 3) * DF_ + min(fc + 16, DF_ - 1)) * 2};
      const unsigned fs = (unsigned)(16 * (rb >> 2) + 4 * ws + 64 * (16 * (ii & 1) + 4 * (rb & 3))) * DF_ * 2;
      sfor<8>([&](auto I) { constexpr int e = I >> 2, r = I & 3;
        ld_u16<0>(fl[2 * I], fo[e], ra, fs + r * 64 * DF_ * 2); ld_u16<0>(fl[2 * I + 1], fo[e], rc, fs + r * 64 * DF_ * 2); });
      mk_dv();
    } else if constexpr (id == eWT) {
      wvm<before<R>(eWT) - pos<R>(eFWL)>();
      const int l = lane_id();
      const unsigned tv = (unsigned)((l >> 2) * 8192 + (l & 3) * 16);
      const unsigned ts = OFF_W + (2 * g + (ii >> 1)) * 262144 + ((rb >> 2) * 128 + (ii & 1) * 64 + 16 * (rb & 3) + 4 * ws) * 16;
#pragma unroll
      for (int e = 0; e < 2; ++e) {
        u32x4 o;
#pragma unroll
        for (int r = 0; r < 4; ++r) { pin(fl[8 * e + 2 * r]); pin(fl[8 * e + 2 * r + 1]); o[r] = fl[8 * e + 2 * r] | (fl[8 * e + 2 * r + 1] << 16); }
        st16_sc1(o, tv, rw, ts + e * 131072);
      }
    } else if constexpr (id == eZF) {
      if (w == 4) st_flag1(rw, OFF_C + (FZ0 + unit1 * 16 + rb) * 8, tag);
    } else if constexpr (id == eWF) {
      if (w == 5) st_flag1(rw, OFF_C + (FW0 + (2 * g + (ii >> 1)) * 32 + rb * 2 + (ii & 1)) * 8, tag);
    } else if constexpr (id == eCL || id == eCL2 || id == eCP || id == eCP2) { constexpr int P = id == eCL2 || id == eCP2;
      const unsigned cv = (lvo & 511) + (lvo & 512) * 3, so = OFF_S + ((P ? unit2 : unit1) * 4 + ws) * 4096;
      auto ldc = [&] { sfor<4>([&](auto I) { ld_sc1<I * 512>(cg[I], cv, rw, so); }); };
      if constexpr (id == eCL || id == eCL2) ldc();
      else {
        wvm<pos<R>(id) - pos<R>(P ? eCL2 : eCL)>();
        auto bad = [&] { unsigned d = 0; sfor<4>([&](auto I) { pin(cg[I]); d |= (cg[I][2] ^ (unsigned)tag) | (cg[I][3] ^ (unsigned)(tag >> 32)); });
          return __builtin_amdgcn_ballot_w64(d != 0) != 0; };
        if (bad()) {
#pragma unroll 1
          for (int it = 0; it < SPIN_CAP; ++it) { __builtin_amdgcn_s_sleep(1); ldc(); wait_vm<0>(); if (!bad()) break; }
        }
        const float s = __uint_as_float(selm(0xffffffffull, cg[0][0], 0u)) + __uint_as_float(cg[1][0]) + (__uint_as_float(cg[2][0]) + __uint_as_float(cg[3][0]));
        const float q1 = s - rbf(s), q2 = q1 - rbf(q1);
        lds_st<u32x4, L_P - 8 * SL + P * 4096>(lds0 + 8 * SL + ws * 1024 + lvo, u32x4{pk2(s, q1), pk2(q2, 0.f), 0u, 0u});
      }
    } else if constexpr (id == eFL1) {
      const int l = lane_id(), i = l < 14 ? l >> 1 : 0;
      gvo = (unsigned)(FH0 + (rtb + (R ? 0 : l & 1)) * 28 + 4 * i + ws) * 8;
      ld_flag(fv, gvo, ctl);
    } else if constexpr (id == eFL2) { ld_flag(fv, gvo, ctl);
    } else if constexpr (id == eP1) { wvm<pos<R>(eP1) - pos<R>(eFL1)>(); pin(fv); ST(5) poll(fv, 0xffull, gvo, ctl, tag); ST(6)
    } else if constexpr (id == eP2) { wvm<pos<R>(eP2) - pos<R>(eFL2)>(); pin(fv); ST(8) poll(fv, 0x3f00ull, gvo, ctl, tag); ST(9)
    } else if constexpr (id == eRES) { res(0, ws);
    }
  };
  auto run = [&](auto Rc, auto Mc) { constexpr int R = Rc, mk = Mc, i0 = mk < 0 ? 0 : sidx<R>(mk) + 1;
    sfor<seglen<R>(i0)>([&](auto K) { ev(Rc, ic<seq<R>(i0 + K).id>{}); SB(); });
  };
  auto mk_tag = [&] { pin(ep); pin(pf);
    tag = (((u64)__builtin_amdgcn_readfirstlane((unsigned)(ep >> 32)) << 32) | (unsigned)__builtin_amdgcn_readfirstlane((unsigned)ep)) + 1; };
  auto tail_h = [&](auto Rc) { constexpr int R = Rc;
    run(Rc, ic<mP + 13>{});
    sfor<28>([&](auto Sc) { constexpr int s = Sc;
      wvm<pos<R>(mK + s) - pos<R>(eH + s)>();
      if constexpr (s == 0) { ST(7) }
      if constexpr (s == 16) { ST(10) }
      sfor<R ? 2 : 4>([&](auto I) { constexpr int a = I & 1, c = I >> 1;
        if constexpr (!a) pin(hf[s][c]);
        ah[c][a] = mfma32(wdf[s >> 2][2 * (s & 3) + a], hf[s][c], s == 0 ? zero16 : ah[c][a]); pin(ah[c][a]); });
      SB();
      run(Rc, ic<mK + s>{});
    });
    ST(11)
    const int l = lane_id();
    put_part(lds0 + (5 + ws) * SL, l, ah[0][0], ah[0][1]);
    if constexpr (!R) { put_part(lds0 + (5 + ws) * SL + 8192, l, ah[1][0], ah[1][1]); pin(xres); pin(r2u); }
  };
  auto tail_c = [&](auto Rc, auto&& mid) { constexpr int R = Rc, NK = R ? 7 : 14;
    const int l = lane_id(), kq = R ? ws : ws >> 1;
    u32x4 t2[NK][2], yf[NK];
    const float c0 = (float)((2 * kq + (l >> 5)) * (4 * NK));
    float dd[2], a0[2];
#pragma unroll
    for (int t = 0; t < 2; ++t) { dd[t] = (float)(64 * dt + 32 * t + (l & 31)) * (1.0f / 448); a0[t] = __builtin_amdgcn_fractf(c0 * dd[t]); }
    sfor<NK>([&](auto Kc) { constexpr int k = Kc;
#pragma unroll
      for (int t = 0; t < 2; ++t) {
#pragma unroll
        for (int e = 0; e < 4; ++e) { const float a = (float)(4 * k + e) * dd[t] + a0[t]; t2[k][t][e] = pk2(vcos(a), vsin(a)); }
        if constexpr (k == 0) t2[0][t][0] = selm(kq == 0 ? 0xffffffffull : 0ull, (l & 1) ? 0xbf003f00u : 0x3f003f00u, t2[0][t][0]);
        pin(t2[k][t]);
      }
      SB();
      if constexpr (k == 3) { mid(); SB(); }
      if constexpr (k == NK - 3) { gvo = (unsigned)(FY0 + 16 * (l < 2 * NK ? 2 * NK * kq + l : 56)) * 8; ld_flag(fv, gvo, ctl); SB(); }
    });
    wait_vm<0>(); pin(fv);
    ST(8)
    poll(fv, ~0ull, gvo, ctl, tag);
    ST(9)
    const unsigned yo = OFF_Y + (rtb + (R ? 0 : ws & 1)) * 512, yv = (unsigned)((l >> 5) * (NK * 32768) + (l & 31) * 16);
    unsigned yn;
    ld_u32_sc1(yn, yv & 511, rw, yo + 56 * 32768);
    sfor<NK>([&](auto I) { ld_sc1<0>(yf[I], yv, rw, yo + (2 * NK * kq + I) * 32768); });
    res(R, 14);
    f32x16 ac[2];
    sfor<NK>([&](auto I) { constexpr int i = I;
      wait_vm<NK + 1 - i>(); pin(yf[i]);
      if constexpr (i == 0) { ST(10) pin(yn); yf[0][0] = selm_n(kq == 0 ? 0xffffffffull : 0ull, (yf[0][0] & 0xffffu) | (yn << 16), yf[0][0]); }
      ac[0] = mfma32(t2[i][0], yf[i], i ? ac[0] : zero16); ac[1] = mfma32(t2[i][1], yf[i], i ? ac[1] : zero16);
      SB();
    });
    ST(11)
    put_part(lds0 + (R ? 3 * SL : 0) + ws * 8192, l, ac[0], ac[1]);
    wait_vm<0>(); pin(xres); pin(r2u);
  };

  auto zw_flags = [&] { const int c0 = gi < 56 ? 4 * gi : 221;
    gvo = (selm(0xffffull, FZ0 + c0 / 15 * 16, selm(0xffffffffull, FZ0 - 16 + (c0 + 3) / 15 * 16, FW0 - 32 + (gi < 56 ? gi >> 3 : 7) * 32)) + lane_id()) * 8; };
  unsigned hso;
  auto hflag = [&] { wait_vm<0>(); st_flag1(rw, OFF_C + FH0 * 8 + (hso >> 9), tag); };
  if (wn == 0) {
    run(ic<0>{}, ic<-1>{});
    u32x4 xf[28], wf[RA], nb, eo;
    f32x16 acc[2][4], accG = zero16;
    auto gram = [&](auto I) { accG = mfma32(xf[I], xf[I], accG); };
    sfor<7>([&](auto J) { constexpr int j = J;
      vbar<pos<0>(mX + j) - pos<0>(eX + j)>();
      if constexpr (j == 0) { ST(1) }
#pragma unroll
      for (int i = 0; i < 4; ++i) {
        if constexpr (j == 3) { xo[i] += 3 * SL; pin(xo[i]); }
        xf[4 * j + i] = l128_at<(j < 3 ? j : j - 3) * SL>(xo[i]);
      }
      SB();
      run(ic<0>{}, ic<mX + j>{});
      if constexpr (j == 3) mk_tag();
      if constexpr (j > 0) sfor<4>([&](auto I) { gram(ic<4 * j - 4 + I>{}); });
      SB();
    });
    hso = ((4 * rb + ws) * 28 + unit1) * 4096;
    auto epi = [&](auto Pc, auto Qc, auto Ec) { constexpr int P = Pc, Q = Qc, e = Ec;
      float h0 = fmaxf(acc[P][2 * Q][2 * e], 0.f), h1 = fmaxf(acc[P][2 * Q][2 * e + 1], 0.f);
      h0 *= acc[P][2 * Q + 1][2 * e]; h1 *= acc[P][2 * Q + 1][2 * e + 1];
      opq(h0); opq(h1);
      unsigned t = pk2(h0, h1); pin(t);
      eo[e & 3] = t;
      if constexpr ((e & 3) == 3) st16_sc1<Q * 2048 + (e >> 2) * 1024>(eo, lvo, rw, hso);
    };
    unsigned a0, a1;
    auto step = [&](auto Tc, auto Endc) { constexpr int T = Tc, M = T - RA;
      if constexpr (T < Endc && T == t_start(t_per(T))) { constexpr int n = t_per(T);
        if constexpr (n == 0) vbar<6>();
        else if constexpr (n < 4) asm volatile("s_waitcnt vmcnt(%0)\n\ts_barrier" ::"n"(6 - 2 * n) : "memory");
        else asm volatile("s_barrier" ::: "memory");
        if constexpr (n == 0) { ST(2) }
        if constexpr (n == 7) { ST(3) }
        pin(wo); a0 = wo + (n % 5) * SL; a1 = a0 ^ 64; pin(a0); pin(a1);
      }
      if constexpr (M >= 0) { constexpr int P = M / 116, n = t_per(M), i = t_idx(M), q = i >= 16 ? i - 16 : 2 * (i >> 3) + (i & 1);
        if constexpr (i >= 16) acc[P][q] = mfma32(wf[M % RA], nb, acc[P][q]);
        else acc[P][q] = mfma32(wf[M % RA], xf[4 * (n % 7) + ((i >> 1) & 3)], (n % 7 == 0 && (i & 6) == 0) ? zero16 : acc[P][q]);
        pin(acc[P][q]);
      } else if constexpr (T < 4) gram(ic<24 + T>{});
      SB();
      if constexpr (T < Endc) { constexpr int i = t_idx(T);
        if constexpr (i >= 16) wf[T % RA] = l128_at<L_P - 8 * SL + (T / 116) * 4096 + (i - 16) * 1024>(lvo + (lds0 + 8 * SL));
        else { constexpr int o = ((i >> 1) & 3) * 4096 + (i & 1) * 1024; wf[T % RA] = tr_at<o, o + 512>(i >= 8 ? a1 : a0); }
      }
      SB();
      if constexpr (T == 7) gram_dump(lds0 + L_GR + ws * 1024 + lvo, accG);
      if constexpr (T == 9) asm volatile("s_waitcnt lgkmcnt(4)\n\ts_barrier" ::: "memory");
      if constexpr (T == 40) { const unsigned nm = lds_ld<unsigned, L_NM - 8 * SL>(lds0 + 8 * SL + ws * 256 + (lvo >> 2)); nb = u32x4{nm, nm, nm, nm}; }
      if constexpr (T == 44) pin(nb);
      if constexpr (M >= 0 && M % 116 >= 108) epi(ic<M / 116>{}, ic<0>{}, ic<M % 116 - 108>{});
      if constexpr (M >= 116 && M < 124) epi(ic<0>{}, ic<1>{}, ic<M - 116>{});
      if constexpr (M == 156) { hflag(); hso += (unit2 - unit1) * 4096; }
      SB();
    };
    sfor<116>([&](auto I) { step(I, ic<232>{}); });
    if (!bcu) {
      sfor<116 + RA>([&](auto I) { step(ic<116 + I>{}, ic<232>{}); });
      sfor<8>([&](auto I) { epi(ic<1>{}, ic<1>{}, I); });
      ST(4)
      tail_c(ic<0>{}, [&] { ST(5) hflag(); ST(6) });
    } else {
      sfor<RA>([&](auto I) { step(ic<116 + I>{}, ic<116>{}); });
      sfor<8>([&](auto I) { epi(ic<0>{}, ic<1>{}, I); });
      ST(3)
    }
  } else {
    __builtin_amdgcn_s_setprio(2);
    run(ic<0>{}, ic<-1>{});
    accA = zero16;
    sfor<7>([&](auto J) { constexpr int j = J;
      vbar<pos<0>(mX + j) - pos<0>(eX + j)>();
      if constexpr (j == 0) { ST(1) }
      const unsigned to = lvo + (lds0 + L_T1 + kof(j) * 4096);
      sfor<4>([&](auto I) { constexpr int i = I;
        if constexpr (j == 3) { xo[i] += 3 * SL; pin(xo[i]); }
        xa[j & 1][i] = l128_at<(j < 3 ? j : j - 3) * SL>(xo[i]); ta[j & 1][i] = l128_at<i * 1024>(to);
      });
      SB();
      run(ic<0>{}, ic<mX + j>{});
      if constexpr (j == 3) mk_tag();
      if constexpr (j > 0) sfor<4>([&](auto I) { accA = mfma32(ta[~j & 1][I], xa[~j & 1][I], accA); });
      SB();
    });
    auto period = [&](auto Rc, auto Nc) { constexpr int R = Rc, n = Nc;
      vbar<pos<R>(mP + n) - need<R>(n)>();
      if constexpr (n == 0) { ST(2) }
      if constexpr (n == 7) { ST(3) }
      if constexpr (n < 13) run(Rc, ic<mP + n>{});
      if constexpr (n == 0) { asm volatile("s_barrier" ::: "memory"); run(Rc, ic<mS>{}); }
    };
    sfor<4>([&](auto I) { period(ic<0>{}, I); });
    if (!bcu) {
      sfor<10>([&](auto I) { period(ic<0>{}, ic<I + 4>{}); });
      ST(4)
      tail_h(ic<0>{});
    } else {
      sfor<3>([&](auto I) { period(ic<1>{}, ic<I + 4>{}); });
      zw_flags();
      ld_flag(fv, gvo, ctl); wait_vm<0>(); pin(fv);
      ST(3)
      poll(fv, 0xffffffffull, gvo, ctl, tag);
      ST(4)
      const int c = gi < 56 ? 4 * gi + ws : 224;
      const unsigned zo = OFF_Z + (c + c / 15) * 8192;
      dma4_sc1<3 * SL>(lds0 + ws * 8192, lvo, rw, zo); dma4_sc1<3 * SL + 4096>(lds0 + ws * 8192, lvo, rw, zo + 4096);
    }
  }
  if (bcu) {
    const int l = lane_id(), v = l & 15, q = l >> 4, vp = 4 * (v & 3) + (v >> 2);
    __builtin_amdgcn_s_setprio(0);
    char* fs = smem + 3 * SL;
    lvo = (unsigned)l * 16;
    zw_flags(); ld_flag(fv, gvo, ctl);
    u32x4 wv[2][2], M1[4], M2[4], C32[2], S32[2], N32[2];
    const float k1f = (float)(16 * ws + v) * (1.0f / 64);
#pragma unroll
    for (int ks = 0; ks < 4; ++ks) {
#pragma unroll
      for (int e = 0; e < 4; ++e) {
        const float a = (float)(16 * ks + 4 * q + e) * k1f, c = vcos(a), s = vsin(a);
        M1[ks][e] = pk2(c, s); M2[ks][e] = pk2(-s, c);
      }
      pin(M1[ks]); pin(M2[ks]);
    }
#pragma unroll
    for (int t = 0; t < 2; ++t) {
      const float nf = (float)(16 * t + v) * (1.0f / 32);
#pragma unroll
      for (int e = 0; e < 4; ++e) {
        const float a0 = (float)(16 * (e >> 1) + 4 * q + 2 * (e & 1)) * nf, a1 = a0 + nf;
        C32[t][e] = pk2(vcos(a0), vcos(a1)); S32[t][e] = pk2(vsin(a0), vsin(a1)); N32[t][e] = S32[t][e] ^ 0x80008000u;
      }
      pin(C32[t]); pin(S32[t]); pin(N32[t]);
    }
    float t1c[2][4], t1s[2][4], u2c[2][4], u2s[2][4];
    constexpr float scale = 2.0f / (448.0f * 2048.0f);
#pragma unroll
    for (int t = 0; t < 2; ++t)
#pragma unroll
      for (int r = 0; r < 4; ++r) {
        const float a1 = (float)(16 * t + 4 * q + r) * (k1f * (1.0f / 32));
        t1c[t][r] = vcos(a1); t1s[t][r] = vsin(a1);
        const float a2 = (float)((16 * t + v) * (16 * ws + 4 * q + r)) * (1.0f / 2048);
        u2c[t][r] = vcos(a2) * scale; u2s[t][r] = vsin(a2) * scale;
        pin(t1c[t][r]); pin(t1s[t][r]); pin(u2c[t][r]); pin(u2s[t][r]);
      }
    SB();
    wait_vm<0>(); pin(fv);
    if (wn == 0) hflag();
    else poll(fv, ~0ull, gvo, ctl, tag);
    bar();
    ST(12)
#pragma unroll
    for (int k = 0; k < 2; ++k) {
      const unsigned wp = OFF_W + (gi < 56 ? 4 * gi + 2 * wn + k : 224) * 8192 + ws * 2048;
      ld_sc1<0>(wv[k][0], lvo, rw, wp); ld_sc1<1024>(wv[k][1], lvo, rw, wp);
    }
    f32x4 pr[2][2], pi[2][2];
#pragma unroll
    for (int k = 0; k < 2; ++k)
#pragma unroll
      for (int ks = 0; ks < 4; ++ks)
#pragma unroll
        for (int t = 0; t < 2; ++t) {
          const u32x4 z = lds128(fs + (2 * wn + k) * 8192 + t * 4096 + ks * 1024 + l * 16);
          pr[k][t] = mfma16(z, M1[ks], ks == 0 ? zero : pr[k][t]); pi[k][t] = mfma16(z, M2[ks], ks == 0 ? zero : pi[k][t]);
        }
    bar();
#pragma unroll
    for (int k = 0; k < 2; ++k) {
      u32x4 fr, fi;
      {
        float a[8], bq[8];
#pragma unroll
        for (int t = 0; t < 2; ++t)
#pragma unroll
          for (int r = 0; r < 4; ++r) {
            a[4 * t + r] = pr[k][t][r] * t1c[t][r] + pi[k][t][r] * t1s[t][r];
            bq[4 * t + r] = pi[k][t][r] * t1c[t][r] - pr[k][t][r] * t1s[t][r];
          }
#pragma unroll
        for (int e = 0; e < 4; ++e) { fr[e] = pk2(a[2 * e], a[2 * e + 1]); fi[e] = pk2(bq[2 * e], bq[2 * e + 1]); }
      }
      f32x4 qr[2], qi[2];
#pragma unroll
      for (int t = 0; t < 2; ++t) {
        qr[t] = mfma16(C32[t], fr, zero); qi[t] = mfma16(C32[t], fi, zero);
        qr[t] = mfma16(S32[t], fi, qr[t]); qi[t] = mfma16(N32[t], fr, qi[t]);
      }
      if (k == 0) { wait_vm<0>(); pin(wv[0][0]); pin(wv[0][1]); pin(wv[1][0]); pin(wv[1][1]); }
      {
        float a[8], bq[8];
#pragma unroll
        for (int t = 0; t < 2; ++t)
#pragma unroll
          for (int r = 0; r < 4; ++r) {
            const float wre = bflo(wv[k][t][r]), wim = bfhi(wv[k][t][r]);
            a[4 * t + r] = qr[t][r] * wre - qi[t][r] * wim;
            bq[4 * t + r] = qr[t][r] * wim + qi[t][r] * wre;
          }
#pragma unroll
        for (int e = 0; e < 4; ++e) { fr[e] = pk2(a[2 * e], a[2 * e + 1]); fi[e] = pk2(bq[2 * e], bq[2 * e + 1]); }
      }
#pragma unroll
      for (int t = 0; t < 2; ++t) {
        f32x4 rr = mfma16(fr, C32[t], zero), ri = mfma16(fi, C32[t], zero);
        rr = mfma16(fi, N32[t], rr); ri = mfma16(fr, S32[t], ri);
        u32x4 o;
#pragma unroll
        for (int r = 0; r < 4; ++r) o[r] = pk2(rr[r] * u2c[t][r] - ri[r] * u2s[t][r], -(rr[r] * u2s[t][r]) - ri[r] * u2c[t][r]);
        *(u32x4*)(fs + (2 * wn + k) * 8192 + (16 * t + v) * 256 + (((4 * ws + q) ^ vp) * 16)) = o;
      }
    }
    bar();
    f32x4 yr[4], yi[4];
#pragma unroll
    for (int ks = 0; ks < 4; ++ks)
#pragma unroll
      for (int k = 0; k < 4; ++k) {
        const u32x4 rf = lds128(fs + k * 8192 + (16 * wn + vp) * 256 + (((4 * ks + q) ^ v) * 16));
        yr[k] = mfma16(rf, M1[ks], ks == 0 ? zero : yr[k]); yi[k] = mfma16(rf, M2[ks], ks == 0 ? zero : yi[k]);
      }
    const unsigned yb = OFF_Y + gi * 32768 + ws * 8192 + wn * 1024;
    sfor<4>([&](auto Rc) { constexpr int R = Rc;
      st16_sc1<R * 256>(u32x4{pk2(yr[0][R], yi[0][R]), pk2(yr[1][R], yi[1][R]), pk2(yr[2][R], yi[2][R]), pk2(yr[3][R], yi[3][R])},
                        (unsigned)((v >> 2) * 2048 + q * 64 + (v & 3) * 16), rw, yb);
    });
    ST(13)
    vbar<0>();
    if (w == 0) st_flag1(rw, OFF_C + (FY0 + 16 * gi) * 8, tag);
    if (wn == 0) tail_c(ic<1>{}, [] {}); else { mk_dv(); tail_h(ic<1>{}); }
  }
  bar();
  if (!bcu) { ST(12) }
  if (!bcu || wn == 0) {
    const int l = lane_id(), ch = l & 7;
    const unsigned q = lds0 + ch * 1024 + (((8 * ws + (l >> 3)) ^ (2 * ch)) * 16), qh = q + 5 * SL + wn * 8192, qc = q + (bcu ? 3 * SL : wn * 8192);
    f32x4 a0 = lds_ld<f32x4, 0>(qh), a1 = lds_ld<f32x4, 512>(qh), c0 = lds_ld<f32x4, 0>(qc), c1 = lds_ld<f32x4, 512>(qc);
    sfor<3>([&](auto I) { a0 += lds_ld<f32x4, (I + 1) * SL>(qh); a1 += lds_ld<f32x4, (I + 1) * SL + 512>(qh); });
    c0 += lds_ld<f32x4, SL>(qc); c1 += lds_ld<f32x4, SL + 512>(qc);
    if (bcu) { c0 += lds_ld<f32x4, 8192>(qc) + lds_ld<f32x4, SL + 8192>(qc); c1 += lds_ld<f32x4, 8704>(qc) + lds_ld<f32x4, SL + 8704>(qc); }
    const float r2 = __uint_as_float(r2u);
    u32x4 o;
    o[0] = pk2(a0[0] * r2 + (c0[0] + bflo(xres[0])), a0[1] * r2 + (c0[1] + bfhi(xres[0])));
    o[1] = pk2(a0[2] * r2 + (c0[2] + bflo(xres[1])), a0[3] * r2 + (c0[3] + bfhi(xres[1])));
    o[2] = pk2(a1[0] * r2 + (c1[0] + bflo(xres[2])), a1[1] * r2 + (c1[1] + bfhi(xres[2])));
    o[3] = pk2(a1[2] * r2 + (c1[2] + bflo(xres[3])), a1[3] * r2 + (c1[3] + bfhi(xres[3])));
    *(u32x4*)((char*)out + ovo) = o;
  }
  asm volatile(".Lpf_end:");
  if (b == 0 && w == 0) st_flag1(rw, OFF_C, tag);
  STAMP_END(bcu ? 1 : 0)
}

extern "C" void kernel_launch(void* const* d_in, const int* in_sizes, int n_in, void* d_out, int out_size, void* d_ws, size_t ws_size,
                              hipStream_t stream) {
  (void)hipFuncSetAttribute((const void*)fwd, hipFuncAttributeMaxDynamicSharedMemorySize, 163840);
  fwd<<<256, 512, 163840, stream>>>((const uint16_t*)d_in[0], (const uint16_t*)d_in[1], (const uint16_t*)d_in[2], (const uint16_t*)d_in[4],
                                    (const uint16_t*)d_in[5], (const uint16_t*)d_in[6], (char*)d_ws, (uint16_t*)d_out STAMP_PASS);
}
```

```cpp
#include <hip/hip_runtime.h>
#include <hip/hip_bf16.h>
#include <stdint.h>
#include <utility>

typedef __attribute__((ext_vector_type(4))) short s16x4;
typedef __attribute__((ext_vector_type(8))) __bf16 bf16x8;
typedef __attribute__((ext_vector_type(2))) __bf16 bf16x2;
typedef __attribute__((ext_vector_type(4))) float f32x4;
typedef __attribute__((ext_vector_type(16))) float f32x16;
typedef __attribute__((ext_vector_type(2))) float f32x2;
typedef __attribute__((ext_vector_type(4))) unsigned u32x4;
typedef __attribute__((ext_vector_type(2))) unsigned u32x2;
typedef __attribute__((address_space(3))) s16x4* lds_s16x4_p;
typedef unsigned long long u64;
typedef __attribute__((address_space(1))) u64 gu64;
typedef __attribute__((address_space(3))) unsigned lu32;
#define DI __device__ __forceinline__
#define SB() __builtin_amdgcn_sched_barrier(0)
template <int N> using ic = std::integral_constant<int, N>;

constexpr int S_ = 2048, D_ = 448, F_ = 1792, DF_ = 225;

template <class Fn, int... I> DI void sfor_impl(Fn&& f, std::integer_sequence<int, I...>) { (f(ic<I>{}), ...); }
template <int N, class Fn> DI void sfor(Fn&& f) { sfor_impl(f, std::make_integer_sequence<int, N>{}); }

DI f32x4 mfma16(u32x4 a, u32x4 b, f32x4 c) {
  return __builtin_amdgcn_mfma_f32_16x16x32_bf16(__builtin_bit_cast(bf16x8, a), __builtin_bit_cast(bf16x8, b), c, 0, 0, 0);
}
DI f32x16 mfma32(u32x4 a, u32x4 b, f32x16 c) {
  return __builtin_amdgcn_mfma_f32_32x32x16_bf16(__builtin_bit_cast(bf16x8, a), __builtin_bit_cast(bf16x8, b), c, 0, 0, 0);
}
DI unsigned pk2(float lo, float hi) { bf16x2 p; p[0] = (__bf16)lo; p[1] = (__bf16)hi; return __builtin_bit_cast(unsigned, p); }
DI float bflo(unsigned u) { return __builtin_bit_cast(float, u << 16); }
DI float bfhi(unsigned u) { return __builtin_bit_cast(float, u & 0xffff0000u); }
DI float rbf(float f) { return (float)(__bf16)f; }
typedef __attribute__((address_space(3))) const char* lds_cp;
typedef __attribute__((address_space(3))) const u32x4* lds_u32x4_p;
template <int O0, int O1> DI u32x4 tr_at(unsigned a) {
  s16x4 x = __builtin_amdgcn_ds_read_tr16_b64_v4i16((lds_s16x4_p)((lds_cp)(uintptr_t)a + O0));
  s16x4 y = __builtin_amdgcn_ds_read_tr16_b64_v4i16((lds_s16x4_p)((lds_cp)(uintptr_t)a + O1));
  return __builtin_bit_cast(u32x4, __builtin_shufflevector(x, y, 0, 1, 2, 3, 4, 5, 6, 7));
}
template <int O> DI u32x4 l128_at(unsigned a) { return *(lds_u32x4_p)((lds_cp)(uintptr_t)a + O); }
DI unsigned lds_addr(const void* p) { return (unsigned)(uintptr_t)(__attribute__((address_space(3))) const char*)p; }
template <int N> DI void wait_vm() { asm volatile("s_waitcnt vmcnt(%0)" ::"n"(N) : "memory"); }
DI void bar() { asm volatile("s_waitcnt lgkmcnt(0)" ::: "memory"); __builtin_amdgcn_s_barrier(); asm volatile("" ::: "memory"); }
DI u32x4 lds128(const char* p) { return *(const u32x4*)p; }
DI int lane_id() { int r; asm volatile("v_mbcnt_lo_u32_b32 %0, -1, 0\n\tv_mbcnt_hi_u32_b32 %0, -1, %0" : "=v"(r)); return r; }
DI float vcos(float a) { return __builtin_amdgcn_cosf(a); }
DI float vsin(float a) { return __builtin_amdgcn_sinf(a); }

#define STINS "s_memrealtime"
#define STAMP_BEGIN unsigned long long _t0; asm volatile("s_memrealtime %0\n\ts_waitcnt lgkmcnt(0)" : "=s"(_t0) :: "memory"); ST(0)
DI void padto(unsigned long long t0) { SB();
#pragma unroll 1
  for (int it = 0; it < 100000; ++it) { unsigned long long t; asm volatile("s_memrealtime %0\n\ts_waitcnt lgkmcnt(0)" : "=s"(t) :: "memory"); if (t - t0 >= 500) break; __builtin_amdgcn_s_sleep(1); }
  SB(); }
#define ST(i) if constexpr (i == 0) padto(_t0);
#define STAMP_END(kid) ST(14)
#define STAMP_ARG
#define STAMP_PASS

constexpr unsigned ONES = 0x3f803f80u;
constexpr int SL = 16384;
constexpr int L_T1 = 8 * SL, L_GR = 9 * SL + 12288, L_P = 9 * SL, L_NM = 9 * SL + 8192;
constexpr unsigned OFF_Z = 8u << 20, OFF_W = 10u << 20, OFF_Y = 12u << 20, OFF_C = 14u << 20, OFF_S = 15u << 20;
constexpr int FZ0 = 512, FW0 = 1024, FY0 = 2048, FH0 = 4096;
constexpr int SPIN_CAP = 200000;
constexpr int RA = 6;

enum : int { eX = 0, eW = 8, eD = 24, eDR = 40, eH = 48, eEP = 80, ePF, eT1, ePRE, eA6, eCS, eCS2, eZ, eFWL, eWT, eZF, eWF, eCL, eCL2, eCP, eCP2, eFL1, eP1, eFL2, eP2, eRES,
             mX = 200, mS = 208, mP = 210, mK = 230, mEnd = 500 };
struct Ev { int id, n; };
#define PRO {eX + 0, 2}, {eX + 1, 2}, {eX + 2, 2}, {eEP, 1}, {ePF, 1}, {eT1, 0}, {eX + 3, 2}, {ePRE, 0}, \
  {mX + 0, 0}, {eX + 4, 2}, {mX + 1, 0}, {eX + 5, 2}, {mX + 2, 0}, {eX + 6, 2}, {mX + 3, 0}, {eW + 0, 2}, {mX + 4, 0}, {eW + 1, 2}, \
  {mX + 5, 0}, {eW + 2, 2}, {mX + 6, 0}, {eW + 3, 2}, \
  {mP + 0, 0}, {eA6, 0}, {eCS, 1}, {eW + 4, 4}, {mS, 0}, {eZ, 8}, \
  {mP + 1, 0}, {eFWL, 16}, {eD + 0, 4}, {mP + 2, 0}, {eW + 5, 4}, {eD + 1, 4}, {mP + 3, 0}, {eZF, 0}, {eW + 6, 4}, {eWT, 2}, {eD + 2, 4}
constexpr Ev SEQ_R[] = {PRO,
  {mP + 4, 0}, {eW + 7, 4}, {eCL, 4}, {eD + 3, 4}, {eDR + 0, 0},
  {mP + 5, 0}, {eWF, 0}, {eW + 8, 4}, {eD + 4, 4}, {eCP, 0},
  {mP + 6, 0}, {eW + 9, 4}, {eD + 5, 4}, {eDR + 1, 0},
  {mP + 7, 0}, {eCS2, 1}, {eW + 10, 4}, {eD + 6, 4},
  {mP + 8, 0}, {eW + 11, 4}, {eD + 7, 4}, {eDR + 2, 0},
  {mP + 9, 0}, {eW + 12, 4}, {eD + 8, 4},
  {mP + 10, 0}, {eW + 13, 4}, {eD + 9, 4}, {eDR + 3, 0},
  {mP + 11, 0}, {eCL2, 4}, {eD + 10, 4},
  {mP + 12, 0}, {eD + 11, 4}, {eCP2, 0}, {eFL1, 1},
  {mP + 13, 0}, {eP1, 0}, {eH + 0, 2}, {eH + 1, 2}, {eH + 2, 2}, {eH + 3, 2}, {eH + 4, 2},
  {mK + 0, 0}, {eH + 5, 2}, {eH + 6, 2}, {mK + 1, 0}, {eH + 7, 2}, {eH + 8, 2}, {mK + 2, 0}, {eH + 9, 2}, {eH + 10, 2}, {mK + 3, 0}, {eH + 11, 2}, {eH + 12, 2},
  {mK + 4, 0}, {eH + 13, 2}, {eH + 14, 2}, {mK + 5, 0}, {eH + 15, 2}, {mK + 6, 0}, {mK + 7, 0}, {eDR + 4, 0},
  {mK + 8, 0}, {eD + 12, 4}, {mK + 9, 0}, {eD + 13, 4}, {mK + 10, 0}, {eRES, 2}, {mK + 11, 0}, {eDR + 5, 0},
  {mK + 12, 0}, {mK + 13, 0}, {eFL2, 1}, {mK + 14, 0}, {mK + 15, 0}, {eP2, 0},
  {eH + 16, 2}, {eH + 17, 2}, {eH + 18, 2}, {eH + 19, 2}, {eH + 20, 2}, {eH + 21, 2}, {eH + 22, 2}, {eH + 23, 2}, {eH + 24, 2}, {eH + 25, 2}, {eH + 26, 2}, {eH + 27, 2},
  {mK + 16, 0}, {mK + 17, 0}, {mK + 18, 0}, {mK + 19, 0}, {eDR + 6, 0}, {mK + 20, 0}, {mK + 21, 0}, {mK + 22, 0}, {mK + 23, 0},
  {mK + 24, 0}, {mK + 25, 0}, {mK + 26, 0}, {mK + 27, 0}, {mEnd, 0}};
constexpr Ev SEQ_B[] = {PRO,
  {mP + 4, 0}, {eCL, 4}, {eD + 3, 4}, {mP + 5, 0}, {eWF, 0}, {eCP, 0}, {mP + 6, 0},
  {mP + 13, 0}, {eFL1, 1}, {eDR + 0, 0}, {eDR + 1, 0}, {eD + 4, 4}, {eD + 5, 4}, {eP1, 0},
  {eH + 0, 1}, {eH + 1, 1}, {eH + 2, 1}, {eH + 3, 1}, {eH + 4, 1}, {eH + 5, 1}, {eH + 6, 1}, {eH + 7, 1}, {eD + 6, 4}, {eD + 7, 4},
  {eH + 8, 1}, {eH + 9, 1}, {eH + 10, 1}, {eH + 11, 1}, {eH + 12, 1}, {eH + 13, 1}, {eH + 14, 1}, {eH + 15, 1}, {eDR + 2, 0}, {eD + 8, 4}, {eD + 9, 4},
  {mK + 0, 0}, {mK + 1, 0}, {mK + 2, 0}, {mK + 3, 0}, {mK + 4, 0}, {mK + 5, 0}, {mK + 6, 0}, {mK + 7, 0}, {eDR + 3, 0}, {eD + 10, 4}, {eD + 11, 4}, {eFL2, 1},
  {mK + 8, 0}, {mK + 9, 0}, {mK + 10, 0}, {mK + 11, 0}, {mK + 12, 0}, {mK + 13, 0}, {mK + 14, 0}, {mK + 15, 0}, {eDR + 4, 0}, {eD + 12, 4}, {eD + 13, 4}, {eP2, 0},
  {eH + 16, 1}, {eH + 17, 1}, {eH + 18, 1}, {eH + 19, 1}, {eH + 20, 1}, {eH + 21, 1}, {eH + 22, 1}, {eH + 23, 1}, {eH + 24, 1}, {eH + 25, 1}, {eH + 26, 1}, {eH + 27, 1},
  {eDR + 5, 0}, {eDR + 6, 0},
  {mK + 16, 0}, {mK + 17, 0}, {mK + 18, 0}, {mK + 19, 0}, {mK + 20, 0}, {mK + 21, 0}, {mK + 22, 0}, {mK + 23, 0},
  {mK + 24, 0}, {mK + 25, 0}, {mK + 26, 0}, {mK + 27, 0}, {mEnd, 0}};
template <int R> constexpr Ev seq(int i) { return R ? SEQ_B[i] : SEQ_R[i]; }
template <int R> constexpr int sidx(int id) { int i = 0; while (seq<R>(i).id != id && seq<R>(i).id != mEnd) ++i; return i; }
template <int R> constexpr bool has(int id) { return seq<R>(sidx<R>(id)).id == id; }
template <int R> constexpr int pos(int id) { int p = has<R>(id) ? 0 : 1000; for (int i = 0; i <= sidx<R>(id); ++i) p += seq<R>(i).n; return p; }
template <int R> constexpr int before(int id) { return pos<R>(id) - seq<R>(sidx<R>(id)).n; }
template <int R> constexpr int seglen(int i0) { int n = 0; while (seq<R>(i0 + n).id < mX) ++n; return n; }
template <int R> constexpr bool inper(int id, int n) { return sidx<R>(id) > sidx<R>(mP + n) && sidx<R>(id) < sidx<R>(mP + n + 1); }
constexpr int cmax(int a, int b) { return a > b ? a : b; }
template <int R> constexpr int need(int n) {
  int p = pos<R>(eW + n);
  if (inper<R>(eZF, n)) p = cmax(p, pos<R>(eZ));
  if (inper<R>(eWF, n)) p = cmax(p, pos<R>(eWT));
  return p;
}
constexpr int t_start(int n) { return 116 * (n / 7) + 16 * (n % 7); }
constexpr int t_per(int T) { return 7 * (T / 116) + (T % 116 >= 96 ? 6 : T % 116 / 16); }
constexpr int t_idx(int T) { const int u = T % 116; return u < 96 ? u % 16 : u < 100 ? u - 80 : u - 100; }

typedef __amdgpu_buffer_rsrc_t srd_t;
DI srd_t mk_srd(const void* p, int bytes) { return __builtin_amdgcn_make_buffer_rsrc((void*)p, 0, bytes, 0x00020000); }
template <int L> DI void dma2(unsigned lw, unsigned v0, srd_t r0, unsigned v1, srd_t r1, unsigned so) {
  asm volatile("s_add_u32 m0, %0, %1\n\ts_nop 0\n\tbuffer_load_dwordx4 %2, %3, %6 offen lds\n\tbuffer_load_dwordx4 %4, %5, %6 offen offset:1024 lds"
               ::"s"(lw), "n"(L), "v"(v0), "s"(r0), "v"(v1), "s"(r1), "s"(so) : "memory", "scc");
}
template <int L> DI void dma4(unsigned lw, unsigned v0, unsigned v1, unsigned v2, unsigned v3, srd_t r0, srd_t r1, unsigned so) {
  asm volatile("s_add_u32 m0, %0, %1\n\ts_nop 0\n\tbuffer_load_dwordx4 %2, %6, %8 offen lds\n\tbuffer_load_dwordx4 %3, %7, %8 offen offset:1024 lds\n\t"
               "buffer_load_dwordx4 %4, %6, %8 offen offset:2048 lds\n\tbuffer_load_dwordx4 %5, %7, %8 offen offset:3072 lds"
               ::"s"(lw), "n"(L), "v"(v0), "v"(v1), "v"(v2), "v"(v3), "s"(r0), "s"(r1), "s"(so) : "memory", "scc");
}
template <int L> DI void dma4_sc1(unsigned lw, unsigned v, srd_t r, unsigned so) {
  asm volatile("s_add_u32 m0, %0, %1\n\ts_nop 0\n\tbuffer_load_dwordx4 %2, %3, %4 offen sc1 lds\n\tbuffer_load_dwordx4 %2, %3, %4 offen offset:1024 sc1 lds\n\t"
               "buffer_load_dwordx4 %2, %3, %4 offen offset:2048 sc1 lds\n\tbuffer_load_dwordx4 %2, %3, %4 offen offset:3072 sc1 lds"
               ::"s"(lw), "n"(L), "v"(v), "s"(r), "s"(so) : "memory", "scc");
}
template <int OFF> DI void ld_sc1(u32x4& d, unsigned voff, srd_t r, unsigned so) { asm volatile("buffer_load_dwordx4 %0, %1, %2, %3 offen offset:%4 sc1" : "=v"(d) : "v"(voff), "s"(r), "s"(so), "n"(OFF) : "memory"); }
template <int OFF> DI void ld_sc1_over(u32x4& d, unsigned voff, srd_t r, unsigned so) { asm volatile("buffer_load_dwordx4 %0, %1, %2, %3 offen offset:%4 sc1" : "+v"(d) : "v"(voff), "s"(r), "s"(so), "n"(OFF) : "memory"); }
DI void ld_pl(u32x4& d, unsigned voff, const void* sb) { asm volatile("global_load_dwordx4 %0, %1, %2" : "=v"(d) : "v"(voff), "s"(sb) : "memory"); }
template <int OFF> DI void ld_u16(unsigned& d, unsigned voff, srd_t r, unsigned so) { asm volatile("buffer_load_ushort %0, %1, %2, %3 offen offset:%4" : "=v"(d) : "v"(voff), "s"(r), "s"(so), "n"(OFF) : "memory"); }
DI void ld_u32(unsigned& d, unsigned voff, const void* sb) { asm volatile("global_load_dword %0, %1, %2" : "=v"(d) : "v"(voff), "s"(sb) : "memory"); }
DI void ld_u32_sc1(unsigned& d, unsigned voff, srd_t r, unsigned so) { asm volatile("buffer_load_dword %0, %1, %2, %3 offen sc1" : "=v"(d) : "v"(voff), "s"(r), "s"(so) : "memory"); }
DI void ld_flag(u64& d, unsigned voff, const void* sb) { asm volatile("global_load_dwordx2 %0, %1, %2 sc1" : "=v"(d) : "v"(voff), "s"(sb) : "memory"); }
template <int OFF = 0> DI void st16_sc1(u32x4 v, unsigned voff, srd_t r, unsigned so) {
  asm volatile("buffer_store_dwordx4 %0, %1, %2, %3 offen offset:%4 sc1\n\ts_nop 1" ::"v"(v), "v"(voff), "s"(r), "s"(so), "n"(OFF) : "memory");
}
DI void st4_sc1(unsigned v, unsigned voff, srd_t r, unsigned so) { asm volatile("buffer_store_dword %0, %1, %2, %3 offen sc1" ::"v"(v), "v"(voff), "s"(r), "s"(so) : "memory"); }
DI void st_flag1(srd_t r, unsigned so, u64 tag) {
  asm volatile("s_mov_b64 exec, 1\n\tbuffer_store_dwordx2 %0, off, %1, %2 sc1\n\ts_mov_b64 exec, -1" ::"v"(tag), "s"(r), "s"(so) : "memory");
}
template <class T> DI void pin(T& a) { asm volatile("" : "+v"(a)); }
template <class T> DI void pins(T& a) { asm volatile("" : "+s"(a)); }
template <class T> DI void opq(T& a) { asm("" : "+v"(a)); }
DI unsigned selm_n(u64 m, unsigned a, unsigned b) { unsigned r; asm("v_cndmask_b32_e64 %0, %2, %1, %3\n\ts_nop 1" : "=v"(r) : "v"(a), "v"(b), "s"(m)); return r; }
DI unsigned selm(u64 m, unsigned a, unsigned b) { unsigned r; asm("v_cndmask_b32_e64 %0, %2, %1, %3" : "=v"(r) : "v"(a), "v"(b), "s"(m)); return r; }
template <int N> DI void vbar() {
  static_assert(N >= 0, "wait");
  asm volatile("s_waitcnt vmcnt(%0) lgkmcnt(0)\n\ts_barrier" ::"n"(N > 63 ? 63 : N) : "memory");
}
template <int N> DI void wvm() { static_assert(N >= 0, "wait"); wait_vm<(N > 63 ? 63 : N)>(); }
DI void poll(u64& fv, u64 mask, unsigned voff, const void* sb, u64 tag) {
  if ((~__builtin_amdgcn_ballot_w64(fv == tag) & mask) != 0) {
#pragma unroll 1
    for (int it = 0; it < SPIN_CAP; ++it) {
      __builtin_amdgcn_s_sleep(1);
      ld_flag(fv, voff, sb); wait_vm<0>(); pin(fv);
      if ((~__builtin_amdgcn_ballot_w64(fv == tag) & mask) == 0) break;
    }
  }
  asm volatile("" ::: "memory");
}
typedef __attribute__((address_space(3))) char* lds_p;
template <class T, int O> DI T lds_ld(unsigned a) { return *(__attribute__((address_space(3))) const T*)((lds_cp)(uintptr_t)a + O); }
template <class T, int O> DI void lds_st(unsigned a, T v) { *(__attribute__((address_space(3))) T*)((lds_p)(uintptr_t)a + O) = v; }
DI void gram_dump(unsigned a, f32x16 g) {
  asm volatile("s_mov_b32 exec_lo, 0xff\n\ts_mov_b32 exec_hi, 0xff\n\tds_write_b128 %0, %1\n\ts_lshl_b64 exec, exec, 8\n\tds_write_b128 %0, %2\n\t"
               "s_lshl_b64 exec, exec, 8\n\tds_write_b128 %0, %3\n\ts_lshl_b64 exec, exec, 8\n\tds_write_b128 %0, %4\n\ts_mov_b64 exec, -1"
               ::"v"(a), "v"(__builtin_shufflevector(g, g, 0, 1, 2, 3)), "v"(__builtin_shufflevector(g, g, 4, 5, 6, 7)),
                 "v"(__builtin_shufflevector(g, g, 8, 9, 10, 11)), "v"(__builtin_shufflevector(g, g, 12, 13, 14, 15)) : "memory", "scc");
}
DI void put_part(unsigned a, int l, const f32x16& c0, const f32x16& c1) {
  sfor<8>([&](auto K) { constexpr int k = K; const f32x16& c = k < 4 ? c0 : c1;
    lds_st<f32x4, k * 1024>(a + (l >> 5) * 512 + (((l & 31) ^ (2 * k)) * 16), f32x4{c[4 * (k & 3)], c[4 * (k & 3) + 1], c[4 * (k & 3) + 2], c[4 * (k & 3) + 3]});
  });
}

__global__ __launch_bounds__(512, 2) void fwd(const uint16_t* __restrict__ x, const uint16_t* __restrict__ wr, const uint16_t* __restrict__ wi,
                                              const uint16_t* __restrict__ wg, const uint16_t* __restrict__ wu, const uint16_t* __restrict__ wd,
                                              char* __restrict__ ws_, uint16_t* __restrict__ out STAMP_ARG) {
  extern __shared__ __attribute__((aligned(16))) char smem[];
  STAMP_BEGIN
  const int w = __builtin_amdgcn_readfirstlane(threadIdx.x >> 6), ws = w & 3, wn = w >> 2;
  const unsigned b = blockIdx.x;
  const int xcd = b & 7, idx = b >> 3;
  const unsigned lds0 = lds_addr(smem), lw = lds0 + w * 2048;
  const int rb = (xcd & 1) * 8 + (idx & 7), g = xcd >> 1, ii = idx >> 3;
  const bool bcu = ii == 3;
  const int unit1 = 4 * g + ii, unit2 = 16 + 3 * g + ii;
  const int r0 = (unsigned)(rb + 2 * ii + g) % 7;
  auto kof = [&](int st) { int k = st + r0; return k >= 7 ? k - 7 : k; };
  constexpr unsigned CH = 64 * F_ * 2;
  unsigned xvo[2];
  const srd_t rx = mk_srd(x, S_ * D_ * 2), rg = mk_srd(wg, D_ * F_ * 2), ru = mk_srd((const char*)wu - 1024, D_ * F_ * 2 + 1024);
  const srd_t rd = mk_srd(wd, D_ * F_ * 2), rw = mk_srd(ws_, 16 << 20);
  const int rt = 4 * xcd + (idx & 3), dt = bcu ? 6 : (idx >> 2);
  const int rot = (idx & 3) + (idx >> 2), rot3 = (unsigned)rot % 3;
  auto rnd = [&](int i) { if (i < 4) return (i + rot) & 3; const int k = i - 4 + rot3; return 4 + (k >= 3 ? k - 3 : k); };
  const int rtb = 2 * rt + (bcu ? (idx >> 2) & 1 : 0);
  const int gi = (idx & 7) * 8 + xcd;
  const char* ctl = ws_ + OFF_C;
  const f32x4 zero = {0.f, 0.f, 0.f, 0.f};
  const f32x16 zero16 = {};

  u64 ep, fv, tag, pc;
  asm volatile("s_getpc_b64 %0\n.Lpf_here:" : "=s"(pc));
  unsigned pf, lvo, wvo, wo, xo[4], gvo, ovo, r2u;
  u32x4 xres;
  unsigned wv1, dv[4], ao, fl[16];
  u32x4 xa[2][4], ta[2][4], cg[4], wdf[7][8], hf[28][2];
  f32x16 accA, ah[2][2];
  auto res = [&](int R, int u) {
    const int l = lane_id(), pp = 32 * (rtb & 3) + 8 * (R ? ws : w) + (l >> 3);
    ld_u32_sc1(r2u, (unsigned)((pp >> 6) * 4096 + (pp & 63) * 4), rw, OFF_Z + (16 * u + 15) * 8192 + (rt >> 1) * 256);
    ovo = (unsigned)((128 * (rt >> 1) + 32 * (pp & 3) + (pp >> 2)) * D_ + dt * 64 + (l & 7) * 8) * 2;
    ld_pl(xres, ovo, x);
  };
  auto mk_dv = [&] { const int l = lane_id();
    dv[0] = (unsigned)((64 * ws + (l >> 3)) * D_ * 2 + dt * 128 + (((l & 7) ^ ((l >> 2) & 4)) * 16));
#pragma unroll
    for (int i = 1; i < 4; ++i) { dv[i] = dv[0] + i * (8 * D_ * 2 - 1024); pin(dv[i]); }
    ao = lds0 + (5 + ws) * SL + (4 * (l >> 5) + ((l & 15) >> 2)) * 128 + ((l >> 3) & 1) * 64 + ((l >> 4) & 1) * 32 + (l & 3) * 8;
    pin(ao);
  };
  auto ev = [&](auto Rc, auto Ic) { constexpr int R = Rc, id = Ic;
    if constexpr (id >= eX && id < eX + 7) { constexpr int j = id - eX;
      if constexpr (j == 0) {
        const int l = lane_id();
#pragma unroll
        for (int a = 0; a < 2; ++a) {
          const int p = 16 * w + 8 * a + (l >> 3);
          xvo[a] = (unsigned)((128 * rb + 32 * (p & 3) + (p >> 2)) * D_ + (((l & 7) ^ (4 * a + (l >> 4))) * 8)) * 2 - 1024 * a;
        }
      }
      dma2<(j + 1) * SL>(lw, xvo[0], rx, xvo[1], rx, kof(j) * 128);
    } else if constexpr (id >= eW && id < eW + 14) { constexpr int j = id - eW;
      if constexpr (j < 4) dma2<j * SL>(lw, wvo, rg, wvo, ru, kof(j) * CH);
      else dma4<(j % 5) * SL>(lds0 + ws * 4096, wvo, wvo, wv1, wv1, rg, ru, kof(j % 7) * CH + (j >= 7 ? (unit2 - unit1) * 128 : 0));
    } else if constexpr (id >= eD && id < eD + 14) { constexpr int i = id - eD;
      if constexpr (i >= 4 && !(i & 1)) asm volatile("s_waitcnt lgkmcnt(0)" ::: "memory");
      dma4<5 * SL + (i & 3) * 4096>(lds0 + ws * SL, dv[0], dv[1], dv[2], dv[3], rd, rd, rnd(i >> 1) * CH + (i & 1) * (32 * D_ * 2));
    } else if constexpr (id >= eDR && id < eDR + 7) { constexpr int i = id - eDR;
      wvm<pos<R>(id) - pos<R>(eD + 2 * i + 1)>();
      unsigned a1 = ao ^ 64; pin(a1);
      sfor<8>([&](auto K) { constexpr int o = (i & 1) * 8192 + (K >> 1) * 2048; wdf[i][K] = tr_at<o, o + 1024>((K & 1) ? a1 : ao); });
    } else if constexpr (id >= eH && id < eH + 28) { constexpr int s = id - eH;
      const unsigned so = (rtb * 28 + ws) * 4096 + rnd(s >> 2) * 16384;
      if constexpr (R) ld_sc1<(s & 3) * 1024>(hf[s][0], lvo, rw, so);
      else sfor<2>([&](auto C) { constexpr int c = C, k = (s - 5) >> 1;
        if constexpr (s < 5) ld_sc1<(s & 3) * 1024>(hf[s][c], lvo, rw, so + c * 28 * 4096);
        else {
          if constexpr (s >= 26) hf[s][c] = wdf[3][2 * (s - 26) + c];
          else if constexpr (s >= 16) hf[s][c] = hf[s - 10][c];
          else if constexpr (s & 1) hf[s][c] = hf[k][c];
          else hf[s][c] = wdf[k >> 2][2 * (k & 3) + c];
          ld_sc1_over<(s & 3) * 1024>(hf[s][c], lvo, rw, so + c * 28 * 4096);
        }
      });
    } else if constexpr (id == eEP) {
      ld_flag(ep, 0u, ctl);
    } else if constexpr (id == ePF) {
      unsigned len;
      asm volatile("s_mov_b32 %0, .Lpf_end-.Lpf_here" : "=s"(len));
      const unsigned o = (unsigned)((idx * 8 + w) * 4 + (lane_id() & 3)) * 64;
      ld_u32(pf, o < len ? o : 0u, (const void*)pc);
    } else if constexpr (id == eT1) {
      const int l = lane_id(), hh = l >> 5, fi = (l & 31) >> 1, f = fi == 15 ? 0 : 15 * unit1 + fi;
      const float cf_ = (float)f * (1.0f / 448);
      const unsigned sg = (f & 1) ? 0x80008000u : 0u;
#pragma unroll 1
      for (int s = w; s < 14; s += 8) {
        const float a0 = (float)(16 * s + 8 * hh) * cf_ + ((l & 1) ? 0.25f : 0.f);
        u32x4 o;
#pragma unroll
        for (int e = 0; e < 4; ++e) o[e] = pk2(vcos(a0 + (float)(2 * e) * cf_), vcos(a0 + (float)(2 * e + 1) * cf_));
        *(u32x4*)(smem + L_T1 + s * 1024 + l * 16) = o;
        *(u32x4*)(smem + L_T1 + 14336 + s * 1024 + l * 16) = o ^ sg;
      }
    } else if constexpr (id == ePRE) {
      const int l = lane_id(), r8 = l >> 3, ch = l & 7, r32 = l & 31, hh = l >> 5;
      lvo = (unsigned)l * 16;
      wvo = (unsigned)((8 * w + r8) * F_ * 2 + (ch ^ ((r8 & 2) << 1)) * 16 + unit1 * 128);
      wo = lds0 + hh * 2048 + ((l & 15) >> 2) * 128 + (r8 & 1) * 64 + ((l >> 4) & 1) * 32 + (l & 3) * 8;
#pragma unroll
      for (int i = 0; i < 4; ++i) { xo[i] = lds0 + SL + (32 * ws + r32) * 128 + (((2 * i + hh) ^ ((r32 >> 1) & 7)) * 16); pin(xo[i]); }
      pin(lvo); pin(wvo); pin(wo);
    } else if constexpr (id == eA6) {
      sfor<4>([&](auto I) { accA = mfma32(ta[0][I], xa[0][I], accA); });
      wvo += (unsigned)((8 * ws - 32) * F_ * 2); wv1 = wvo + (8 * F_ * 2 - 2048); pin(wvo); pin(wv1);
    } else if constexpr (id == eCS || id == eCS2) {
      constexpr int P = id == eCS2;
      const int l = lane_id();
      const unsigned c = (wo ^ ((ws >> 1) * 64)) + ((ws & 1) * 1024 + P * 2 * SL), sel = (((l >> 2) ^ (l >> 4)) & 1) ? 0u : ONES;
      const u32x4 sl = {sel, sel, sel, sel};
      f32x4 c0 = mfma16(sl, tr_at<0, 512>(c), zero), c1 = mfma16(sl, tr_at<4096, 4608>(c), zero);
      c0 = mfma16(sl, tr_at<8192, 8704>(c), c0); c1 = mfma16(sl, tr_at<12288, 12800>(c), c1);
      st16_sc1(u32x4{__float_as_uint(c0[0] + c1[0]), 0u, (unsigned)tag, (unsigned)(tag >> 32)}, lvo & 511, rw,
               OFF_S + ((P ? unit2 : unit1) * 4 + ws) * 4096 + r0 * 512);
    } else if constexpr (id == eZ) {
      const int l = lane_id(), r32 = l & 31;
      const float sq = lds_ld<float, L_GR - 8 * SL>(lds0 + 8 * SL + ws * 1024 + (r32 + 8 * (r32 & 4)) * 16 + (l & 3) * 4);
      const unsigned su = __float_as_uint(accA[14]);
      const float sm = __uint_as_float(__builtin_amdgcn_permlane32_swap(su, su, false, false)[1]);
      const float mean = sm * (1.0f / D_);
      const unsigned nm = pk2(-mean, -mean);
      lds_st<unsigned, L_NM - 8 * SL>(lds0 + 8 * SL + ws * 256 + l * 4, nm);
      const float rs = __builtin_amdgcn_rcpf(rbf(__builtin_amdgcn_sqrtf(rbf(rbf(fmaxf(sq - sm * mean, 0.f) * (1.0f / D_)) + 1e-5f))));
      accA[0] += __uint_as_float(selm(unit1 == 0 ? 0xffffffffull : 0ull, nm << 16, 0u)) * 448.f;
      const unsigned zv = (unsigned)((l >> 5) * 16384 + r32 * 4), zs = OFF_Z + unit1 * 131072 + (ws >> 1) * 4096 + rb * 256 + (ws & 1) * 128;
      sfor<8>([&](auto E) { constexpr int e = E;
        float p = accA[2 * e] * rs, q = accA[2 * e + 1] * rs; opq(p); opq(q);
        unsigned t = pk2(p, q);
        if constexpr (e == 7) t = selm(0xffffffffull, t, __float_as_uint(rs * rs));
        st4_sc1(t, zv, rw, zs + (4 * (e >> 1) + (e & 1)) * 8192);
      });
    } else if constexpr (id == eFWL) {
      const int l = lane_id();
      const srd_t ra = mk_srd(wr, S_ * DF_ * 2), rc = mk_srd(wi, S_ * DF_ * 2);
      const int fc = 32 * (2 * g + (ii >> 1)) + (l >> 2);
      const unsigned fo[2] = {(unsigned)((l & 3) * DF_ + min(fc, DF_ - 1)) * 2, (unsigned)((l & 3) * DF_ + min(fc + 16, DF_ - 1)) * 2};
      const unsigned fs = (unsigned)(16 * (rb >> 2) + 4 * ws + 64 * (16 * (ii & 1) + 4 * (rb & 3))) * DF_ * 2;
      sfor<8>([&](auto I) { constexpr int e = I >> 2, r = I & 3;
        ld_u16<0>(fl[2 * I], fo[e], ra, fs + r * 64 * DF_ * 2); ld_u16<0>(fl[2 * I + 1], fo[e], rc, fs + r * 64 * DF_ * 2); });
      mk_dv();
    } else if constexpr (id == eWT) {
      wvm<before<R>(eWT) - pos<R>(eFWL)>();
      const int l = lane_id();
      const unsigned tv = (unsigned)((l >> 2) * 8192 + (l & 3) * 16);
      const unsigned ts = OFF_W + (2 * g + (ii >> 1)) * 262144 + ((rb >> 2) * 128 + (ii & 1) * 64 + 16 * (rb & 3) + 4 * ws) * 16;
#pragma unroll
      for (int e = 0; e < 2; ++e) {
        u32x4 o;
#pragma unroll
        for (int r = 0; r < 4; ++r) { pin(fl[8 * e + 2 * r]); pin(fl[8 * e + 2 * r + 1]); o[r] = fl[8 * e + 2 * r] | (fl[8 * e + 2 * r + 1] << 16); }
        st16_sc1(o, tv, rw, ts + e * 131072);
      }
    } else if constexpr (id == eZF) {
      if (w == 4) st_flag1(rw, OFF_C + (FZ0 + unit1 * 16 + rb) * 8, tag);
    } else if constexpr (id == eWF) {
      if (w == 5) st_flag1(rw, OFF_C + (FW0 + (2 * g + (ii >> 1)) * 32 + rb * 2 + (ii & 1)) * 8, tag);
    } else if constexpr (id == eCL || id == eCL2 || id == eCP || id == eCP2) { constexpr int P = id == eCL2 || id == eCP2;
      const unsigned cv = (lvo & 511) + (lvo & 512) * 3, so = OFF_S + ((P ? unit2 : unit1) * 4 + ws) * 4096;
      auto ldc = [&] { sfor<4>([&](auto I) { ld_sc1<I * 512>(cg[I], cv, rw, so); }); };
      if constexpr (id == eCL || id == eCL2) ldc();
      else {
        wvm<pos<R>(id) - pos<R>(P ? eCL2 : eCL)>();
        auto bad = [&] { unsigned d = 0; sfor<4>([&](auto I) { pin(cg[I]); d |= (cg[I][2] ^ (unsigned)tag) | (cg[I][3] ^ (unsigned)(tag >> 32)); });
          return __builtin_amdgcn_ballot_w64(d != 0) != 0; };
        if (bad()) {
#pragma unroll 1
          for (int it = 0; it < SPIN_CAP; ++it) { __builtin_amdgcn_s_sleep(1); ldc(); wait_vm<0>(); if (!bad()) break; }
        }
        const float s = __uint_as_float(selm(0xffffffffull, cg[0][0], 0u)) + __uint_as_float(cg[1][0]) + (__uint_as_float(cg[2][0]) + __uint_as_float(cg[3][0]));
        const float q1 = s - rbf(s), q2 = q1 - rbf(q1);
        lds_st<u32x4, L_P - 8 * SL + P * 4096>(lds0 + 8 * SL + ws * 1024 + lvo, u32x4{pk2(s, q1), pk2(q2, 0.f), 0u, 0u});
      }
    } else if constexpr (id == eFL1) {
      const int l = lane_id(), i = l < 14 ? l >> 1 : 0;
      gvo = (unsigned)(FH0 + (rtb + (R ? 0 : l & 1)) * 28 + 4 * i + ws) * 8;
      ld_flag(fv, gvo, ctl);
    } else if constexpr (id == eFL2) { ld_flag(fv, gvo, ctl);
    } else if constexpr (id == eP1) { wvm<pos<R>(eP1) - pos<R>(eFL1)>(); pin(fv); ST(5) poll(fv, 0xffull, gvo, ctl, tag); ST(6)
    } else if constexpr (id == eP2) { wvm<pos<R>(eP2) - pos<R>(eFL2)>(); pin(fv); ST(8) poll(fv, 0x3f00ull, gvo, ctl, tag); ST(9)
    } else if constexpr (id == eRES) { res(0, ws);
    }
  };
  auto run = [&](auto Rc, auto Mc) { constexpr int R = Rc, mk = Mc, i0 = mk < 0 ? 0 : sidx<R>(mk) + 1;
    sfor<seglen<R>(i0)>([&](auto K) { ev(Rc, ic<seq<R>(i0 + K).id>{}); SB(); });
  };
  auto mk_tag = [&] { pin(ep); pin(pf);
    tag = (((u64)__builtin_amdgcn_readfirstlane((unsigned)(ep >> 32)) << 32) | (unsigned)__builtin_amdgcn_readfirstlane((unsigned)ep)) + 1; };
  auto tail_h = [&](auto Rc) { constexpr int R = Rc;
    run(Rc, ic<mP + 13>{});
    sfor<28>([&](auto Sc) { constexpr int s = Sc;
      wvm<pos<R>(mK + s) - pos<R>(eH + s)>();
      if constexpr (s == 0) { ST(7) }
      if constexpr (s == 16) { ST(10) }
      sfor<R ? 2 : 4>([&](auto I) { constexpr int a = I & 1, c = I >> 1;
        if constexpr (!a) pin(hf[s][c]);
        ah[c][a] = mfma32(wdf[s >> 2][2 * (s & 3) + a], hf[s][c], s == 0 ? zero16 : ah[c][a]); pin(ah[c][a]); });
      SB();
      run(Rc, ic<mK + s>{});
    });
    ST(11)
    const int l = lane_id();
    put_part(lds0 + (5 + ws) * SL, l, ah[0][0], ah[0][1]);
    if constexpr (!R) { put_part(lds0 + (5 + ws) * SL + 8192, l, ah[1][0], ah[1][1]); pin(xres); pin(r2u); }
  };
  auto tail_c = [&](auto Rc, auto&& mid) { constexpr int R = Rc, NK = R ? 7 : 14;
    const int l = lane_id(), kq = R ? ws : ws >> 1;
    u32x4 t2[NK][2], yf[NK];
    const float c0 = (float)((2 * kq + (l >> 5)) * (4 * NK));
    float dd[2], a0[2];
#pragma unroll
    for (int t = 0; t < 2; ++t) { dd[t] = (float)(64 * dt + 32 * t + (l & 31)) * (1.0f / 448); a0[t] = __builtin_amdgcn_fractf(c0 * dd[t]); }
    sfor<NK>([&](auto Kc) { constexpr int k = Kc;
#pragma unroll
      for (int t = 0; t < 2; ++t) {
#pragma unroll
        for (int e = 0; e < 4; ++e) { const float a = (float)(4 * k + e) * dd[t] + a0[t]; t2[k][t][e] = pk2(vcos(a), vsin(a)); }
        if constexpr (k == 0) t2[0][t][0] = selm(kq == 0 ? 0xffffffffull : 0ull, (l & 1) ? 0xbf003f00u : 0x3f003f00u, t2[0][t][0]);
        pin(t2[k][t]);
      }
      SB();
      if constexpr (k == 3) { mid(); SB(); }
      if constexpr (k == NK - 3) { gvo = (unsigned)(FY0 + 16 * (l < 2 * NK ? 2 * NK * kq + l : 56)) * 8; ld_flag(fv, gvo, ctl); SB(); }
    });
    wait_vm<0>(); pin(fv);
    ST(8)
    poll(fv, ~0ull, gvo, ctl, tag);
    ST(9)
    const unsigned yo = OFF_Y + (rtb + (R ? 0 : ws & 1)) * 512, yv = (unsigned)((l >> 5) * (NK * 32768) + (l & 31) * 16);
    unsigned yn;
    ld_u32_sc1(yn, yv & 511, rw, yo + 56 * 32768);
    sfor<NK>([&](auto I) { ld_sc1<0>(yf[I], yv, rw, yo + (2 * NK * kq + I) * 32768); });
    res(R, 14);
    f32x16 ac[2];
    sfor<NK>([&](auto I) { constexpr int i = I;
      wait_vm<NK + 1 - i>(); pin(yf[i]);
      if constexpr (i == 0) { ST(10) pin(yn); yf[0][0] = selm_n(kq == 0 ? 0xffffffffull : 0ull, (yf[0][0] & 0xffffu) | (yn << 16), yf[0][0]); }
      ac[0] = mfma32(t2[i][0], yf[i], i ? ac[0] : zero16); ac[1] = mfma32(t2[i][1], yf[i], i ? ac[1] : zero16);
      SB();
    });
    ST(11)
    put_part(lds0 + (R ? 3 * SL : 0) + ws * 8192, l, ac[0], ac[1]);
    wait_vm<0>(); pin(xres); pin(r2u);
  };

  auto zw_flags = [&] { const int c0 = gi < 56 ? 4 * gi : 221;
    gvo = (selm(0xffffull, FZ0 + c0 / 15 * 16, selm(0xffffffffull, FZ0 - 16 + (c0 + 3) / 15 * 16, FW0 - 32 + (gi < 56 ? gi >> 3 : 7) * 32)) + lane_id()) * 8; };
  unsigned hso;
  auto hflag = [&] { wait_vm<0>(); st_flag1(rw, OFF_C + FH0 * 8 + (hso >> 9), tag); };
  if (wn == 0) {
    run(ic<0>{}, ic<-1>{});
    u32x4 xf[28], wf[RA], nb, eo;
    f32x16 acc[2][4], accG = zero16;
    auto gram = [&](auto I) { accG = mfma32(xf[I], xf[I], accG); };
    sfor<7>([&](auto J) { constexpr int j = J;
      vbar<pos<0>(mX + j) - pos<0>(eX + j)>();
      if constexpr (j == 0) { ST(1) }
#pragma unroll
      for (int i = 0; i < 4; ++i) {
        if constexpr (j == 3) { xo[i] += 3 * SL; pin(xo[i]); }
        xf[4 * j + i] = l128_at<(j < 3 ? j : j - 3) * SL>(xo[i]);
      }
      SB();
      run(ic<0>{}, ic<mX + j>{});
      if constexpr (j == 3) mk_tag();
      if constexpr (j > 0) sfor<4>([&](auto I) { gram(ic<4 * j - 4 + I>{}); });
      SB();
    });
    hso = ((4 * rb + ws) * 28 + unit1) * 4096;
    auto epi = [&](auto Pc, auto Qc, auto Ec) { constexpr int P = Pc, Q = Qc, e = Ec;
      float h0 = fmaxf(acc[P][2 * Q][2 * e], 0.f), h1 = fmaxf(acc[P][2 * Q][2 * e + 1], 0.f);
      h0 *= acc[P][2 * Q + 1][2 * e]; h1 *= acc[P][2 * Q + 1][2 * e + 1];
      opq(h0); opq(h1);
      unsigned t = pk2(h0, h1); pin(t);
      eo[e & 3] = t;
      if constexpr ((e & 3) == 3) st16_sc1<Q * 2048 + (e >> 2) * 1024>(eo, lvo, rw, hso);
    };
    unsigned a0, a1;
    auto step = [&](auto Tc, auto Endc) { constexpr int T = Tc, M = T - RA;
      if constexpr (T < Endc && T == t_start(t_per(T))) { constexpr int n = t_per(T);
        if constexpr (n == 0) vbar<6>();
        else if constexpr (n < 4) asm volatile("s_waitcnt vmcnt(%0)\n\ts_barrier" ::"n"(6 - 2 * n) : "memory");
        else asm volatile("s_barrier" ::: "memory");
        if constexpr (n == 0) { ST(2) }
        if constexpr (n == 7) { ST(3) }
        pin(wo); a0 = wo + (n % 5) * SL; a1 = a0 ^ 64; pin(a0); pin(a1);
      }
      if constexpr (M >= 0) { constexpr int P = M / 116, n = t_per(M), i = t_idx(M), q = i >= 16 ? i - 16 : 2 * (i >> 3) + (i & 1);
        if constexpr (i >= 16) acc[P][q] = mfma32(wf[M % RA], nb, acc[P][q]);
        else acc[P][q] = mfma32(wf[M % RA], xf[4 * (n % 7) + ((i >> 1) & 3)], (n % 7 == 0 && (i & 6) == 0) ? zero16 : acc[P][q]);
        pin(acc[P][q]);
      } else if constexpr (T < 4) gram(ic<24 + T>{});
      SB();
      if constexpr (T < Endc) { constexpr int i = t_idx(T);
        if constexpr (i >= 16) wf[T % RA] = l128_at<L_P - 8 * SL + (T / 116) * 4096 + (i - 16) * 1024>(lvo + (lds0 + 8 * SL));
        else { constexpr int o = ((i >> 1) & 3) * 4096 + (i & 1) * 1024; wf[T % RA] = tr_at<o, o + 512>(i >= 8 ? a1 : a0); }
      }
      SB();
      if constexpr (T == 7) gram_dump(lds0 + L_GR + ws * 1024 + lvo, accG);
      if constexpr (T == 9) asm volatile("s_waitcnt lgkmcnt(4)\n\ts_barrier" ::: "memory");
      if constexpr (T == 40) { const unsigned nm = lds_ld<unsigned, L_NM - 8 * SL>(lds0 + 8 * SL + ws * 256 + (lvo >> 2)); nb = u32x4{nm, nm, nm, nm}; }
      if constexpr (T == 44) pin(nb);
      if constexpr (M >= 0 && M % 116 >= 108) epi(ic<M / 116>{}, ic<0>{}, ic<M % 116 - 108>{});
      if constexpr (M >= 116 && M < 124) epi(ic<0>{}, ic<1>{}, ic<M - 116>{});
      if constexpr (M == 156) { hflag(); hso += (unit2 - unit1) * 4096; }
      SB();
    };
    sfor<116>([&](auto I) { step(I, ic<232>{}); });
    if (!bcu) {
      sfor<116 + RA>([&](auto I) { step(ic<116 + I>{}, ic<232>{}); });
      sfor<8>([&](auto I) { epi(ic<1>{}, ic<1>{}, I); });
      ST(4)
      tail_c(ic<0>{}, [&] { ST(5) hflag(); ST(6) });
    } else {
      sfor<RA>([&](auto I) { step(ic<116 + I>{}, ic<116>{}); });
      sfor<8>([&](auto I) { epi(ic<0>{}, ic<1>{}, I); });
      ST(3)
    }
  } else {
    __builtin_amdgcn_s_setprio(2);
    run(ic<0>{}, ic<-1>{});
    accA = zero16;
    sfor<7>([&](auto J) { constexpr int j = J;
      vbar<pos<0>(mX + j) - pos<0>(eX + j)>();
      if constexpr (j == 0) { ST(1) }
      const unsigned to = lvo + (lds0 + L_T1 + kof(j) * 4096);
      sfor<4>([&](auto I) { constexpr int i = I;
        if constexpr (j == 3) { xo[i] += 3 * SL; pin(xo[i]); }
        xa[j & 1][i] = l128_at<(j < 3 ? j : j - 3) * SL>(xo[i]); ta[j & 1][i] = l128_at<i * 1024>(to);
      });
      SB();
      run(ic<0>{}, ic<mX + j>{});
      if constexpr (j == 3) mk_tag();
      if constexpr (j > 0) sfor<4>([&](auto I) { accA = mfma32(ta[~j & 1][I], xa[~j & 1][I], accA); });
      SB();
    });
    auto period = [&](auto Rc, auto Nc) { constexpr int R = Rc, n = Nc;
      vbar<pos<R>(mP + n) - need<R>(n)>();
      if constexpr (n == 0) { ST(2) }
      if constexpr (n == 7) { ST(3) }
      if constexpr (n < 13) run(Rc, ic<mP + n>{});
      if constexpr (n == 0) { asm volatile("s_barrier" ::: "memory"); run(Rc, ic<mS>{}); }
    };
    sfor<4>([&](auto I) { period(ic<0>{}, I); });
    if (!bcu) {
      sfor<10>([&](auto I) { period(ic<0>{}, ic<I + 4>{}); });
      ST(4)
      tail_h(ic<0>{});
    } else {
      sfor<3>([&](auto I) { period(ic<1>{}, ic<I + 4>{}); });
      zw_flags();
      ld_flag(fv, gvo, ctl); wait_vm<0>(); pin(fv);
      ST(3)
      poll(fv, 0xffffffffull, gvo, ctl, tag);
      ST(4)
      const int c = gi < 56 ? 4 * gi + ws : 224;
      const unsigned zo = OFF_Z + (c + c / 15) * 8192;
      dma4_sc1<3 * SL>(lds0 + ws * 8192, lvo, rw, zo); dma4_sc1<3 * SL + 4096>(lds0 + ws * 8192, lvo, rw, zo + 4096);
    }
  }
  if (bcu) {
    const int l = lane_id(), v = l & 15, q = l >> 4, vp = 4 * (v & 3) + (v >> 2);
    __builtin_amdgcn_s_setprio(0);
    char* fs = smem + 3 * SL;
    lvo = (unsigned)l * 16;
    zw_flags(); ld_flag(fv, gvo, ctl);
    u32x4 wv[2][2], M1[4], M2[4], C32[2], S32[2], N32[2];
    const float k1f = (float)(16 * ws + v) * (1.0f / 64);
#pragma unroll
    for (int ks = 0; ks < 4; ++ks) {
#pragma unroll
      for (int e = 0; e < 4; ++e) {
        const float a = (float)(16 * ks + 4 * q + e) * k1f, c = vcos(a), s = vsin(a);
        M1[ks][e] = pk2(c, s); M2[ks][e] = pk2(-s, c);
      }
      pin(M1[ks]); pin(M2[ks]);
    }
#pragma unroll
    for (int t = 0; t < 2; ++t) {
      const float nf = (float)(16 * t + v) * (1.0f / 32);
#pragma unroll
      for (int e = 0; e < 4; ++e) {
        const float a0 = (float)(16 * (e >> 1) + 4 * q + 2 * (e & 1)) * nf, a1 = a0 + nf;
        C32[t][e] = pk2(vcos(a0), vcos(a1)); S32[t][e] = pk2(vsin(a0), vsin(a1)); N32[t][e] = S32[t][e] ^ 0x80008000u;
      }
      pin(C32[t]); pin(S32[t]); pin(N32[t]);
    }
    float t1c[2][4], t1s[2][4], u2c[2][4], u2s[2][4];
    constexpr float scale = 2.0f / (448.0f * 2048.0f);
#pragma unroll
    for (int t = 0; t < 2; ++t)
#pragma unroll
      for (int r = 0; r < 4; ++r) {
        const float a1 = (float)(16 * t + 4 * q + r) * (k1f * (1.0f / 32));
        t1c[t][r] = vcos(a1); t1s[t][r] = vsin(a1);
        const float a2 = (float)((16 * t + v) * (16 * ws + 4 * q + r)) * (1.0f / 2048);
        u2c[t][r] = vcos(a2) * scale; u2s[t][r] = vsin(a2) * scale;
        pin(t1c[t][r]); pin(t1s[t][r]); pin(u2c[t][r]); pin(u2s[t][r]);
      }
    SB();
    wait_vm<0>(); pin(fv);
    if (wn == 0) hflag();
    else poll(fv, ~0ull, gvo, ctl, tag);
    bar();
    ST(12)
#pragma unroll
    for (int k = 0; k < 2; ++k) {
      const unsigned wp = OFF_W + (gi < 56 ? 4 * gi + 2 * wn + k : 224) * 8192 + ws * 2048;
      ld_sc1<0>(wv[k][0], lvo, rw, wp); ld_sc1<1024>(wv[k][1], lvo, rw, wp);
    }
    f32x4 pr[2][2], pi[2][2];
#pragma unroll
    for (int k = 0; k < 2; ++k)
#pragma unroll
      for (int ks = 0; ks < 4; ++ks)
#pragma unroll
        for (int t = 0; t < 2; ++t) {
          const u32x4 z = lds128(fs + (2 * wn + k) * 8192 + t * 4096 + ks * 1024 + l * 16);
          pr[k][t] = mfma16(z, M1[ks], ks == 0 ? zero : pr[k][t]); pi[k][t] = mfma16(z, M2[ks], ks == 0 ? zero : pi[k][t]);
        }
    bar();
#pragma unroll
    for (int k = 0; k < 2; ++k) {
      u32x4 fr, fi;
      {
        float a[8], bq[8];
#pragma unroll
        for (int t = 0; t < 2; ++t)
#pragma unroll
          for (int r = 0; r < 4; ++r) {
            a[4 * t + r] = pr[k][t][r] * t1c[t][r] + pi[k][t][r] * t1s[t][r];
            bq[4 * t + r] = pi[k][t][r] * t1c[t][r] - pr[k][t][r] * t1s[t][r];
          }
#pragma unroll
        for (int e = 0; e < 4; ++e) { fr[e] = pk2(a[2 * e], a[2 * e + 1]); fi[e] = pk2(bq[2 * e], bq[2 * e + 1]); }
      }
      f32x4 qr[2], qi[2];
#pragma unroll
      for (int t = 0; t < 2; ++t) {
        qr[t] = mfma16(C32[t], fr, zero); qi[t] = mfma16(C32[t], fi, zero);
        qr[t] = mfma16(S32[t], fi, qr[t]); qi[t] = mfma16(N32[t], fr, qi[t]);
      }
      if (k == 0) { wait_vm<0>(); pin(wv[0][0]); pin(wv[0][1]); pin(wv[1][0]); pin(wv[1][1]); }
      {
        float a[8], bq[8];
#pragma unroll
        for (int t = 0; t < 2; ++t)
#pragma unroll
          for (int r = 0; r < 4; ++r) {
            const float wre = bflo(wv[k][t][r]), wim = bfhi(wv[k][t][r]);
            a[4 * t + r] = qr[t][r] * wre - qi[t][r] * wim;
            bq[4 * t + r] = qr[t][r] * wim + qi[t][r] * wre;
          }
#pragma unroll
        for (int e = 0; e < 4; ++e) { fr[e] = pk2(a[2 * e], a[2 * e + 1]); fi[e] = pk2(bq[2 * e], bq[2 * e + 1]); }
      }
#pragma unroll
      for (int t = 0; t < 2; ++t) {
        f32x4 rr = mfma16(fr, C32[t], zero), ri = mfma16(fi, C32[t], zero);
        rr = mfma16(fi, N32[t], rr); ri = mfma16(fr, S32[t], ri);
        u32x4 o;
#pragma unroll
        for (int r = 0; r < 4; ++r) o[r] = pk2(rr[r] * u2c[t][r] - ri[r] * u2s[t][r], -(rr[r] * u2s[t][r]) - ri[r] * u2c[t][r]);
        *(u32x4*)(fs + (2 * wn + k) * 8192 + (16 * t + v) * 256 + (((4 * ws + q) ^ vp) * 16)) = o;
      }
    }
    bar();
    f32x4 yr[4], yi[4];
#pragma unroll
    for (int ks = 0; ks < 4; ++ks)
#pragma unroll
      for (int k = 0; k < 4; ++k) {
        const u32x4 rf = lds128(fs + k * 8192 + (16 * wn + vp) * 256 + (((4 * ks + q) ^ v) * 16));
        yr[k] = mfma16(rf, M1[ks], ks == 0 ? zero : yr[k]); yi[k] = mfma16(rf, M2[ks], ks == 0 ? zero : yi[k]);
      }
    const unsigned yb = OFF_Y + gi * 32768 + ws * 8192 + wn * 1024;
    sfor<4>([&](auto Rc) { constexpr int R = Rc;
      st16_sc1<R * 256>(u32x4{pk2(yr[0][R], yi[0][R]), pk2(yr[1][R], yi[1][R]), pk2(yr[2][R], yi[2][R]), pk2(yr[3][R], yi[3][R])},
                        (unsigned)((v >> 2) * 2048 + q * 64 + (v & 3) * 16), rw, yb);
    });
    ST(13)
    vbar<0>();
    if (w == 0) st_flag1(rw, OFF_C + (FY0 + 16 * gi) * 8, tag);
    if (wn == 0) tail_c(ic<1>{}, [] {}); else { mk_dv(); tail_h(ic<1>{}); }
  }
  bar();
  if (!bcu) { ST(12) }
  if (!bcu || wn == 0) {
    const int l = lane_id(), ch = l & 7;
    const unsigned q = lds0 + ch * 1024 + (((8 * ws + (l >> 3)) ^ (2 * ch)) * 16), qh = q + 5 * SL + wn * 8192, qc = q + (bcu ? 3 * SL : wn * 8192);
    f32x4 a0 = lds_ld<f32x4, 0>(qh), a1 = lds_ld<f32x4, 512>(qh), c0 = lds_ld<f32x4, 0>(qc), c1 = lds_ld<f32x4, 512>(qc);
    sfor<3>([&](auto I) { a0 += lds_ld<f32x4, (I + 1) * SL>(qh); a1 += lds_ld<f32x4, (I + 1) * SL + 512>(qh); });
    c0 += lds_ld<f32x4, SL>(qc); c1 += lds_ld<f32x4, SL + 512>(qc);
    if (bcu) { c0 += lds_ld<f32x4, 8192>(qc) + lds_ld<f32x4, SL + 8192>(qc); c1 += lds_ld<f32x4, 8704>(qc) + lds_ld<f32x4, SL + 8704>(qc); }
    const float r2 = __uint_as_float(r2u);
    u32x4 o;
    o[0] = pk2(a0[0] * r2 + (c0[0] + bflo(xres[0])), a0[1] * r2 + (c0[1] + bfhi(xres[0])));
    o[1] = pk2(a0[2] * r2 + (c0[2] + bflo(xres[1])), a0[3] * r2 + (c0[3] + bfhi(xres[1])));
    o[2] = pk2(a1[0] * r2 + (c1[0] + bflo(xres[2])), a1[1] * r2 + (c1[1] + bfhi(xres[2])));
    o[3] = pk2(a1[2] * r2 + (c1[2] + bflo(xres[3])), a1[3] * r2 + (c1[3] + bfhi(xres[3])));
    *(u32x4*)((char*)out + ovo) = o;
  }
  asm volatile(".Lpf_end:");
  if (b == 0 && w == 0) st_flag1(rw, OFF_C, tag);
  STAMP_END(bcu ? 1 : 0)
}

extern "C" void kernel_launch(void* const* d_in, const int* in_sizes, int n_in, void* d_out, int out_size, void* d_ws, size_t ws_size,
                              hipStream_t stream) {
  (void)hipFuncSetAttribute((const void*)fwd, hipFuncAttributeMaxDynamicSharedMemorySize, 163840);
  fwd<<<256, 512, 163840, stream>>>((const uint16_t*)d_in[0], (const uint16_t*)d_in[1], (const uint16_t*)d_in[2], (const uint16_t*)d_in[4],
                                    (const uint16_t*)d_in[5], (const uint16_t*)d_in[6], (char*)d_ws, (uint16_t*)d_out STAMP_PASS);
}
```

```cpp
#include <hip/hip_runtime.h>
#include <hip/hip_bf16.h>
#include <stdint.h>
#include <utility>

typedef __attribute__((ext_vector_type(4))) short s16x4;
typedef __attribute__((ext_vector_type(8))) __bf16 bf16x8;
typedef __attribute__((ext_vector_type(2))) __bf16 bf16x2;
typedef __attribute__((ext_vector_type(4))) float f32x4;
typedef __attribute__((ext_vector_type(16))) float f32x16;
typedef __attribute__((ext_vector_type(2))) float f32x2;
typedef __attribute__((ext_vector_type(4))) unsigned u32x4;
typedef __attribute__((ext_vector_type(2))) unsigned u32x2;
typedef __attribute__((address_space(3))) s16x4* lds_s16x4_p;
typedef unsigned long long u64;
typedef __attribute__((address_space(1))) u64 gu64;
typedef __attribute__((address_space(3))) unsigned lu32;
#define DI __device__ __forceinline__
#define SB() __builtin_amdgcn_sched_barrier(0)
template <int N> using ic = std::integral_constant<int, N>;

constexpr int S_ = 2048, D_ = 448, F_ = 1792, DF_ = 225;

template <class Fn, int... I> DI void sfor_impl(Fn&& f, std::integer_sequence<int, I...>) { (f(ic<I>{}), ...); }
template <int N, class Fn> DI void sfor(Fn&& f) { sfor_impl(f, std::make_integer_sequence<int, N>{}); }

DI f32x4 mfma16(u32x4 a, u32x4 b, f32x4 c) {
  return __builtin_amdgcn_mfma_f32_16x16x32_bf16(__builtin_bit_cast(bf16x8, a), __builtin_bit_cast(bf16x8, b), c, 0, 0, 0);
}
DI f32x16 mfma32(u32x4 a, u32x4 b, f32x16 c) {
  return __builtin_amdgcn_mfma_f32_32x32x16_bf16(__builtin_bit_cast(bf16x8, a), __builtin_bit_cast(bf16x8, b), c, 0, 0, 0);
}
DI unsigned pk2(float lo, float hi) { bf16x2 p; p[0] = (__bf16)lo; p[1] = (__bf16)hi; return __builtin_bit_cast(unsigned, p); }
DI float bflo(unsigned u) { return __builtin_bit_cast(float, u << 16); }
DI float bfhi(unsigned u) { return __builtin_bit_cast(float, u & 0xffff0000u); }
DI float rbf(float f) { return (float)(__bf16)f; }
typedef __attribute__((address_space(3))) const char* lds_cp;
typedef __attribute__((address_space(3))) const u32x4* lds_u32x4_p;
template <int O0, int O1> DI u32x4 tr_at(unsigned a) {
  s16x4 x = __builtin_amdgcn_ds_read_tr16_b64_v4i16((lds_s16x4_p)((lds_cp)(uintptr_t)a + O0));
  s16x4 y = __builtin_amdgcn_ds_read_tr16_b64_v4i16((lds_s16x4_p)((lds_cp)(uintptr_t)a + O1));
  return __builtin_bit_cast(u32x4, __builtin_shufflevector(x, y, 0, 1, 2, 3, 4, 5, 6, 7));
}
template <int O> DI u32x4 l128_at(unsigned a) { return *(lds_u32x4_p)((lds_cp)(uintptr_t)a + O); }
DI unsigned lds_addr(const void* p) { return (unsigned)(uintptr_t)(__attribute__((address_space(3))) const char*)p; }
template <int N> DI void wait_vm() { asm volatile("s_waitcnt vmcnt(%0)" ::"n"(N) : "memory"); }
DI void bar() { asm volatile("s_waitcnt lgkmcnt(0)" ::: "memory"); __builtin_amdgcn_s_barrier(); asm volatile("" ::: "memory"); }
DI u32x4 lds128(const char* p) { return *(const u32x4*)p; }
DI int lane_id() { int r; asm volatile("v_mbcnt_lo_u32_b32 %0, -1, 0\n\tv_mbcnt_hi_u32_b32 %0, -1, %0" : "=v"(r)); return r; }
DI float vcos(float a) { return __builtin_amdgcn_cosf(a); }
DI float vsin(float a) { return __builtin_amdgcn_sinf(a); }

#define STINS "s_memrealtime"
#define STAMP_BEGIN
#define ST(i)
#define STAMP_END(kid)
#define STAMP_ARG
#define STAMP_PASS

constexpr unsigned ONES = 0x3f803f80u;
constexpr int SL = 16384;
constexpr int L_T1 = 8 * SL, L_GR = 9 * SL + 12288, L_P = 9 * SL, L_NM = 9 * SL + 8192;
constexpr unsigned OFF_Z = 8u << 20, OFF_W = 10u << 20, OFF_Y = 12u << 20, OFF_C = 14u << 20, OFF_S = 15u << 20;
constexpr int FZ0 = 512, FW0 = 1024, FY0 = 2048, FH0 = 4096;
constexpr int SPIN_CAP = 200000;
constexpr int RA = 6;

enum : int { eX = 0, eW = 8, eD = 24, eDR = 40, eH = 48, eEP = 80, ePF, eT1, ePRE, eA6, eCS, eCS2, eZ, eFWL, eWT, eZF, eWF, eCL, eCL2, eCP, eCP2, eFL1, eP1, eFL2, eP2, eRES,
             mX = 200, mS = 208, mP = 210, mK = 230, mEnd = 500 };
struct Ev { int id, n; };
#define PRO {eX + 0, 2}, {eX + 1, 2}, {eX + 2, 2}, {eEP, 1}, {ePF, 1}, {eT1, 0}, {eX + 3, 2}, {ePRE, 0}, \
  {mX + 0, 0}, {eX + 4, 2}, {mX + 1, 0}, {eX + 5, 2}, {mX + 2, 0}, {eX + 6, 2}, {mX + 3, 0}, {eW + 0, 2}, {mX + 4, 0}, {eW + 1, 2}, \
  {mX + 5, 0}, {eW + 2, 2}, {mX + 6, 0}, {eW + 3, 2}, \
  {mP + 0, 0}, {eA6, 0}, {eCS, 1}, {eW + 4, 4}, {mS, 0}, {eZ, 8}, \
  {mP + 1, 0}, {eFWL, 16}, {eD + 0, 4}, {mP + 2, 0}, {eW + 5, 4}, {eD + 1, 4}, {mP + 3, 0}, {eZF, 0}, {eW + 6, 4}, {eWT, 2}, {eD + 2, 4}
constexpr Ev SEQ_R[] = {PRO,
  {mP + 4, 0}, {eW + 7, 4}, {eCL, 4}, {eD + 3, 4}, {eDR + 0, 0},
  {mP + 5, 0}, {eWF, 0}, {eW + 8, 4}, {eD + 4, 4}, {eCP, 0},
  {mP + 6, 0}, {eW + 9, 4}, {eD + 5, 4}, {eDR + 1, 0},
  {mP + 7, 0}, {eCS2, 1}, {eW + 10, 4}, {eD + 6, 4},
  {mP + 8, 0}, {eW + 11, 4}, {eD + 7, 4}, {eDR + 2, 0},
  {mP + 9, 0}, {eW + 12, 4}, {eD + 8, 4},
  {mP + 10, 0}, {eW + 13, 4}, {eD + 9, 4}, {eDR + 3, 0},
  {mP + 11, 0}, {eCL2, 4}, {eD + 10, 4},
  {mP + 12, 0}, {eD + 11, 4}, {eCP2, 0}, {eFL1, 1},
  {mP + 13, 0}, {eP1, 0}, {eH + 0, 2}, {eH + 1, 2}, {eH + 2, 2}, {eH + 3, 2}, {eH + 4, 2},
  {mK + 0, 0}, {eH + 5, 2}, {eH + 6, 2}, {mK + 1, 0}, {eH + 7, 2}, {eH + 8, 2}, {mK + 2, 0}, {eH + 9, 2}, {eH + 10, 2}, {mK + 3, 0}, {eH + 11, 2}, {eH + 12, 2},
  {mK + 4, 0}, {eH + 13, 2}, {eH + 14, 2}, {mK + 5, 0}, {eH + 15, 2}, {mK + 6, 0}, {mK + 7, 0}, {eDR + 4, 0},
  {mK + 8, 0}, {eD + 12, 4}, {mK + 9, 0}, {eD + 13, 4}, {mK + 10, 0}, {eRES, 2}, {mK + 11, 0}, {eDR + 5, 0},
  {mK + 12, 0}, {mK + 13, 0}, {eFL2, 1}, {mK + 14, 0}, {mK + 15, 0}, {eP2, 0},
  {eH + 16, 2}, {eH + 17, 2}, {eH + 18, 2}, {eH + 19, 2}, {eH + 20, 2}, {eH + 21, 2}, {eH + 22, 2}, {eH + 23, 2}, {eH + 24, 2}, {eH + 25, 2}, {eH + 26, 2}, {eH + 27, 2},
  {mK + 16, 0}, {mK + 17, 0}, {mK + 18, 0}, {mK + 19, 0}, {eDR + 6, 0}, {mK + 20, 0}, {mK + 21, 0}, {mK + 22, 0}, {mK + 23, 0},
  {mK + 24, 0}, {mK + 25, 0}, {mK + 26, 0}, {mK + 27, 0}, {mEnd, 0}};
constexpr Ev SEQ_B[] = {PRO,
  {mP + 4, 0}, {eCL, 4}, {eD + 3, 4}, {mP + 5, 0}, {eWF, 0}, {eCP, 0}, {mP + 6, 0},
  {mP + 13, 0}, {eFL1, 1}, {eDR + 0, 0}, {eDR + 1, 0}, {eD + 4, 4}, {eD + 5, 4}, {eP1, 0},
  {eH + 0, 1}, {eH + 1, 1}, {eH + 2, 1}, {eH + 3, 1}, {eH + 4, 1}, {eH + 5, 1}, {eH + 6, 1}, {eH + 7, 1}, {eD + 6, 4}, {eD + 7, 4},
  {eH + 8, 1}, {eH + 9, 1}, {eH + 10, 1}, {eH + 11, 1}, {eH + 12, 1}, {eH + 13, 1}, {eH + 14, 1}, {eH + 15, 1}, {eDR + 2, 0}, {eD + 8, 4}, {eD + 9, 4},
  {mK + 0, 0}, {mK + 1, 0}, {mK + 2, 0}, {mK + 3, 0}, {mK + 4, 0}, {mK + 5, 0}, {mK + 6, 0}, {mK + 7, 0}, {eDR + 3, 0}, {eD + 10, 4}, {eD + 11, 4}, {eFL2, 1},
  {mK + 8, 0}, {mK + 9, 0}, {mK + 10, 0}, {mK + 11, 0}, {mK + 12, 0}, {mK + 13, 0}, {mK + 14, 0}, {mK + 15, 0}, {eDR + 4, 0}, {eD + 12, 4}, {eD + 13, 4}, {eP2, 0},
  {eH + 16, 1}, {eH + 17, 1}, {eH + 18, 1}, {eH + 19, 1}, {eH + 20, 1}, {eH + 21, 1}, {eH + 22, 1}, {eH + 23, 1}, {eH + 24, 1}, {eH + 25, 1}, {eH + 26, 1}, {eH + 27, 1},
  {eDR + 5, 0}, {eDR + 6, 0},
  {mK + 16, 0}, {mK + 17, 0}, {mK + 18, 0}, {mK + 19, 0}, {mK + 20, 0}, {mK + 21, 0}, {mK + 22, 0}, {mK + 23, 0},
  {mK + 24, 0}, {mK + 25, 0}, {mK + 26, 0}, {mK + 27, 0}, {mEnd, 0}};
template <int R> constexpr Ev seq(int i) { return R ? SEQ_B[i] : SEQ_R[i]; }
template <int R> constexpr int sidx(int id) { int i = 0; while (seq<R>(i).id != id && seq<R>(i).id != mEnd) ++i; return i; }
template <int R> constexpr bool has(int id) { return seq<R>(sidx<R>(id)).id == id; }
template <int R> constexpr int pos(int id) { int p = has<R>(id) ? 0 : 1000; for (int i = 0; i <= sidx<R>(id); ++i) p += seq<R>(i).n; return p; }
template <int R> constexpr int before(int id) { return pos<R>(id) - seq<R>(sidx<R>(id)).n; }
template <int R> constexpr int seglen(int i0) { int n = 0; while (seq<R>(i0 + n).id < mX) ++n; return n; }
template <int R> constexpr bool inper(int id, int n) { return sidx<R>(id) > sidx<R>(mP + n) && sidx<R>(id) < sidx<R>(mP + n + 1); }
constexpr int cmax(int a, int b) { return a > b ? a : b; }
template <int R> constexpr int need(int n) {
  int p = pos<R>(eW + n);
  if (inper<R>(eZF, n)) p = cmax(p, pos<R>(eZ));
  if (inper<R>(eWF, n)) p = cmax(p, pos<R>(eWT));
  return p;
}
constexpr int t_start(int n) { return 116 * (n / 7) + 16 * (n % 7); }
constexpr int t_per(int T) { return 7 * (T / 116) + (T % 116 >= 96 ? 6 : T % 116 / 16); }
constexpr int t_idx(int T) { const int u = T % 116; return u < 96 ? u % 16 : u < 100 ? u - 80 : u - 100; }

typedef __amdgpu_buffer_rsrc_t srd_t;
DI srd_t mk_srd(const void* p, int bytes) { return __builtin_amdgcn_make_buffer_rsrc((void*)p, 0, bytes, 0x00020000); }
template <int L> DI void dma2(unsigned lw, unsigned v0, srd_t r0, unsigned v1, srd_t r1, unsigned so) {
  asm volatile("s_add_u32 m0, %0, %1\n\ts_nop 0\n\tbuffer_load_dwordx4 %2, %3, %6 offen lds\n\tbuffer_load_dwordx4 %4, %5, %6 offen offset:1024 lds"
               ::"s"(lw), "n"(L), "v"(v0), "s"(r0), "v"(v1), "s"(r1), "s"(so) : "memory", "scc");
}
template <int L> DI void dma4(unsigned lw, unsigned v0, unsigned v1, unsigned v2, unsigned v3, srd_t r0, srd_t r1, unsigned so) {
  asm volatile("s_add_u32 m0, %0, %1\n\ts_nop 0\n\tbuffer_load_dwordx4 %2, %6, %8 offen lds\n\tbuffer_load_dwordx4 %3, %7, %8 offen offset:1024 lds\n\t"
               "buffer_load_dwordx4 %4, %6, %8 offen offset:2048 lds\n\tbuffer_load_dwordx4 %5, %7, %8 offen offset:3072 lds"
               ::"s"(lw), "n"(L), "v"(v0), "v"(v1), "v"(v2), "v"(v3), "s"(r0), "s"(r1), "s"(so) : "memory", "scc");
}
template <int L> DI void dma4_sc1(unsigned lw, unsigned v, srd_t r, unsigned so) {
  asm volatile("s_add_u32 m0, %0, %1\n\ts_nop 0\n\tbuffer_load_dwordx4 %2, %3, %4 offen sc1 lds\n\tbuffer_load_dwordx4 %2, %3, %4 offen offset:1024 sc1 lds\n\t"
               "buffer_load_dwordx4 %2, %3, %4 offen offset:2048 sc1 lds\n\tbuffer_load_dwordx4 %2, %3, %4 offen offset:3072 sc1 lds"
               ::"s"(lw), "n"(L), "v"(v), "s"(r), "s"(so) : "memory", "scc");
}
template <int OFF> DI void ld_sc1(u32x4& d, unsigned voff, srd_t r, unsigned so) { asm volatile("buffer_load_dwordx4 %0, %1, %2, %3 offen offset:%4 sc1" : "=v"(d) : "v"(voff), "s"(r), "s"(so), "n"(OFF) : "memory"); }
template <int OFF> DI void ld_sc1_over(u32x4& d, unsigned voff, srd_t r, unsigned so) { asm volatile("buffer_load_dwordx4 %0, %1, %2, %3 offen offset:%4 sc1" : "+v"(d) : "v"(voff), "s"(r), "s"(so), "n"(OFF) : "memory"); }
DI void ld_pl(u32x4& d, unsigned voff, const void* sb) { asm volatile("global_load_dwordx4 %0, %1, %2" : "=v"(d) : "v"(voff), "s"(sb) : "memory"); }
template <int OFF> DI void ld_u16(unsigned& d, unsigned voff, srd_t r, unsigned so) { asm volatile("buffer_load_ushort %0, %1, %2, %3 offen offset:%4" : "=v"(d) : "v"(voff), "s"(r), "s"(so), "n"(OFF) : "memory"); }
DI void ld_u32(unsigned& d, unsigned voff, const void* sb) { asm volatile("global_load_dword %0, %1, %2" : "=v"(d) : "v"(voff), "s"(sb) : "memory"); }
DI void ld_u32_sc1(unsigned& d, unsigned voff, srd_t r, unsigned so) { asm volatile("buffer_load_dword %0, %1, %2, %3 offen sc1" : "=v"(d) : "v"(voff), "s"(r), "s"(so) : "memory"); }
DI void ld_flag(u64& d, unsigned voff, const void* sb) { asm volatile("global_load_dwordx2 %0, %1, %2 sc1" : "=v"(d) : "v"(voff), "s"(sb) : "memory"); }
template <int OFF = 0> DI void st16_sc1(u32x4 v, unsigned voff, srd_t r, unsigned so) {
  asm volatile("buffer_store_dwordx4 %0, %1, %2, %3 offen offset:%4 sc1\n\ts_nop 1" ::"v"(v), "v"(voff), "s"(r), "s"(so), "n"(OFF) : "memory");
}
DI void st4_sc1(unsigned v, unsigned voff, srd_t r, unsigned so) { asm volatile("buffer_store_dword %0, %1, %2, %3 offen sc1" ::"v"(v), "v"(voff), "s"(r), "s"(so) : "memory"); }
DI void st_flag1(srd_t r, unsigned so, u64 tag) {
  asm volatile("s_mov_b64 exec, 1\n\tbuffer_store_dwordx2 %0, off, %1, %2 sc1\n\ts_mov_b64 exec, -1" ::"v"(tag), "s"(r), "s"(so) : "memory");
}
template <class T> DI void pin(T& a) { asm volatile("" : "+v"(a)); }
template <class T> DI void pins(T& a) { asm volatile("" : "+s"(a)); }
template <class T> DI void opq(T& a) { asm("" : "+v"(a)); }
DI unsigned selm_n(u64 m, unsigned a, unsigned b) { unsigned r; asm("v_cndmask_b32_e64 %0, %2, %1, %3\n\ts_nop 1" : "=v"(r) : "v"(a), "v"(b), "s"(m)); return r; }
DI unsigned selm(u64 m, unsigned a, unsigned b) { unsigned r; asm("v_cndmask_b32_e64 %0, %2, %1, %3" : "=v"(r) : "v"(a), "v"(b), "s"(m)); return r; }
template <int N> DI void vbar() {
  static_assert(N >= 0, "wait");
  asm volatile("s_waitcnt vmcnt(%0) lgkmcnt(0)\n\ts_barrier" ::"n"(N > 63 ? 63 : N) : "memory");
}
template <int N> DI void wvm() { static_assert(N >= 0, "wait"); wait_vm<(N > 63 ? 63 : N)>(); }
DI void poll(u64& fv, u64 mask, unsigned voff, const void* sb, u64 tag) {
  if ((~__builtin_amdgcn_ballot_w64(fv == tag) & mask) != 0) {
#pragma unroll 1
    for (int it = 0; it < SPIN_CAP; ++it) {
      __builtin_amdgcn_s_sleep(1);
      ld_flag(fv, voff, sb); wait_vm<0>(); pin(fv);
      if ((~__builtin_amdgcn_ballot_w64(fv == tag) & mask) == 0) break;
    }
  }
  asm volatile("" ::: "memory");
}
typedef __attribute__((address_space(3))) char* lds_p;
template <class T, int O> DI T lds_ld(unsigned a) { return *(__attribute__((address_space(3))) const T*)((lds_cp)(uintptr_t)a + O); }
template <class T, int O> DI void lds_st(unsigned a, T v) { *(__attribute__((address_space(3))) T*)((lds_p)(uintptr_t)a + O) = v; }
DI void gram_dump(unsigned a, f32x16 g) {
  asm volatile("s_mov_b32 exec_lo, 0xff\n\ts_mov_b32 exec_hi, 0xff\n\tds_write_b128 %0, %1\n\ts_lshl_b64 exec, exec, 8\n\tds_write_b128 %0, %2\n\t"
               "s_lshl_b64 exec, exec, 8\n\tds_write_b128 %0, %3\n\ts_lshl_b64 exec, exec, 8\n\tds_write_b128 %0, %4\n\ts_mov_b64 exec, -1"
               ::"v"(a), "v"(__builtin_shufflevector(g, g, 0, 1, 2, 3)), "v"(__builtin_shufflevector(g, g, 4, 5, 6, 7)),
                 "v"(__builtin_shufflevector(g, g, 8, 9, 10, 11)), "v"(__builtin_shufflevector(g, g, 12, 13, 14, 15)) : "memory", "scc");
}
DI void put_part(unsigned a, int l, const f32x16& c0, const f32x16& c1) {
  sfor<8>([&](auto K) { constexpr int k = K; const f32x16& c = k < 4 ? c0 : c1;
    lds_st<f32x4, k * 1024>(a + (l >> 5) * 512 + (((l & 31) ^ (2 * k)) * 16), f32x4{c[4 * (k & 3)], c[4 * (k & 3) + 1], c[4 * (k & 3) + 2], c[4 * (k & 3) + 3]});
  });
}

__global__ __launch_bounds__(512, 2) void fwd(const uint16_t* __restrict__ x, const uint16_t* __restrict__ wr, const uint16_t* __restrict__ wi,
                                              const uint16_t* __restrict__ wg, const uint16_t* __restrict__ wu, const uint16_t* __restrict__ wd,
                                              char* __restrict__ ws_, uint16_t* __restrict__ out STAMP_ARG) {
  extern __shared__ __attribute__((aligned(16))) char smem[];
  STAMP_BEGIN
  const int w = __builtin_amdgcn_readfirstlane(threadIdx.x >> 6), ws = w & 3, wn = w >> 2;
  const unsigned b = blockIdx.x;
  const int xcd = b & 7, idx = b >> 3;
  const unsigned lds0 = lds_addr(smem), lw = lds0 + w * 2048;
  const int rb = (xcd & 1) * 8 + (idx & 7), g = xcd >> 1, ii = idx >> 3;
  const bool bcu = ii == 3;
  const int unit1 = 4 * g + ii, unit2 = 16 + 3 * g + ii;
  const int r0 = (unsigned)(rb + 2 * ii + g) % 7;
  auto kof = [&](int st) { int k = st + r0; return k >= 7 ? k - 7 : k; };
  constexpr unsigned CH = 64 * F_ * 2;
  unsigned xvo[2];
  const srd_t rx = mk_srd(x, S_ * D_ * 2), rg = mk_srd(wg, D_ * F_ * 2), ru = mk_srd((const char*)wu - 1024, D_ * F_ * 2 + 1024);
  const srd_t rd = mk_srd(wd, D_ * F_ * 2), rw = mk_srd(ws_, 16 << 20);
  const int rt = 4 * xcd + (idx & 3), dt = bcu ? 6 : (idx >> 2);
  const int rot = (idx & 3) + (idx >> 2), rot3 = (unsigned)rot % 3;
  auto rnd = [&](int i) { if (i < 4) return (i + rot) & 3; const int k = i - 4 + rot3; return 4 + (k >= 3 ? k - 3 : k); };
  const int rtb = 2 * rt + (bcu ? (idx >> 2) & 1 : 0);
  const int gi = (idx & 7) * 8 + xcd;
  const char* ctl = ws_ + OFF_C;
  const f32x4 zero = {0.f, 0.f, 0.f, 0.f};
  const f32x16 zero16 = {};

  u64 ep, fv, tag, pc;
  asm volatile("s_getpc_b64 %0\n.Lpf_here:" : "=s"(pc));
  unsigned pf, lvo, wvo, wo, xo[4], gvo, ovo, r2u;
  u32x4 xres;
  unsigned wv1, dv[4], ao, fl[16];
  u32x4 xa[2][4], ta[2][4], cg[4], wdf[7][8], hf[28][2];
  f32x16 accA, ah[2][2];
  auto res = [&](int R, int u) {
    const int l = lane_id(), pp = 32 * (rtb & 3) + 8 * (R ? ws : w) + (l >> 3);
    ld_u32_sc1(r2u, (unsigned)((pp >> 6) * 4096 + (pp & 63) * 4), rw, OFF_Z + (16 * u + 15) * 8192 + (rt >> 1) * 256);
    ovo = (unsigned)((128 * (rt >> 1) + 32 * (pp & 3) + (pp >> 2)) * D_ + dt * 64 + (l & 7) * 8) * 2;
    ld_pl(xres, ovo, x);
  };
  auto mk_dv = [&] { const int l = lane_id();
    dv[0] = (unsigned)((64 * ws + (l >> 3)) * D_ * 2 + dt * 128 + (((l & 7) ^ ((l >> 2) & 4)) * 16));
#pragma unroll
    for (int i = 1; i < 4; ++i) { dv[i] = dv[0] + i * (8 * D_ * 2 - 1024); pin(dv[i]); }
    ao = lds0 + (5 + ws) * SL + (4 * (l >> 5) + ((l & 15) >> 2)) * 128 + ((l >> 3) & 1) * 64 + ((l >> 4) & 1) * 32 + (l & 3) * 8;
    pin(ao);
  };
  auto ev = [&](auto Rc, auto Ic) { constexpr int R = Rc, id = Ic;
    if constexpr (id >= eX && id < eX + 7) { constexpr int j = id - eX;
      if constexpr (j == 0) {
        const int l = lane_id();
#pragma unroll
        for (int a = 0; a < 2; ++a) {
          const int p = 16 * w + 8 * a + (l >> 3);
          xvo[a] = (unsigned)((128 * rb + 32 * (p & 3) + (p >> 2)) * D_ + (((l & 7) ^ (4 * a + (l >> 4))) * 8)) * 2 - 1024 * a;
        }
      }
      dma2<(j + 1) * SL>(lw, xvo[0], rx, xvo[1], rx, kof(j) * 128);
    } else if constexpr (id >= eW && id < eW + 14) { constexpr int j = id - eW;
      if constexpr (j < 4) dma2<j * SL>(lw, wvo, rg, wvo, ru, kof(j) * CH);
      else dma4<(j % 5) * SL>(lds0 + ws * 4096, wvo, wvo, wv1, wv1, rg, ru, kof(j % 7) * CH + (j >= 7 ? (unit2 - unit1) * 128 : 0));
    } else if constexpr (id >= eD && id < eD + 14) { constexpr int i = id - eD;
      if constexpr (i >= 4 && !(i & 1)) asm volatile("s_waitcnt lgkmcnt(0)" ::: "memory");
      dma4<5 * SL + (i & 3) * 4096>(lds0 + ws * SL, dv[0], dv[1], dv[2], dv[3], rd, rd, rnd(i >> 1) * CH + (i & 1) * (32 * D_ * 2));
    } else if constexpr (id >= eDR && id < eDR + 7) { constexpr int i = id - eDR;
      wvm<pos<R>(id) - pos<R>(eD + 2 * i + 1)>();
      unsigned a1 = ao ^ 64; pin(a1);
      sfor<8>([&](auto K) { constexpr int o = (i & 1) * 8192 + (K >> 1) * 2048; wdf[i][K] = tr_at<o, o + 1024>((K & 1) ? a1 : ao); });
    } else if constexpr (id >= eH && id < eH + 28) { constexpr int s = id - eH;
      const unsigned so = (rtb * 28 + ws) * 4096 + rnd(s >> 2) * 16384;
      if constexpr (R) ld_sc1<(s & 3) * 1024>(hf[s][0], lvo, rw, so);
      else sfor<2>([&](auto C) { constexpr int c = C, k = (s - 5) >> 1;
        if constexpr (s < 5) ld_sc1<(s & 3) * 1024>(hf[s][c], lvo, rw, so + c * 28 * 4096);
        else {
          if constexpr (s >= 26) hf[s][c] = wdf[3][2 * (s - 26) + c];
          else if constexpr (s >= 16) hf[s][c] = hf[s - 10][c];
          else if constexpr (s & 1) hf[s][c] = hf[k][c];
          else hf[s][c] = wdf[k >> 2][2 * (k & 3) + c];
          ld_sc1_over<(s & 3) * 1024>(hf[s][c], lvo, rw, so + c * 28 * 4096);
        }
      });
    } else if constexpr (id == eEP) {
      ld_flag(ep, 0u, ctl);
    } else if constexpr (id == ePF) {
      unsigned len;
      asm volatile("s_mov_b32 %0, .Lpf_end-.Lpf_here" : "=s"(len));
      const unsigned o = (unsigned)((idx * 8 + w) * 4 + (lane_id() & 3)) * 64;
      ld_u32(pf, o < len ? o : 0u, (const void*)pc);
    } else if constexpr (id == eT1) {
      const int l = lane_id(), hh = l >> 5, fi = (l & 31) >> 1, f = fi == 15 ? 0 : 15 * unit1 + fi;
      const float cf_ = (float)f * (1.0f / 448);
      const unsigned sg = (f & 1) ? 0x80008000u : 0u;
#pragma unroll 1
      for (int s = w; s < 14; s += 8) {
        const float a0 = (float)(16 * s + 8 * hh) * cf_ + ((l & 1) ? 0.25f : 0.f);
        u32x4 o;
#pragma unroll
        for (int e = 0; e < 4; ++e) o[e] = pk2(vcos(a0 + (float)(2 * e) * cf_), vcos(a0 + (float)(2 * e + 1) * cf_));
        *(u32x4*)(smem + L_T1 + s * 1024 + l * 16) = o;
        *(u32x4*)(smem + L_T1 + 14336 + s * 1024 + l * 16) = o ^ sg;
      }
    } else if constexpr (id == ePRE) {
      const int l = lane_id(), r8 = l >> 3, ch = l & 7, r32 = l & 31, hh = l >> 5;
      lvo = (unsigned)l * 16;
      wvo = (unsigned)((8 * w + r8) * F_ * 2 + (ch ^ ((r8 & 2) << 1)) * 16 + unit1 * 128);
      wo = lds0 + hh * 2048 + ((l & 15) >> 2) * 128 + (r8 & 1) * 64 + ((l >> 4) & 1) * 32 + (l & 3) * 8;
#pragma unroll
      for (int i = 0; i < 4; ++i) { xo[i] = lds0 + SL + (32 * ws + r32) * 128 + (((2 * i + hh) ^ ((r32 >> 1) & 7)) * 16); pin(xo[i]); }
      pin(lvo); pin(wvo); pin(wo);
    } else if constexpr (id == eA6) {
      sfor<4>([&](auto I) { accA = mfma32(ta[0][I], xa[0][I], accA); });
      wvo += (unsigned)((8 * ws - 32) * F_ * 2); wv1 = wvo + (8 * F_ * 2 - 2048); pin(wvo); pin(wv1);
    } else if constexpr (id == eCS || id == eCS2) {
      constexpr int P = id == eCS2;
      const int l = lane_id();
      const unsigned c = (wo ^ ((ws >> 1) * 64)) + ((ws & 1) * 1024 + P * 2 * SL), sel = (((l >> 2) ^ (l >> 4)) & 1) ? 0u : ONES;
      const u32x4 sl = {sel, sel, sel, sel};
      f32x4 c0 = mfma16(sl, tr_at<0, 512>(c), zero), c1 = mfma16(sl, tr_at<4096, 4608>(c), zero);
      c0 = mfma16(sl, tr_at<8192, 8704>(c), c0); c1 = mfma16(sl, tr_at<12288, 12800>(c), c1);
      st16_sc1(u32x4{__float_as_uint(c0[0] + c1[0]), 0u, (unsigned)tag, (unsigned)(tag >> 32)}, lvo & 511, rw,
               OFF_S + ((P ? unit2 : unit1) * 4 + ws) * 4096 + r0 * 512);
    } else if constexpr (id == eZ) {
      const int l = lane_id(), r32 = l & 31;
      const float sq = lds_ld<float, L_GR - 8 * SL>(lds0 + 8 * SL + ws * 1024 + (r32 + 8 * (r32 & 4)) * 16 + (l & 3) * 4);
      const unsigned su = __float_as_uint(accA[14]);
      const float sm = __uint_as_float(__builtin_amdgcn_permlane32_swap(su, su, false, false)[1]);
      const float mean = sm * (1.0f / D_);
      const unsigned nm = pk2(-mean, -mean);
      lds_st<unsigned, L_NM - 8 * SL>(lds0 + 8 * SL + ws * 256 + l * 4, nm);
      const float rs = __builtin_amdgcn_rcpf(rbf(__builtin_amdgcn_sqrtf(rbf(rbf(fmaxf(sq - sm * mean, 0.f) * (1.0f / D_)) + 1e-5f))));
      accA[0] += __uint_as_float(selm(unit1 == 0 ? 0xffffffffull : 0ull, nm << 16, 0u)) * 448.f;
      const unsigned zv = (unsigned)((l >> 5) * 16384 + r32 * 4), zs = OFF_Z + unit1 * 131072 + (ws >> 1) * 4096 + rb * 256 + (ws & 1) * 128;
      sfor<8>([&](auto E) { constexpr int e = E;
        float p = accA[2 * e] * rs, q = accA[2 * e + 1] * rs; opq(p); opq(q);
        unsigned t = pk2(p, q);
        if constexpr (e == 7) t = selm(0xffffffffull, t, __float_as_uint(rs * rs));
        st4_sc1(t, zv, rw, zs + (4 * (e >> 1) + (e & 1)) * 8192);
      });
    } else if constexpr (id == eFWL) {
      const int l = lane_id();
      const srd_t ra = mk_srd(wr, S_ * DF_ * 2), rc = mk_srd(wi, S_ * DF_ * 2);
      const int fc = 32 * (2 * g + (ii >> 1)) + (l >> 2);
      const unsigned fo[2] = {(unsigned)((l & 3) * DF_ + min(fc, DF_ - 1)) * 2, (unsigned)((l & 3) * DF_ + min(fc + 16, DF_ - 1)) * 2};
      const unsigned fs = (unsigned)(16 * (rb >> 2) + 4 * ws + 64 * (16 * (ii & 1) + 4 * (rb & 3))) * DF_ * 2;
      sfor<8>([&](auto I) { constexpr int e = I >> 2, r = I & 3;
        ld_u16<0>(fl[2 * I], fo[e], ra, fs + r * 64 * DF_ * 2); ld_u16<0>(fl[2 * I + 1], fo[e], rc, fs + r * 64 * DF_ * 2); });
      mk_dv();
    } else if constexpr (id == eWT) {
      wvm<before<R>(eWT) - pos<R>(eFWL)>();
      const int l = lane_id();
      const unsigned tv = (unsigned)((l >> 2) * 8192 + (l & 3) * 16);
      const unsigned ts = OFF_W + (2 * g + (ii >> 1)) * 262144 + ((rb >> 2) * 128 + (ii & 1) * 64 + 16 * (rb & 3) + 4 * ws) * 16;
#pragma unroll
      for (int e = 0; e < 2; ++e) {
        u32x4 o;
#pragma unroll
        for (int r = 0; r < 4; ++r) { pin(fl[8 * e + 2 * r]); pin(fl[8 * e + 2 * r + 1]); o[r] = fl[8 * e + 2 * r] | (fl[8 * e + 2 * r + 1] << 16); }
        st16_sc1(o, tv, rw, ts + e * 131072);
      }
    } else if constexpr (id == eZF) {
      if (w == 4) st_flag1(rw, OFF_C + (FZ0 + unit1 * 16 + rb) * 8, tag);
    } else if constexpr (id == eWF) {
      if (w == 5) st_flag1(rw, OFF_C + (FW0 + (2 * g + (ii >> 1)) * 32 + rb * 2 + (ii & 1)) * 8, tag);
    } else if constexpr (id == eCL || id == eCL2 || id == eCP || id == eCP2) { constexpr int P = id == eCL2 || id == eCP2;
      const unsigned cv = (lvo & 511) + (lvo & 512) * 3, so = OFF_S + ((P ? unit2 : unit1) * 4 + ws) * 4096;
      auto ldc = [&] { sfor<4>([&](auto I) { ld_sc1<I * 512>(cg[I], cv, rw, so); }); };
      if constexpr (id == eCL || id == eCL2) ldc();
      else {
        wvm<pos<R>(id) - pos<R>(P ? eCL2 : eCL)>();
        auto bad = [&] { unsigned d = 0; sfor<4>([&](auto I) { pin(cg[I]); d |= (cg[I][2] ^ (unsigned)tag) | (cg[I][3] ^ (unsigned)(tag >> 32)); });
          return __builtin_amdgcn_ballot_w64(d != 0) != 0; };
        if (bad()) {
#pragma unroll 1
          for (int it = 0; it < SPIN_CAP; ++it) { __builtin_amdgcn_s_sleep(1); ldc(); wait_vm<0>(); if (!bad()) break; }
        }
        const float s = __uint_as_float(selm(0xffffffffull, cg[0][0], 0u)) + __uint_as_float(cg[1][0]) + (__uint_as_float(cg[2][0]) + __uint_as_float(cg[3][0]));
        const float q1 = s - rbf(s), q2 = q1 - rbf(q1);
        lds_st<u32x4, L_P - 8 * SL + P * 4096>(lds0 + 8 * SL + ws * 1024 + lvo, u32x4{pk2(s, q1), pk2(q2, 0.f), 0u, 0u});
      }
    } else if constexpr (id == eFL1) {
      const int l = lane_id(), i = l < 14 ? l >> 1 : 0;
      gvo = (unsigned)(FH0 + (rtb + (R ? 0 : l & 1)) * 28 + 4 * i + ws) * 8;
      ld_flag(fv, gvo, ctl);
    } else if constexpr (id == eFL2) { ld_flag(fv, gvo, ctl);
    } else if constexpr (id == eP1) { wvm<pos<R>(eP1) - pos<R>(eFL1)>(); pin(fv); ST(5) poll(fv, 0xffull, gvo, ctl, tag); ST(6)
    } else if constexpr (id == eP2) { wvm<pos<R>(eP2) - pos<R>(eFL2)>(); pin(fv); ST(8) poll(fv, 0x3f00ull, gvo, ctl, tag); ST(9)
    } else if constexpr (id == eRES) { res(0, ws);
    }
  };
  auto run = [&](auto Rc, auto Mc) { constexpr int R = Rc, mk = Mc, i0 = mk < 0 ? 0 : sidx<R>(mk) + 1;
    sfor<seglen<R>(i0)>([&](auto K) { ev(Rc, ic<seq<R>(i0 + K).id>{}); SB(); });
  };
  auto mk_tag = [&] { pin(ep); pin(pf);
    tag = (((u64)__builtin_amdgcn_readfirstlane((unsigned)(ep >> 32)) << 32) | (unsigned)__builtin_amdgcn_readfirstlane((unsigned)ep)) + 1; };
  auto tail_h = [&](auto Rc) { constexpr int R = Rc;
    run(Rc, ic<mP + 13>{});
    sfor<28>([&](auto Sc) { constexpr int s = Sc;
      wvm<pos<R>(mK + s) - pos<R>(eH + s)>();
      if constexpr (s == 0) { ST(7) }
      if constexpr (s == 16) { ST(10) }
      sfor<R ? 2 : 4>([&](auto I) { constexpr int a = I & 1, c = I >> 1;
        if constexpr (!a) pin(hf[s][c]);
        ah[c][a] = mfma32(wdf[s >> 2][2 * (s & 3) + a], hf[s][c], s == 0 ? zero16 : ah[c][a]); pin(ah[c][a]); });
      SB();
      run(Rc, ic<mK + s>{});
    });
    ST(11)
    const int l = lane_id();
    put_part(lds0 + (5 + ws) * SL, l, ah[0][0], ah[0][1]);
    if constexpr (!R) { put_part(lds0 + (5 + ws) * SL + 8192, l, ah[1][0], ah[1][1]); pin(xres); pin(r2u); }
  };
  auto tail_c = [&](auto Rc, auto&& mid) { constexpr int R = Rc, NK = R ? 7 : 14;
    const int l = lane_id(), kq = R ? ws : ws >> 1;
    u32x4 t2[NK][2], yf[NK];
    const float c0 = (float)((2 * kq + (l >> 5)) * (4 * NK));
    float dd[2], a0[2];
#pragma unroll
    for (int t = 0; t < 2; ++t) { dd[t] = (float)(64 * dt + 32 * t + (l & 31)) * (1.0f / 448); a0[t] = __builtin_amdgcn_fractf(c0 * dd[t]); }
    sfor<NK>([&](auto Kc) { constexpr int k = Kc;
#pragma unroll
      for (int t = 0; t < 2; ++t) {
#pragma unroll
        for (int e = 0; e < 4; ++e) { const float a = (float)(4 * k + e) * dd[t] + a0[t]; t2[k][t][e] = pk2(vcos(a), vsin(a)); }
        if constexpr (k == 0) t2[0][t][0] = selm(kq == 0 ? 0xffffffffull : 0ull, (l & 1) ? 0xbf003f00u : 0x3f003f00u, t2[0][t][0]);
        pin(t2[k][t]);
      }
      SB();
      if constexpr (k == 3) { mid(); SB(); }
      if constexpr (k == NK - 3) { gvo = (unsigned)(FY0 + 16 * (l < 2 * NK ? 2 * NK * kq + l : 56)) * 8; ld_flag(fv, gvo, ctl); SB(); }
    });
    wait_vm<0>(); pin(fv);
    ST(8)
    poll(fv, ~0ull, gvo, ctl, tag);
    ST(9)
    const unsigned yo = OFF_Y + (rtb + (R ? 0 : ws & 1)) * 512, yv = (unsigned)((l >> 5) * (NK * 32768) + (l & 31) * 16);
    unsigned yn;
    ld_u32_sc1(yn, yv & 511, rw, yo + 56 * 32768);
    sfor<NK>([&](auto I) { ld_sc1<0>(yf[I], yv, rw, yo + (2 * NK * kq + I) * 32768); });
    res(R, 14);
    f32x16 ac[2];
    sfor<NK>([&](auto I) { constexpr int i = I;
      wait_vm<NK + 1 - i>(); pin(yf[i]);
      if constexpr (i == 0) { ST(10) pin(yn); yf[0][0] = selm_n(kq == 0 ? 0xffffffffull : 0ull, (yf[0][0] & 0xffffu) | (yn << 16), yf[0][0]); }
      ac[0] = mfma32(t2[i][0], yf[i], i ? ac[0] : zero16); ac[1] = mfma32(t2[i][1], yf[i], i ? ac[1] : zero16);
      SB();
    });
    ST(11)
    put_part(lds0 + (R ? 3 * SL : 0) + ws * 8192, l, ac[0], ac[1]);
    wait_vm<0>(); pin(xres); pin(r2u);
  };

  auto zw_flags = [&] { const int c0 = gi < 56 ? 4 * gi : 221;
    gvo = (selm(0xffffull, FZ0 + c0 / 15 * 16, selm(0xffffffffull, FZ0 - 16 + (c0 + 3) / 15 * 16, FW0 - 32 + (gi < 56 ? gi >> 3 : 7) * 32)) + lane_id()) * 8; };
  unsigned hso;
  auto hflag = [&] { wait_vm<0>(); st_flag1(rw, OFF_C + FH0 * 8 + (hso >> 9), tag); };
  if (wn == 0) {
    run(ic<0>{}, ic<-1>{});
    u32x4 xf[28], wf[RA], nb, eo;
    f32x16 acc[2][4], accG = zero16;
    auto gram = [&](auto I) { accG = mfma32(xf[I], xf[I], accG); };
    sfor<7>([&](auto J) { constexpr int j = J;
      vbar<pos<0>(mX + j) - pos<0>(eX + j)>();
      if constexpr (j == 0) { ST(1) }
#pragma unroll
      for (int i = 0; i < 4; ++i) {
        if constexpr (j == 3) { xo[i] += 3 * SL; pin(xo[i]); }
        xf[4 * j + i] = l128_at<(j < 3 ? j : j - 3) * SL>(xo[i]);
      }
      SB();
      run(ic<0>{}, ic<mX + j>{});
      if constexpr (j == 3) mk_tag();
      if constexpr (j > 0) sfor<4>([&](auto I) { gram(ic<4 * j - 4 + I>{}); });
      SB();
    });
    hso = ((4 * rb + ws) * 28 + unit1) * 4096;
    auto epi = [&](auto Pc, auto Qc, auto Ec) { constexpr int P = Pc, Q = Qc, e = Ec;
      float h0 = fmaxf(acc[P][2 * Q][2 * e], 0.f), h1 = fmaxf(acc[P][2 * Q][2 * e + 1], 0.f);
      h0 *= acc[P][2 * Q + 1][2 * e]; h1 *= acc[P][2 * Q + 1][2 * e + 1];
      opq(h0); opq(h1);
      unsigned t = pk2(h0, h1); pin(t);
      eo[e & 3] = t;
      if constexpr ((e & 3) == 3) st16_sc1<Q * 2048 + (e >> 2) * 1024>(eo, lvo, rw, hso);
    };
    unsigned a0, a1;
    auto step = [&](auto Tc, auto Endc) { constexpr int T = Tc, M = T - RA;
      if constexpr (T < Endc && T == t_start(t_per(T))) { constexpr int n = t_per(T);
        if constexpr (n == 0) vbar<6>();
        else if constexpr (n < 4) asm volatile("s_waitcnt vmcnt(%0)\n\ts_barrier" ::"n"(6 - 2 * n) : "memory");
        else asm volatile("s_barrier" ::: "memory");
        if constexpr (n == 0) { ST(2) }
        if constexpr (n == 7) { ST(3) }
        pin(wo); a0 = wo + (n % 5) * SL; a1 = a0 ^ 64; pin(a0); pin(a1);
      }
      if constexpr (M >= 0) { constexpr int P = M / 116, n = t_per(M), i = t_idx(M), q = i >= 16 ? i - 16 : 2 * (i >> 3) + (i & 1);
        if constexpr (i >= 16) acc[P][q] = mfma32(wf[M % RA], nb, acc[P][q]);
        else acc[P][q] = mfma32(wf[M % RA], xf[4 * (n % 7) + ((i >> 1) & 3)], (n % 7 == 0 && (i & 6) == 0) ? zero16 : acc[P][q]);
        pin(acc[P][q]);
      } else if constexpr (T < 4) gram(ic<24 + T>{});
      SB();
      if constexpr (T < Endc) { constexpr int i = t_idx(T);
        if constexpr (i >= 16) wf[T % RA] = l128_at<L_P - 8 * SL + (T / 116) * 4096 + (i - 16) * 1024>(lvo + (lds0 + 8 * SL));
        else { constexpr int o = ((i >> 1) & 3) * 4096 + (i & 1) * 1024; wf[T % RA] = tr_at<o, o + 512>(i >= 8 ? a1 : a0); }
      }
      SB();
      if constexpr (T == 7) gram_dump(lds0 + L_GR + ws * 1024 + lvo, accG);
      if constexpr (T == 9) asm volatile("s_waitcnt lgkmcnt(4)\n\ts_barrier" ::: "memory");
      if constexpr (T == 40) { const unsigned nm = lds_ld<unsigned, L_NM - 8 * SL>(lds0 + 8 * SL + ws * 256 + (lvo >> 2)); nb = u32x4{nm, nm, nm, nm}; }
      if constexpr (T == 44) pin(nb);
      if constexpr (M >= 0 && M % 116 >= 108) epi(ic<M / 116>{}, ic<0>{}, ic<M % 116 - 108>{});
      if constexpr (M >= 116 && M < 124) epi(ic<0>{}, ic<1>{}, ic<M - 116>{});
      if constexpr (M == 156) { hflag(); hso += (unit2 - unit1) * 4096; }
      SB();
    };
    sfor<116>([&](auto I) { step(I, ic<232>{}); });
    if (!bcu) {
      sfor<116 + RA>([&](auto I) { step(ic<116 + I>{}, ic<232>{}); });
      sfor<8>([&](auto I) { epi(ic<1>{}, ic<1>{}, I); });
      ST(4)
      tail_c(ic<0>{}, [&] { ST(5) hflag(); ST(6) });
    } else {
      sfor<RA>([&](auto I) { step(ic<116 + I>{}, ic<116>{}); });
      sfor<8>([&](auto I) { epi(ic<0>{}, ic<1>{}, I); });
      ST(3)
    }
  } else {
    __builtin_amdgcn_s_setprio(2);
    run(ic<0>{}, ic<-1>{});
    accA = zero16;
    sfor<7>([&](auto J) { constexpr int j = J;
      vbar<pos<0>(mX + j) - pos<0>(eX + j)>();
      if constexpr (j == 0) { ST(1) }
      const unsigned to = lvo + (lds0 + L_T1 + kof(j) * 4096);
      sfor<4>([&](auto I) { constexpr int i = I;
        if constexpr (j == 3) { xo[i] += 3 * SL; pin(xo[i]); }
        xa[j & 1][i] = l128_at<(j < 3 ? j : j - 3) * SL>(xo[i]); ta[j & 1][i] = l128_at<i * 1024>(to);
      });
      SB();
      run(ic<0>{}, ic<mX + j>{});
      if constexpr (j == 3) mk_tag();
      if constexpr (j > 0) sfor<4>([&](auto I) { accA = mfma32(ta[~j & 1][I], xa[~j & 1][I], accA); });
      SB();
    });
    auto period = [&](auto Rc, auto Nc) { constexpr int R = Rc, n = Nc;
      vbar<pos<R>(mP + n) - need<R>(n)>();
      if constexpr (n == 0) { ST(2) }
      if constexpr (n == 7) { ST(3) }
      if constexpr (n < 13) run(Rc, ic<mP + n>{});
      if constexpr (n == 0) { asm volatile("s_barrier" ::: "memory"); run(Rc, ic<mS>{}); }
    };
    sfor<4>([&](auto I) { period(ic<0>{}, I); });
    if (!bcu) {
      sfor<10>([&](auto I) { period(ic<0>{}, ic<I + 4>{}); });
      ST(4)
      tail_h(ic<0>{});
    } else {
      sfor<3>([&](auto I) { period(ic<1>{}, ic<I + 4>{}); });
      zw_flags();
      ld_flag(fv, gvo, ctl); wait_vm<0>(); pin(fv);
      ST(3)
      poll(fv, 0xffffffffull, gvo, ctl, tag);
      ST(4)
      const int c = gi < 56 ? 4 * gi + ws : 224;
      const unsigned zo = OFF_Z + (c + c / 15) * 8192;
      dma4_sc1<3 * SL>(lds0 + ws * 8192, lvo, rw, zo); dma4_sc1<3 * SL + 4096>(lds0 + ws * 8192, lvo, rw, zo + 4096);
    }
  }
  if (bcu) {
    const int l = lane_id(), v = l & 15, q = l >> 4, vp = 4 * (v & 3) + (v >> 2);
    __builtin_amdgcn_s_setprio(0);
    char* fs = smem + 3 * SL;
    lvo = (unsigned)l * 16;
    zw_flags(); ld_flag(fv, gvo, ctl);
    u32x4 wv[2][2], M1[4], M2[4], C32[2], S32[2], N32[2];
    const float k1f = (float)(16 * ws + v) * (1.0f / 64);
#pragma unroll
    for (int ks = 0; ks < 4; ++ks) {
#pragma unroll
      for (int e = 0; e < 4; ++e) {
        const float a = (float)(16 * ks + 4 * q + e) * k1f, c = vcos(a), s = vsin(a);
        M1[ks][e] = pk2(c, s); M2[ks][e] = pk2(-s, c);
      }
      pin(M1[ks]); pin(M2[ks]);
    }
#pragma unroll
    for (int t = 0; t < 2; ++t) {
      const float nf = (float)(16 * t + v) * (1.0f / 32);
#pragma unroll
      for (int e = 0; e < 4; ++e) {
        const float a0 = (float)(16 * (e >> 1) + 4 * q + 2 * (e & 1)) * nf, a1 = a0 + nf;
        C32[t][e] = pk2(vcos(a0), vcos(a1)); S32[t][e] = pk2(vsin(a0), vsin(a1)); N32[t][e] = S32[t][e] ^ 0x80008000u;
      }
      pin(C32[t]); pin(S32[t]); pin(N32[t]);
    }
    float t1c[2][4], t1s[2][4], u2c[2][4], u2s[2][4];
    constexpr float scale = 2.0f / (448.0f * 2048.0f);
#pragma unroll
    for (int t = 0; t < 2; ++t)
#pragma unroll
      for (int r = 0; r < 4; ++r) {
        const float a1 = (float)(16 * t + 4 * q + r) * (k1f * (1.0f / 32));
        t1c[t][r] = vcos(a1); t1s[t][r] = vsin(a1);
        const float a2 = (float)((16 * t + v) * (16 * ws + 4 * q + r)) * (1.0f / 2048);
        u2c[t][r] = vcos(a2) * scale; u2s[t][r] = vsin(a2) * scale;
        pin(t1c[t][r]); pin(t1s[t][r]); pin(u2c[t][r]); pin(u2s[t][r]);
      }
    SB();
    wait_vm<0>(); pin(fv);
    if (wn == 0) hflag();
    else poll(fv, ~0ull, gvo, ctl, tag);
    bar();
    ST(12)
#pragma unroll
    for (int k = 0; k < 2; ++k) {
      const unsigned wp = OFF_W + (gi < 56 ? 4 * gi + 2 * wn + k : 224) * 8192 + ws * 2048;
      ld_sc1<0>(wv[k][0], lvo, rw, wp); ld_sc1<1024>(wv[k][1], lvo, rw, wp);
    }
    f32x4 pr[2][2], pi[2][2];
#pragma unroll
    for (int k = 0; k < 2; ++k)
#pragma unroll
      for (int ks = 0; ks < 4; ++ks)
#pragma unroll
        for (int t = 0; t < 2; ++t) {
          const u32x4 z = lds128(fs + (2 * wn + k) * 8192 + t * 4096 + ks * 1024 + l * 16);
          pr[k][t] = mfma16(z, M1[ks], ks == 0 ? zero : pr[k][t]); pi[k][t] = mfma16(z, M2[ks], ks == 0 ? zero : pi[k][t]);
        }
    bar();
#pragma unroll
    for (int k = 0; k < 2; ++k) {
      u32x4 fr, fi;
      {
        float a[8], bq[8];
#pragma unroll
        for (int t = 0; t < 2; ++t)
#pragma unroll
          for (int r = 0; r < 4; ++r) {
            a[4 * t + r] = pr[k][t][r] * t1c[t][r] + pi[k][t][r] * t1s[t][r];
            bq[4 * t + r] = pi[k][t][r] * t1c[t][r] - pr[k][t][r] * t1s[t][r];
          }
#pragma unroll
        for (int e = 0; e < 4; ++e) { fr[e] = pk2(a[2 * e], a[2 * e + 1]); fi[e] = pk2(bq[2 * e], bq[2 * e + 1]); }
      }
      f32x4 qr[2], qi[2];
#pragma unroll
      for (int t = 0; t < 2; ++t) {
        qr[t] = mfma16(C32[t], fr, zero); qi[t] = mfma16(C32[t], fi, zero);
        qr[t] = mfma16(S32[t], fi, qr[t]); qi[t] = mfma16(N32[t], fr, qi[t]);
      }
      if (k == 0) { wait_vm<0>(); pin(wv[0][0]); pin(wv[0][1]); pin(wv[1][0]); pin(wv[1][1]); }
      {
        float a[8], bq[8];
#pragma unroll
        for (int t = 0; t < 2; ++t)
#pragma unroll
          for (int r = 0; r < 4; ++r) {
            const float wre = bflo(wv[k][t][r]), wim = bfhi(wv[k][t][r]);
            a[4 * t + r] = qr[t][r] * wre - qi[t][r] * wim;
            bq[4 * t + r] = qr[t][r] * wim + qi[t][r] * wre;
          }
#pragma unroll
        for (int e = 0; e < 4; ++e) { fr[e] = pk2(a[2 * e], a[2 * e + 1]); fi[e] = pk2(bq[2 * e], bq[2 * e + 1]); }
      }
#pragma unroll
      for (int t = 0; t < 2; ++t) {
        f32x4 rr = mfma16(fr, C32[t], zero), ri = mfma16(fi, C32[t], zero);
        rr = mfma16(fi, N32[t], rr); ri = mfma16(fr, S32[t], ri);
        u32x4 o;
#pragma unroll
        for (int r = 0; r < 4; ++r) o[r] = pk2(rr[r] * u2c[t][r] - ri[r] * u2s[t][r], -(rr[r] * u2s[t][r]) - ri[r] * u2c[t][r]);
        *(u32x4*)(fs + (2 * wn + k) * 8192 + (16 * t + v) * 256 + (((4 * ws + q) ^ vp) * 16)) = o;
      }
    }
    bar();
    f32x4 yr[4], yi[4];
#pragma unroll
    for (int ks = 0; ks < 4; ++ks)
#pragma unroll
      for (int k = 0; k < 4; ++k) {
        const u32x4 rf = lds128(fs + k * 8192 + (16 * wn + vp) * 256 + (((4 * ks + q) ^ v) * 16));
        yr[k] = mfma16(rf, M1[ks], ks == 0 ? zero : yr[k]); yi[k] = mfma16(rf, M2[ks], ks == 0 ? zero : yi[k]);
      }
    const unsigned yb = OFF_Y + gi * 32768 + ws * 8192 + wn * 1024;
    sfor<4>([&](auto Rc) { constexpr int R = Rc;
      st16_sc1<R * 256>(u32x4{pk2(yr[0][R], yi[0][R]), pk2(yr[1][R], yi[1][R]), pk2(yr[2][R], yi[2][R]), pk2(yr[3][R], yi[3][R])},
                        (unsigned)((v >> 2) * 2048 + q * 64 + (v & 3) * 16), rw, yb);
    });
    ST(13)
    vbar<0>();
    if (w == 0) st_flag1(rw, OFF_C + (FY0 + 16 * gi) * 8, tag);
    if (wn == 0) tail_c(ic<1>{}, [] {}); else { mk_dv(); tail_h(ic<1>{}); }
  }
  bar();
  if (!bcu) { ST(12) }
  if (!bcu || wn == 0) {
    const int l = lane_id(), ch = l & 7;
    const unsigned q = lds0 + ch * 1024 + (((8 * ws + (l >> 3)) ^ (2 * ch)) * 16), qh = q + 5 * SL + wn * 8192, qc = q + (bcu ? 3 * SL : wn * 8192);
    f32x4 a0 = lds_ld<f32x4, 0>(qh), a1 = lds_ld<f32x4, 512>(qh), c0 = lds_ld<f32x4, 0>(qc), c1 = lds_ld<f32x4, 512>(qc);
    sfor<3>([&](auto I) { a0 += lds_ld<f32x4, (I + 1) * SL>(qh); a1 += lds_ld<f32x4, (I + 1) * SL + 512>(qh); });
    c0 += lds_ld<f32x4, SL>(qc); c1 += lds_ld<f32x4, SL + 512>(qc);
    if (bcu) { c0 += lds_ld<f32x4, 8192>(qc) + lds_ld<f32x4, SL + 8192>(qc); c1 += lds_ld<f32x4, 8704>(qc) + lds_ld<f32x4, SL + 8704>(qc); }
    const float r2 = __uint_as_float(r2u);
    u32x4 o;
    o[0] = pk2(a0[0] * r2 + (c0[0] + bflo(xres[0])), a0[1] * r2 + (c0[1] + bfhi(xres[0])));
    o[1] = pk2(a0[2] * r2 + (c0[2] + bflo(xres[1])), a0[3] * r2 + (c0[3] + bfhi(xres[1])));
    o[2] = pk2(a1[0] * r2 + (c1[0] + bflo(xres[2])), a1[1] * r2 + (c1[1] + bfhi(xres[2])));
    o[3] = pk2(a1[2] * r2 + (c1[2] + bflo(xres[3])), a1[3] * r2 + (c1[3] + bfhi(xres[3])));
    asm volatile("global_store_dwordx4 %0, %1, %2 sc0 sc1\n\ts_nop 1" ::"v"(ovo), "v"(o), "s"(out) : "memory");
  }
  asm volatile(".Lpf_end:");
  if (b == 0 && w == 0) st_flag1(rw, OFF_C, tag);
  STAMP_END(bcu ? 1 : 0)
}

extern "C" void kernel_launch(void* const* d_in, const int* in_sizes, int n_in, void* d_out, int out_size, void* d_ws, size_t ws_size,
                              hipStream_t stream) {
  (void)hipFuncSetAttribute((const void*)fwd, hipFuncAttributeMaxDynamicSharedMemorySize, 163840);
  fwd<<<256, 512, 163840, stream>>>((const uint16_t*)d_in[0], (const uint16_t*)d_in[1], (const uint16_t*)d_in[2], (const uint16_t*)d_in[4],
                                    (const uint16_t*)d_in[5], (const uint16_t*)d_in[6], (char*)d_ws, (uint16_t*)d_out STAMP_PASS);
}
```

```cpp
#define RO 0
#define ROK 0
#define ROB 100
#include <hip/hip_runtime.h>
#include <hip/hip_bf16.h>
#include <stdint.h>
#include <utility>

typedef __attribute__((ext_vector_type(4))) short s16x4;
typedef __attribute__((ext_vector_type(8))) __bf16 bf16x8;
typedef __attribute__((ext_vector_type(2))) __bf16 bf16x2;
typedef __attribute__((ext_vector_type(4))) float f32x4;
typedef __attribute__((ext_vector_type(16))) float f32x16;
typedef __attribute__((ext_vector_type(2))) float f32x2;
typedef __attribute__((ext_vector_type(4))) unsigned u32x4;
typedef __attribute__((ext_vector_type(2))) unsigned u32x2;
typedef __attribute__((address_space(3))) s16x4* lds_s16x4_p;
typedef unsigned long long u64;
typedef __attribute__((address_space(1))) u64 gu64;
typedef __attribute__((address_space(3))) unsigned lu32;
#define DI __device__ __forceinline__
#define SB() __builtin_amdgcn_sched_barrier(0)
template <int N> using ic = std::integral_constant<int, N>;

constexpr int S_ = 2048, D_ = 448, F_ = 1792, DF_ = 225;

template <class Fn, int... I> DI void sfor_impl(Fn&& f, std::integer_sequence<int, I...>) { (f(ic<I>{}), ...); }
template <int N, class Fn> DI void sfor(Fn&& f) { sfor_impl(f, std::make_integer_sequence<int, N>{}); }

DI f32x4 mfma16(u32x4 a, u32x4 b, f32x4 c) {
  return __builtin_amdgcn_mfma_f32_16x16x32_bf16(__builtin_bit_cast(bf16x8, a), __builtin_bit_cast(bf16x8, b), c, 0, 0, 0);
}
DI f32x16 mfma32(u32x4 a, u32x4 b, f32x16 c) {
  return __builtin_amdgcn_mfma_f32_32x32x16_bf16(__builtin_bit_cast(bf16x8, a), __builtin_bit_cast(bf16x8, b), c, 0, 0, 0);
}
DI unsigned pk2(float lo, float hi) { bf16x2 p; p[0] = (__bf16)lo; p[1] = (__bf16)hi; return __builtin_bit_cast(unsigned, p); }
DI float bflo(unsigned u) { return __builtin_bit_cast(float, u << 16); }
DI float bfhi(unsigned u) { return __builtin_bit_cast(float, u & 0xffff0000u); }
DI float rbf(float f) { return (float)(__bf16)f; }
typedef __attribute__((address_space(3))) const char* lds_cp;
typedef __attribute__((address_space(3))) const u32x4* lds_u32x4_p;
template <int O0, int O1> DI u32x4 tr_at(unsigned a) {
  s16x4 x = __builtin_amdgcn_ds_read_tr16_b64_v4i16((lds_s16x4_p)((lds_cp)(uintptr_t)a + O0));
  s16x4 y = __builtin_amdgcn_ds_read_tr16_b64_v4i16((lds_s16x4_p)((lds_cp)(uintptr_t)a + O1));
  return __builtin_bit_cast(u32x4, __builtin_shufflevector(x, y, 0, 1, 2, 3, 4, 5, 6, 7));
}
template <int O> DI u32x4 l128_at(unsigned a) { return *(lds_u32x4_p)((lds_cp)(uintptr_t)a + O); }
DI unsigned lds_addr(const void* p) { return (unsigned)(uintptr_t)(__attribute__((address_space(3))) const char*)p; }
template <int N> DI void wait_vm() { asm volatile("s_waitcnt vmcnt(%0)" ::"n"(N) : "memory"); }
DI void bar() { asm volatile("s_waitcnt lgkmcnt(0)" ::: "memory"); __builtin_amdgcn_s_barrier(); asm volatile("" ::: "memory"); }
DI u32x4 lds128(const char* p) { return *(const u32x4*)p; }
DI int lane_id() { int r; asm volatile("v_mbcnt_lo_u32_b32 %0, -1, 0\n\tv_mbcnt_hi_u32_b32 %0, -1, %0" : "=v"(r)); return r; }
DI float vcos(float a) { return __builtin_amdgcn_cosf(a); }
DI float vsin(float a) { return __builtin_amdgcn_sinf(a); }

#define STAMP_BEGIN unsigned long long _r0, _rk; asm volatile("s_memrealtime %0\n\ts_waitcnt lgkmcnt(0)" : "=s"(_r0) :: "memory"); _rk = _r0;
#define ST(i) { if constexpr ((i) == RO) { SB(); asm volatile("s_memrealtime %0\n\ts_waitcnt lgkmcnt(0)" : "=s"(_rk) :: "memory"); SB(); } }
#define STP(i)
#define STX(i)
#define STAMP_END(kid) { asm volatile("s_waitcnt vmcnt(0)" ::: "memory"); ST(16) if constexpr ((kid) == ROK) { if (blockIdx.x == ROB && ws == 0) { \
  const unsigned _e = 4000u + 4u * (unsigned)(_rk - _r0); \
  _Pragma("unroll 1") for (int _i = 0; _i < 200000; ++_i) { if ((unsigned)(__builtin_amdgcn_s_memrealtime() - _r0) >= _e) break; __builtin_amdgcn_s_sleep(1); } } } }
#define STAMP_ARG
#define STAMP_PASS

constexpr unsigned ONES = 0x3f803f80u;
constexpr int SL = 16384;
constexpr int L_T1 = 8 * SL, L_P = 9 * SL, L_CT = 9 * SL + 13312;
constexpr unsigned OFF_Z = 8u << 20, OFF_W = 10u << 20, OFF_Y = 12u << 20, OFF_C = 14u << 20;
constexpr int FY0 = 256, FW0 = 512, FZ0 = 1536, FH0 = 2560;
#define SPINC 200000
constexpr int SPIN_CAP = SPINC;
constexpr int LEAD = 10, HFAT = 180;
constexpr int RA = 6;

enum : int { eX = 0, eW = 8, eD = 24, eDR = 40, eH = 48, eTB = 77, eEP = 81, ePF, eT1, ePRE, eDV, eF, eFA, eFB, eFC, eZ, eZF, eWF, eWL,
             eHF, eFL1, eP1, eFL2, eP2, eRES, mX = 200, mR = 209, mP = 210, mK = 230, mEnd = 500 };
struct Ev { int id, n; };
#define PRO0 {ePF, 1}, {eX + 0, 2}, {eX + 1, 2}, {eX + 2, 2}, {eT1, 0}, {mR, 0}, {ePRE, 0}
#define PRO PRO0, {mX + 0, 0}, {eEP, 1}, {eF, 2}, {mX + 1, 0}, {mX + 2, 0}, {mX + 3, 0}, {mX + 4, 0}, {mX + 5, 0}, {mX + 6, 0}, {mP + 0, 0}, {eZ, 2}, {eW + 3, 4}
constexpr Ev SEQ_R[] = {PRO,
  {mP + 1, 0}, {eFA, 0}, {eW + 4, 4}, {eFB, 0}, {eDV, 0}, {eD + 0, 4}, {eFC, 2},
  {mP + 2, 0}, {eW + 5, 4}, {eZF, 1}, {eD + 1, 4},
  {mP + 3, 0}, {eW + 6, 4}, {eD + 2, 4},
  {mP + 4, 0}, {eW + 7, 4}, {eWF, 1}, {eD + 3, 4}, {eDR + 0, 0},
  {mP + 5, 0}, {eW + 8, 4}, {eD + 4, 4},
  {mP + 6, 0}, {eW + 9, 4}, {eD + 5, 4}, {eDR + 1, 0},
  {mP + 7, 0}, {eW + 10, 4},
  {mP + 8, 0}, {eW + 11, 4}, {eD + 6, 4},
  {mP + 9, 0}, {eW + 12, 4}, {eD + 7, 4}, {eDR + 2, 0},
  {mP + 10, 0}, {eW + 13, 4}, {eD + 8, 4},
  {mP + 11, 0}, {eD + 9, 4}, {eDR + 3, 0},
  {mP + 12, 0}, {eD + 10, 4}, {eD + 11, 4}, {eFL1, 1},
  {mP + 13, 0}, {eP1, 0}, {eH + 0, 2}, {eH + 1, 2}, {eH + 2, 2}, {eH + 3, 2}, {eH + 4, 2},
  {mK + 0, 0}, {eH + 5, 2}, {eH + 6, 2}, {mK + 1, 0}, {eH + 7, 2}, {eH + 8, 2}, {mK + 2, 0}, {eH + 9, 2}, {eH + 10, 2}, {mK + 3, 0}, {eH + 11, 2}, {eH + 12, 2},
  {mK + 4, 0}, {eH + 13, 2}, {eH + 14, 2}, {mK + 5, 0}, {eH + 15, 2}, {mK + 6, 0}, {mK + 7, 0}, {eDR + 4, 0},
  {mK + 8, 0}, {eD + 12, 4}, {mK + 9, 0}, {eD + 13, 4}, {mK + 10, 0}, {eRES, 2}, {mK + 11, 0}, {eDR + 5, 0},
  {mK + 12, 0}, {mK + 13, 0}, {eFL2, 1}, {mK + 14, 0}, {mK + 15, 0}, {eP2, 0},
  {eH + 16, 2}, {eH + 17, 2}, {eH + 18, 2}, {eH + 19, 2}, {eH + 20, 2}, {eH + 21, 2}, {eH + 22, 2}, {eH + 23, 2}, {eH + 24, 2}, {eH + 25, 2}, {eH + 26, 2}, {eH + 27, 2},
  {mK + 16, 0}, {mK + 17, 0}, {mK + 18, 0}, {mK + 19, 0}, {eDR + 6, 0}, {mK + 20, 0}, {mK + 21, 0}, {mK + 22, 0}, {mK + 23, 0},
  {mK + 24, 0}, {mK + 25, 0}, {mK + 26, 0}, {mK + 27, 0}, {mEnd, 0}};
constexpr Ev SEQ_B[] = {PRO,
  {mP + 1, 0}, {eFA, 0}, {eW + 4, 4}, {eFB, 0}, {eTB + 0, 0}, {eFC, 2},
  {mP + 2, 0}, {eW + 5, 4}, {eZF, 1}, {eTB + 1, 0},
  {mP + 3, 0}, {eW + 6, 4}, {eTB + 2, 0},
  {mP + 4, 0}, {eWF, 1}, {eTB + 3, 0}, {eWL, 1}, {mP + 5, 0}, {mEnd, 0}};
constexpr Ev SEQ_T[] = {
  {mP + 13, 0}, {eDV, 0}, {eD + 0, 4}, {eD + 1, 4}, {eD + 2, 4}, {eD + 3, 4}, {eHF, 1}, {eDR + 0, 0}, {eD + 4, 4}, {eD + 5, 4}, {eDR + 1, 0}, {eD + 6, 4}, {eD + 7, 4},
  {eFL1, 1}, {eP1, 0},
  {eH + 0, 1}, {eH + 1, 1}, {eH + 2, 1}, {eH + 3, 1}, {eH + 4, 1}, {eH + 5, 1}, {eH + 6, 1}, {eH + 7, 1},
  {eH + 8, 1}, {eH + 9, 1}, {eH + 10, 1}, {eH + 11, 1}, {eH + 12, 1}, {eH + 13, 1}, {eH + 14, 1}, {eH + 15, 1}, {eDR + 2, 0}, {eD + 8, 4}, {eD + 9, 4},
  {mK + 0, 0}, {mK + 1, 0}, {mK + 2, 0}, {mK + 3, 0}, {mK + 4, 0}, {mK + 5, 0}, {mK + 6, 0}, {mK + 7, 0}, {eDR + 3, 0}, {eD + 10, 4}, {eD + 11, 4},
  {mK + 8, 0}, {mK + 9, 0}, {mK + 10, 0}, {mK + 11, 0}, {mK + 12, 0}, {mK + 13, 0}, {mK + 14, 0}, {mK + 15, 0}, {eDR + 4, 0}, {eD + 12, 4}, {eD + 13, 4}, {eFL2, 1}, {eDR + 5, 0}, {eP2, 0},
  {eH + 16, 1}, {eH + 17, 1}, {eH + 18, 1}, {eH + 19, 1}, {eH + 20, 1}, {eH + 21, 1}, {eH + 22, 1}, {eH + 23, 1}, {eH + 24, 1}, {eH + 25, 1}, {eH + 26, 1}, {eH + 27, 1},
  {eDR + 6, 0},
  {mK + 16, 0}, {mK + 17, 0}, {mK + 18, 0}, {mK + 19, 0}, {mK + 20, 0}, {mK + 21, 0}, {mK + 22, 0}, {mK + 23, 0},
  {mK + 24, 0}, {mK + 25, 0}, {mK + 26, 0}, {mK + 27, 0}, {mEnd, 0}};
constexpr Ev SEQ_C[] = {PRO0, {eX + 3, 4}, {mX + 0, 0}, {eEP, 1}, {eX + 4, 4}, {mX + 1, 0}, {eX + 5, 4}, {mX + 2, 0}, {eX + 6, 4}, {mX + 3, 0}, {eW + 0, 4},
  {mX + 4, 0}, {eW + 1, 4}, {mX + 5, 0}, {eW + 2, 4}, {mX + 6, 0}, {mP + 0, 0}, {mEnd, 0}};
template <int R> constexpr Ev seq(int i) { return R == 0 ? SEQ_R[i] : R == 1 ? SEQ_B[i] : R == 2 ? SEQ_T[i] : SEQ_C[i]; }
template <int R> constexpr int sidx(int id) { int i = 0; while (seq<R>(i).id != id && seq<R>(i).id != mEnd) ++i; return i; }
template <int R> constexpr bool has(int id) { return seq<R>(sidx<R>(id)).id == id; }
template <int R> constexpr int pos(int id) { int p = has<R>(id) ? 0 : 1000; for (int i = 0; i <= sidx<R>(id); ++i) p += seq<R>(i).n; return p; }
template <int R> constexpr int before(int id) { return pos<R>(id) - seq<R>(sidx<R>(id)).n; }
template <int R> constexpr int seglen(int i0) { int n = 0; while (seq<R>(i0 + n).id < mX) ++n; return n; }
constexpr int cmax(int a, int b) { return a > b ? a : b; }

DI float dot2(unsigned a, unsigned b, float c) { return __builtin_amdgcn_fdot2_f32_bf16(__builtin_bit_cast(bf16x2, a), __builtin_bit_cast(bf16x2, b), c, false); }
typedef u32x4 srd_t;
typedef __attribute__((ext_vector_type(16))) unsigned u32x16;
template <int I> DI srd_t srd_of(u32x16 a) { return __builtin_shufflevector(a, a, 4 * I, 4 * I + 1, 4 * I + 2, 4 * I + 3); }
template <int L> DI void dma2(unsigned lw, unsigned v0, srd_t r0, unsigned v1, srd_t r1, unsigned so) {
  asm volatile("s_add_u32 m0, %0, %1\n\ts_nop 0\n\tbuffer_load_dwordx4 %2, %3, %6 offen lds\n\tbuffer_load_dwordx4 %4, %5, %6 offen offset:1024 lds"
               ::"s"(lw), "n"(L), "v"(v0), "s"(r0), "v"(v1), "s"(r1), "s"(so) : "memory", "scc");
}
template <int L> DI void dma4(unsigned lw, unsigned v0, unsigned v1, unsigned v2, unsigned v3, srd_t r0, srd_t r1, unsigned so) {
  asm volatile("s_add_u32 m0, %0, %1\n\ts_nop 0\n\tbuffer_load_dwordx4 %2, %6, %8 offen lds\n\tbuffer_load_dwordx4 %3, %7, %8 offen offset:1024 lds\n\t"
               "buffer_load_dwordx4 %4, %6, %8 offen offset:2048 lds\n\tbuffer_load_dwordx4 %5, %7, %8 offen offset:3072 lds"
               ::"s"(lw), "n"(L), "v"(v0), "v"(v1), "v"(v2), "v"(v3), "s"(r0), "s"(r1), "s"(so) : "memory", "scc");
}
template <int L> DI void dma4_sc1(unsigned lw, unsigned v, srd_t r, unsigned so) {
  asm volatile("s_add_u32 m0, %0, %1\n\ts_nop 0\n\tbuffer_load_dwordx4 %2, %3, %4 offen sc1 lds\n\tbuffer_load_dwordx4 %2, %3, %4 offen offset:1024 sc1 lds\n\t"
               "buffer_load_dwordx4 %2, %3, %4 offen offset:2048 sc1 lds\n\tbuffer_load_dwordx4 %2, %3, %4 offen offset:3072 sc1 lds"
               ::"s"(lw), "n"(L), "v"(v), "s"(r), "s"(so) : "memory", "scc");
}
template <int OFF> DI void ld_sc1(u32x4& d, unsigned voff, srd_t r, unsigned so) { asm volatile("buffer_load_dwordx4 %0, %1, %2, %3 offen offset:%4 sc1" : "=v"(d) : "v"(voff), "s"(r), "s"(so), "n"(OFF) : "memory"); }
template <int OFF> DI void ld_sc1_over(u32x4& d, unsigned voff, srd_t r, unsigned so) { asm volatile("buffer_load_dwordx4 %0, %1, %2, %3 offen offset:%4 sc1" : "+v"(d) : "v"(voff), "s"(r), "s"(so), "n"(OFF) : "memory"); }
DI void ld_pl(u32x4& d, unsigned voff, srd_t r, unsigned so) { asm volatile("buffer_load_dwordx4 %0, %1, %2, %3 offen" : "=v"(d) : "v"(voff), "s"(r), "s"(so) : "memory"); }
DI void ld_u32_sc1(unsigned& d, unsigned voff, srd_t r, unsigned so) { asm volatile("buffer_load_dword %0, %1, %2, %3 offen sc1" : "=v"(d) : "v"(voff), "s"(r), "s"(so) : "memory"); }
DI void ld_flag(u64& d, unsigned voff, srd_t r) { asm volatile("buffer_load_dwordx2 %0, %1, %2, %3 offen sc1" : "=v"(d) : "v"(voff), "s"(r), "s"(OFF_C) : "memory"); }
template <int OFF = 0> DI void st16_sc1(u32x4 v, unsigned voff, srd_t r, unsigned so) {
  asm volatile("buffer_store_dwordx4 %0, %1, %2, %3 offen offset:%4 sc1\n\ts_nop 1" ::"v"(v), "v"(voff), "s"(r), "s"(so), "n"(OFF) : "memory");
}
DI void st_flag1(srd_t r, unsigned so, u64 tag) {
  asm volatile("s_mov_b64 exec, 1\n\tbuffer_store_dwordx2 %0, off, %1, %2 sc1\n\ts_mov_b64 exec, -1" ::"v"(tag), "s"(r), "s"(so) : "memory");
}
template <class T> DI void pin(T& a) { asm volatile("" : "+v"(a)); }
template <class T> DI void pins(T& a) { asm volatile("" : "+s"(a)); }
template <class T> DI void opq(T& a) { asm("" : "+v"(a)); }
DI unsigned selm(u64 m, unsigned a, unsigned b) { unsigned r; asm("v_cndmask_b32_e64 %0, %2, %1, %3" : "=v"(r) : "v"(a), "v"(b), "s"(m)); return r; }
DI unsigned selz(u64 m, unsigned a) { unsigned r; asm("v_cndmask_b32_e64 %0, 0, %1, %2" : "=v"(r) : "v"(a), "s"(m)); return r; }
template <int N> DI void vbar() {
  static_assert(N >= 0, "wait");
  asm volatile("s_waitcnt vmcnt(%0) lgkmcnt(0)\n\ts_barrier" ::"n"(N > 63 ? 63 : N) : "memory");
}
template <int N> DI void wvm() { static_assert(N >= 0, "wait"); wait_vm<(N > 63 ? 63 : N)>(); }
DI void poll(u64& fv, u64 mask, unsigned voff, srd_t sb, u64 tag) {
  if ((~__builtin_amdgcn_ballot_w64(fv == tag) & mask) != 0) {
#pragma unroll 1
    for (int it = 0; it < SPIN_CAP; ++it) {
      __builtin_amdgcn_s_sleep(1);
      ld_flag(fv, voff, sb); wait_vm<0>(); pin(fv);
      if ((~__builtin_amdgcn_ballot_w64(fv == tag) & mask) == 0) break;
    }
  }
  asm volatile("" ::: "memory");
}
typedef __attribute__((address_space(3))) char* lds_p;
template <class T, int O> DI T lds_ld(unsigned a) { return *(__attribute__((address_space(3))) const T*)((lds_cp)(uintptr_t)a + O); }
template <class T, int O> DI void lds_st(unsigned a, T v) { *(__attribute__((address_space(3))) T*)((lds_p)(uintptr_t)a + O) = v; }
template <int I> DI void arrive(unsigned lds0) {
  asm volatile("s_waitcnt lgkmcnt(0)\n\ts_mov_b64 exec, 1\n\tds_add_u32 %0, %1 offset:%2\n\ts_mov_b64 exec, -1" ::"v"(lds0 + L_CT), "v"(1u), "n"(4 * I) : "memory");
}
template <int I> DI void await4(unsigned lds0) {
#pragma unroll 1
  for (int it = 0; it < SPIN_CAP * 8; ++it) {
    unsigned c;
    asm volatile("ds_read_b32 %0, %1 offset:%2\n\ts_waitcnt lgkmcnt(0)" : "=v"(c) : "v"(lds0 + L_CT), "n"(4 * I) : "memory");
    if (__builtin_amdgcn_readfirstlane(c) >= 4) break;
    __builtin_amdgcn_s_sleep(1);
  }
}
template <int N> DI void trig(float a, float d, float k, float m, float* c, float* s) {
  c[0] = vcos(a) * m; s[0] = vsin(a) * m; c[1] = vcos(a + d) * m; s[1] = vsin(a + d) * m;
#pragma unroll
  for (int j = 2; j < N; ++j) { c[j] = __builtin_fmaf(k, c[j - 1], -c[j - 2]); opq(c[j]); s[j] = __builtin_fmaf(k, s[j - 1], -s[j - 2]); opq(s[j]); }
}
DI void put_part(unsigned a, int l, const f32x16& c0, const f32x16& c1) {
  sfor<8>([&](auto K) { constexpr int k = K; const f32x16& c = k < 4 ? c0 : c1;
    lds_st<f32x4, k * 1024>(a + (l >> 5) * 512 + (((l & 31) ^ (2 * k)) * 16), f32x4{c[4 * (k & 3)], c[4 * (k & 3) + 1], c[4 * (k & 3) + 2], c[4 * (k & 3) + 3]});
  });
}

#define BUF(n) const void* n##p, unsigned n##b, unsigned n##f
__global__ __launch_bounds__(512, 2) void fwd(BUF(x), BUF(g), BUF(u), BUF(w), BUF(d), BUF(a), BUF(c), BUF(o) STAMP_ARG) {
  extern __shared__ __attribute__((aligned(16))) char smem[];
  u32x16 ka, kb;
  unsigned b = blockIdx.x, tid = threadIdx.x;
  STAMP_BEGIN
  {
    u64 pc; unsigned t, pf;
    asm volatile("s_load_dwordx16 %0, %7, 0\n\ts_load_dwordx16 %1, %7, 64\n\ts_getpc_b64 %2\n.Lpf_here:\n\ts_lshr_b32 %3, %5, 3\n\ts_lshl_b32 %3, %3, 11\n\t"
                 "v_lshl_add_u32 %4, %6, 2, %3\n\ts_mov_b32 m0, 0\n\tv_min_u32 %4, .Lpf_end-.Lpf_here, %4\n\tglobal_load_lds_dword %4, %2"
                 : "=&s"(ka), "=&s"(kb), "=&s"(pc), "=&s"(t), "=&v"(pf), "+s"(b), "+v"(tid) : "s"(__builtin_amdgcn_kernarg_segment_ptr()) : "memory", "scc");
  }
  SB();
  const int w = __builtin_amdgcn_readfirstlane(tid >> 6), ws = w & 3, wn = w >> 2;
  const int xcd = b & 7, idx = b >> 3;
  const unsigned lds0 = lds_addr(smem), lw = lds0 + w * 2048;
  const int rb = (xcd & 1) * 8 + (idx & 7), g = xcd >> 1, ii = idx >> 3;
  const int r0 = (unsigned)((idx & 7) + 2 * ii) % 7;
  const int unit1 = 4 * g + ii, unit2 = 16 + 3 * g + ii;
  unsigned xvo[2];
  auto mk_xv = [&](unsigned t) {
    xvo[0] = (unsigned)(128 * rb + 32 * ((t >> 3) & 3) + ((t >> 4) & 28) + ((t >> 5) & 1)) * (D_ * 2) + ((t ^ ((t >> 4) & 3)) & 7) * 16;
    xvo[1] = (xvo[0] ^ 64) + (2 * D_ * 2 - 1024);
    pin(xvo[0]); pin(xvo[1]);
  };
  mk_xv(tid);
  unsigned xs0 = r0 * 128; pins(xs0);
  auto t1gen = [&](int s) {
    const int l = lane_id(), hh = l >> 5, fi = ((l >> 1) & 9) | (l & 4) | ((l >> 2) & 2), f = (15 * unit1 + fi) & ((fi - 15) >> 31);
    const float cf_ = (float)f * (1.0f / 448), a0 = (float)(16 * s + 8 * hh) * cf_ + (float)(l & 1) * 0.25f;
    const unsigned sg = (unsigned)(f & 1) * 0x80008000u, a = lds0 + L_T1 + s * 1024 + l * 16;
    u32x4 o;
#pragma unroll
    for (int e = 0; e < 4; ++e) o[e] = pk2(vcos(a0 + (float)(2 * e) * cf_), vcos(a0 + (float)(2 * e + 1) * cf_));
    lds_st<u32x4, 0>(a, o); lds_st<u32x4, 14336>(a, o ^ sg);
  };
  t1gen(w);
  SB();
  asm volatile("s_waitcnt lgkmcnt(0)" : "+s"(ka), "+s"(kb));
  const srd_t rx = srd_of<0>(ka), rg = srd_of<1>(ka), rw = srd_of<3>(ka), rd = srd_of<0>(kb), ra = srd_of<1>(kb), rc = srd_of<2>(kb), ro = srd_of<3>(kb);
  STX(50)
  dma2<SL>(lw, xvo[0], rx, xvo[1], rx, xs0);
  STX(51)
  SB();
  srd_t ru = srd_of<2>(ka);
  { const u64 p = ((u64)ru[1] << 32 | ru[0]) - 1024; ru[0] = (unsigned)p; ru[1] = (unsigned)(p >> 32); }
  const bool bcu = ii == 3;
  auto kof = [&](int st) { int k = st + r0; return k >= 7 ? k - 7 : k; };
  constexpr unsigned CH = 64 * F_ * 2;
  const int rt = 4 * xcd + (idx & 3), dt = bcu ? 6 : (idx >> 2);
  const int rot = dt + (idx & 3);
  const int rot3 = (unsigned)rot % 3;
  auto rnd = [&](int i) { if (i < 4) return (i + rot) & 3; const int k = i - 4 + rot3; return 4 + (k >= 3 ? k - 3 : k); };
  const int rtb = 2 * rt + (bcu ? (idx >> 2) & 1 : 0);
  const int gi = b & 63;
  const f32x4 zero = {0.f, 0.f, 0.f, 0.f};
  const f32x16 zero16 = {};

  u64 ep, fv, tag;
  unsigned lvo, wvo, wo, xo[4], gvo, ovo, r2u, hso, wv1, dv[4], ao;
  float sq[2], t1c[8], t1s[8], u2c[8], u2s[8];
  u32x4 xres, fl[2], wdf[7][8], hf[28][2], C64[2], S64[2], M1[4], M2[4], C32[2], S32[2], N32[2];
  f32x16 accA, ah[2][2];
  auto res = [&](int R, int u) {
    const int l = lane_id(), pp = 32 * (rtb & 3) + 8 * (R ? ws : w) + (l >> 3);
    ld_u32_sc1(r2u, (unsigned)(pp * 16), rw, OFF_Z + 12 + (4 * u + 3) * 32768 + (rt >> 1) * 2048);
    ovo = (unsigned)((128 * (rt >> 1) + 32 * (pp & 3) + (pp >> 2)) * D_ + dt * 64 + (l & 7) * 8) * 2;
    ld_pl(xres, ovo, rx, 0);
  };
  auto ev = [&](auto Rc, auto Ic) { constexpr int R = Rc, id = Ic;
    if constexpr (id >= eX && id < eX + 7) { constexpr int j = id - eX;
      if constexpr (j > 2) dma4<(j + 1) * SL>(lds0 + ws * 4096, xvo[0], xvo[1], dv[0], dv[1], rx, rx, kof(j) * 128);
      else if constexpr (j > 0) dma2<(j + 1) * SL>(lw, xvo[0], rx, xvo[1], rx, kof(j) * 128);
    } else if constexpr (id >= eW && id < eW + 14) { constexpr int j = id - eW;
      dma4<(j % 5) * SL>(lds0 + ws * 4096, wvo, wvo, wv1, wv1, rg, ru, kof(j % 7) * CH + (j >= 7 ? (unit2 - unit1) * 128 : 0));
    } else if constexpr (id >= eD && id < eD + 14) { constexpr int i = id - eD;
      if constexpr (i >= 4 && !(i & 1)) asm volatile("s_waitcnt lgkmcnt(0)" ::: "memory");
      dma4<(R ? 0 : 5 * SL) + (i & 3) * 4096>(lds0 + ws * SL, dv[0], dv[1], dv[2], dv[3], rd, rd, rnd(i >> 1) * CH + (i & 1) * (32 * D_ * 2));
    } else if constexpr (id >= eDR && id < eDR + 7) { constexpr int i = id - eDR;
      if constexpr (has<R>(eD + 2 * i + 1)) wvm<pos<R>(id) - pos<R>(eD + 2 * i + 1)>();
      unsigned a1 = ao ^ 64; pin(a1);
      sfor<8>([&](auto K) { constexpr int o = (i & 1) * 8192 + (K >> 1) * 2048; wdf[i][K] = tr_at<o, o + 1024>((K & 1) ? a1 : ao); });
    } else if constexpr (id >= eH && id < eH + 28) { constexpr int s = id - eH;
      const unsigned so = (rtb * 28 + ws) * 4096 + rnd(s >> 2) * 16384;
      if constexpr (R) ld_sc1<(s & 3) * 1024>(hf[s][0], lvo, rw, so);
      else sfor<2>([&](auto C) { constexpr int c = C, k = (s - 5) >> 1;
        if constexpr (s < 5) ld_sc1<(s & 3) * 1024>(hf[s][c], lvo, rw, so + c * 28 * 4096);
        else {
          if constexpr (s >= 26) hf[s][c] = wdf[3][2 * (s - 26) + c];
          else if constexpr (s >= 16) hf[s][c] = hf[s - 10][c];
          else if constexpr (s & 1) hf[s][c] = hf[k][c];
          else hf[s][c] = wdf[k >> 2][2 * (k & 3) + c];
          ld_sc1_over<(s & 3) * 1024>(hf[s][c], lvo, rw, so + c * 28 * 4096);
        }
      });
    } else if constexpr (id == eEP) {
      ld_flag(ep, 0u, rw);
    } else if constexpr (id == eT1) {
      STX(52)
      lds_st<u32x4, 0>(lds0 + L_CT, u32x4{0u, 0u, 0u, 0u});
      if (w >= 2) t1gen(w + 6);
      STX(53)
    } else if constexpr (id == ePRE) {
      STX(54)
      const int l = lane_id(), r8 = l >> 3, ch = l & 7, r32 = l & 31, hh = l >> 5;
      lvo = (unsigned)l * 16;
      if constexpr (R == 3) {
        mk_xv(128 * ws + l); dv[0] = xvo[0] + (4 * D_ * 2 - 2048); dv[1] = xvo[1] + (4 * D_ * 2 - 2048); pin(dv[0]); pin(dv[1]);
        wo = lds0 + hh * 2048 + ((l & 15) >> 2) * 128 + (r8 & 1) * 64 + ((l >> 4) & 1) * 32 + (l & 3) * 8; pin(wo);
        ao = ONES & (unsigned)((((l >> 2) ^ (l >> 4)) & 1) - 1); pin(ao);
      }
      wvo = (unsigned)((16 * ws + r8) * F_ * 2 + (ch ^ ((r8 & 2) << 1)) * 16 + unit1 * 128); wv1 = wvo + (8 * F_ * 2 - 2048); pin(wvo); pin(wv1);
#pragma unroll
      for (int i = 0; i < 4; ++i) { xo[i] = lds0 + SL + (32 * ws + r32) * 128 + (((2 * i + hh) ^ ((r32 >> 1) & 7)) * 16); pin(xo[i]); }
      pin(lvo);
    } else if constexpr (id == eDV) {
      const int l = lane_id();
      dv[0] = (unsigned)((64 * ws + (l >> 3)) * D_ * 2 + dt * 128 + (((l & 7) ^ ((l >> 2) & 4)) * 16));
#pragma unroll
      for (int i = 1; i < 4; ++i) { dv[i] = dv[0] + i * (8 * D_ * 2 - 1024); pin(dv[i]); }
      ao = lds0 + ((R ? 0 : 5) + ws) * SL + (4 * (l >> 5) + ((l & 15) >> 2)) * 128 + ((l >> 3) & 1) * 64 + ((l >> 4) & 1) * 32 + (l & 3) * 8;
      pin(ao);
    } else if constexpr (id == eZ) {
      const unsigned su = __float_as_uint(accA[14]), qu = __float_as_uint(sq[0] + sq[1]);
      const float sm = __uint_as_float(__builtin_amdgcn_permlane32_swap(su, su, false, false)[1]);
      const u32x2 q2 = __builtin_amdgcn_permlane32_swap(qu, qu, false, false);
      const float mean = sm * (1.0f / D_);
      const unsigned nm = pk2(-mean, -mean);
      const float rs = __builtin_amdgcn_rcpf(rbf(__builtin_amdgcn_sqrtf(rbf(rbf(fmaxf(__uint_as_float(q2[0]) + __uint_as_float(q2[1]) - sm * mean, 0.f) * (1.0f / D_)) + 1e-5f))));
      const u64 m0 = unit1 == 0 ? 0xffffffffull : 0ull, m6 = unit1 == 14 ? ~0xffffffffull : 0ull;
      accA[0] += __uint_as_float(selz(m0, nm << 16)) * 448.f;
      const unsigned rh = __float_as_uint(0.5f * rs);
      const unsigned zv = (lvo & 511) + (lvo & 512) * 64;
      u32x4 t;
      sfor<8>([&](auto E) { constexpr int e = E;
        const float r = e == 0 ? __uint_as_float(selm(m0, rh, __float_as_uint(rs))) : e == 6 ? __uint_as_float(selm(m6, rh, __float_as_uint(rs))) : rs;
        float p = accA[2 * e] * r, q = accA[2 * e + 1] * r; opq(p); opq(q);
        t[e & 3] = pk2(p, q);
        if constexpr (e == 7) t[3] = selm(0xffffffffull, t[3], __float_as_uint(rs * rs));
        if constexpr ((e & 3) == 3) st16_sc1(t, zv, rw, OFF_Z + unit1 * 131072 + (e >> 2) * 65536 + rb * 2048 + ws * 512);
      });
    } else if constexpr (id == eF) {
      const int l = lane_id(), fc = idx & 7;
      const unsigned fo = (unsigned)(((l >> 4) * 256 + ((l >> 2) & 3)) * DF_ + min(30 * fc - (fc == 7) + 8 * (l & 3), DF_ - 8)) * 2;
      const unsigned fs = (unsigned)(16 * ii + 4 * ws + 64 * (16 * (xcd >> 2) + (xcd & 3))) * DF_ * 2;
      ld_pl(fl[0], fo, ra, fs); ld_pl(fl[1], fo, rc, fs);
    } else if constexpr (id == eFA) {
      wvm<pos<R>(eFA) - pos<R>(eF)>(); pin(fl[0]); pin(fl[1]);
      const unsigned a = lvo + (lds0 + L_P + ws * 2048); lds_st<u32x4, 0>(a, fl[0]); lds_st<u32x4, 1024>(a, fl[1]);
    } else if constexpr (id == eFB) {
      const unsigned a = ((lvo & 192) << 2) + ((lvo >> 2) & 192) + ((lvo >> 1) & 24) + (lds0 + L_P + ws * 2048);
      fl[0] = tr_at<0, 1024>(a); fl[1] = tr_at<32, 1056>(a);
    } else if constexpr (id == eFC) {
      const unsigned a = ((lvo & 240) << 9) + ((lvo >> 4) & 48), so = OFF_W + (idx & 7) * 262144 + ii * 2048 + xcd * 256 + ws * 64;
      st16_sc1(fl[0], a, rw, so); st16_sc1(fl[1], a, rw, so + 131072);
    } else if constexpr (id == eZF) {
      wvm<before<R>(eZF) - pos<R>(eZ)>();
      st_flag1(rw, OFF_C + (FZ0 + unit1 * 64 + rb * 4 + ws) * 8, tag);
    } else if constexpr (id == eWF) {
      wvm<before<R>(eWF) - pos<R>(eFC)>();
      st_flag1(rw, OFF_C + (FW0 + (idx & 7) * 128 + ii * 32 + xcd * 4 + ws) * 8, tag);
    } else if constexpr (id == eWL) {
      gvo = selm(0xffffffffull, (FW0 + (gi >> 3) * 128 + 32 * ws) * 8 + (lvo >> 1), (FZ0 + (gi >> 2) * 64 + 16 * ws) * 8 + ((lvo >> 1) & 127));
      ld_flag(fv, gvo, rw);
    } else if constexpr (id >= eTB && id < eTB + 4) { constexpr int j = id - eTB;
      const int l = lane_id(), v = l & 15, q = l >> 4, vp = 4 * (v & 3) + (v >> 2);
      float c[8], s[8];
      if constexpr (j == 0) {
        const float d = (float)(16 * ws + v) * (1.0f / 64);
        const unsigned sg = (v & 1) ? 0x80008000u : 0u;
        trig<8>((float)(8 * q) * d, d, 2.f * vcos(d), 1.f, c, s);
#pragma unroll
        for (int e = 0; e < 4; ++e) {
          C64[0][e] = pk2(c[2 * e], c[2 * e + 1]); S64[0][e] = pk2(s[2 * e], s[2 * e + 1]);
          M1[0][e] = pk2(c[e], s[e]); M1[1][e] = pk2(c[4 + e], s[4 + e]); M2[0][e] = pk2(-s[e], c[e]); M2[1][e] = pk2(-s[4 + e], c[4 + e]);
        }
        C64[1] = C64[0] ^ sg; S64[1] = S64[0] ^ sg;
        pin(C64[0]); pin(C64[1]); pin(S64[0]); pin(S64[1]); pin(M1[0]); pin(M1[1]); pin(M2[0]); pin(M2[1]);
      } else if constexpr (j == 1) {
        const float d = (float)vp * (1.0f / 32);
        const unsigned sg = (q & 1) ? 0x80008000u : 0u;
        trig<8>((float)q * d, 4.f * d, 2.f * vcos(4.f * d), 1.f, c, s);
#pragma unroll
        for (int e = 0; e < 4; ++e) { C32[0][e] = pk2(c[2 * e], c[2 * e + 1]); S32[0][e] = pk2(s[2 * e], s[2 * e + 1]); }
        C32[1] = C32[0] ^ sg; S32[1] = S32[0] ^ sg; N32[0] = S32[0] ^ 0x80008000u; N32[1] = N32[0] ^ sg;
        sfor<2>([&](auto T) { pin(C32[T]); pin(S32[T]); pin(N32[T]); });
      } else if constexpr (j == 2) {
        const float d = (float)(16 * ws + v) * (1.0f / 2048);
        const unsigned sg = (v & 1) ? 0x80008000u : 0u;
        trig<8>((float)q * d, 4.f * d, 2.f * vcos(4.f * d), 2.0f / (448.0f * 2048.0f), t1c, t1s);
        sfor<8>([&](auto I) { pin(t1c[I]); pin(t1s[I]); });
        sfor<2>([&](auto I) { M1[2 + I] = M1[I] ^ sg; M2[2 + I] = M2[I] ^ sg; pin(M1[2 + I]); pin(M2[2 + I]); });
      } else {
        const float k = (float)(16 * ws + 4 * q);
        sfor<8>([&](auto I) { constexpr int t = I >> 2, r = I & 3;
          const float a = (k + (float)r) * ((float)(16 * t + vp) * (1.0f / 2048)); u2c[I] = vcos(a); u2s[I] = vsin(a); pin(u2c[I]); pin(u2s[I]); });
      }
    } else if constexpr (id == eHF) {
      wvm<has<R>(eHF) ? before<R>(eHF) : 0>(); st_flag1(rw, OFF_C + FH0 * 8 + (hso >> 9), tag);
    } else if constexpr (id == eFL1) {
      const int l = lane_id(), i = l < 14 ? l >> 1 : 0;
      gvo = (unsigned)(FH0 + (rtb + (R ? 0 : l & 1)) * 28 + 4 * i + ws) * 8;
      ld_flag(fv, gvo, rw);
    } else if constexpr (id == eFL2) { ld_flag(fv, gvo, rw);
    } else if constexpr (id == eP1) { wvm<pos<R>(eP1) - pos<R>(eFL1)>(); pin(fv); ST(5) poll(fv, 0xffull, gvo, rw, tag); ST(6)
    } else if constexpr (id == eP2) { wvm<pos<R>(eP2) - pos<R>(eFL2)>(); pin(fv); ST(8) poll(fv, 0x3f00ull, gvo, rw, tag); ST(9)
    } else if constexpr (id == eRES) { res(0, ws);
    }
  };
  auto run = [&](auto Rc, auto Mc) { constexpr int R = Rc, mk = Mc, i0 = mk < 0 ? 0 : sidx<R>(mk) + 1;
    sfor<seglen<R>(i0)>([&](auto K) { ev(Rc, ic<seq<R>(i0 + K).id>{}); SB(); });
  };
  auto mk_tag = [&] { pin(ep);
    tag = (((u64)__builtin_amdgcn_readfirstlane((unsigned)(ep >> 32)) << 32) | (unsigned)__builtin_amdgcn_readfirstlane((unsigned)ep)) + 1; };
  auto tail_h = [&](auto Rc) { constexpr int R = Rc;
    run(Rc, ic<mP + 13>{});
    sfor<28>([&](auto Sc) { constexpr int s = Sc;
      wvm<pos<R>(mK + s) - pos<R>(eH + s)>();
      if constexpr (s == 0) { ST(7) }
      if constexpr (s == 16) { ST(10) }
      sfor<R ? 2 : 4>([&](auto I) { constexpr int a = I & 1, c = I >> 1;
        if constexpr (!a) pin(hf[s][c]);
        ah[c][a] = mfma32(wdf[s >> 2][2 * (s & 3) + a], hf[s][c], s == 0 ? zero16 : ah[c][a]); pin(ah[c][a]); });
      SB();
      run(Rc, ic<mK + s>{});
    });
    ST(11)
    const int l = lane_id();
    put_part(lds0 + ((R ? 0 : 5) + ws) * SL, l, ah[0][0], ah[0][1]);
    if constexpr (!R) { put_part(lds0 + (5 + ws) * SL + 8192, l, ah[1][0], ah[1][1]); pin(xres); pin(r2u); }
  };
  auto tail_c = [&](auto Rc, auto&& mid) { constexpr int R = Rc, NK = R ? 8 : 16;
    const int l = lane_id(), kq = R ? ws : ws >> 1;
    u32x4 t2[NK][2], yf[NK];
    const float c0 = (float)((2 * kq + (l >> 5)) * (15 * NK / 4));
    float dd[2], a0[2], k2[2];
#pragma unroll
    for (int t = 0; t < 2; ++t) { dd[t] = (float)(64 * dt + 32 * t + (l & 31)) * (1.0f / 448); a0[t] = __builtin_amdgcn_fractf(c0 * dd[t]); k2[t] = 2.f * vcos(dd[t]); }
    sfor<NK / 4>([&](auto Kc) { constexpr int k = Kc;
#pragma unroll
      for (int t = 0; t < 2; ++t) {
        float c[15], s[15];
        trig<15>((float)(15 * k) * dd[t] + a0[t], dd[t], k2[t], 1.f, c, s);
#pragma unroll
        for (int e = 0; e < 16; ++e) t2[4 * k + (e >> 2)][t][e & 3] = e < 15 ? pk2(c[e % 15], s[e % 15]) : 0u;
#pragma unroll
        for (int e = 0; e < 4; ++e) pin(t2[4 * k + e][t]);
      }
      SB();
      if constexpr (k == 1 - R) { mid(); SB(); }
      if constexpr (k == NK / 4 - 2) { gvo = (unsigned)(FY0 + (rt >> 3) * 64 + 2 * NK * kq + (l & (2 * NK - 1))) * 8; ld_flag(fv, gvo, rw); SB(); }
    });
    wait_vm<0>(); pin(fv);
    ST(8)
    poll(fv, ~0ull, gvo, rw, tag);
    ST(9)
    const unsigned yo = OFF_Y + (rtb + (R ? 0 : ws & 1)) * 512, yv = (unsigned)((l >> 5) * (NK * 32768) + (l & 31) * 16);
    sfor<NK>([&](auto I) { ld_sc1<0>(yf[I], yv, rw, yo + (2 * NK * kq + I) * 32768); });
    res(R, NK / 2 * kq);
    f32x16 ac[2];
    sfor<NK>([&](auto I) { constexpr int i = I;
      wait_vm<NK + 1 - i>(); pin(yf[i]);
      if constexpr (i == 0) { ST(10) }
      ac[0] = mfma32(t2[i][0], yf[i], i ? ac[0] : zero16); ac[1] = mfma32(t2[i][1], yf[i], i ? ac[1] : zero16);
      SB();
    });
    ST(11)
    put_part(lds0 + (R ? 5 * SL : 0) + ws * 8192, l, ac[0], ac[1]);
    wait_vm<0>(); pin(xres); pin(r2u);
  };

  auto finish = [&](auto Bc, auto Wc) { constexpr int B = Bc, WN = Wc;
    bar();
    if constexpr (!B) { ST(12) }
    if constexpr (!B || WN == 1) {
      const int l = lane_id(), ch = l & 7;
      const unsigned q = lds0 + ch * 1024 + (((8 * ws + (l >> 3)) ^ (2 * ch)) * 16), qh = q + (B ? 0 : 5 * SL + WN * 8192), qc = q + (B ? 5 * SL : WN * 8192);
      f32x4 a0 = lds_ld<f32x4, 0>(qh), a1 = lds_ld<f32x4, 512>(qh), c0 = lds_ld<f32x4, 0>(qc), c1 = lds_ld<f32x4, 512>(qc);
      sfor<3>([&](auto I) { a0 += lds_ld<f32x4, (I + 1) * SL>(qh); a1 += lds_ld<f32x4, (I + 1) * SL + 512>(qh); });
      c0 += lds_ld<f32x4, SL>(qc); c1 += lds_ld<f32x4, SL + 512>(qc);
      if constexpr (B) { c0 += lds_ld<f32x4, 8192>(qc) + lds_ld<f32x4, SL + 8192>(qc); c1 += lds_ld<f32x4, 8704>(qc) + lds_ld<f32x4, SL + 8704>(qc); }
      const float r2 = __uint_as_float(r2u);
      u32x4 o;
      o[0] = pk2(a0[0] * r2 + (c0[0] + bflo(xres[0])), a0[1] * r2 + (c0[1] + bfhi(xres[0])));
      o[1] = pk2(a0[2] * r2 + (c0[2] + bflo(xres[1])), a0[3] * r2 + (c0[3] + bfhi(xres[1])));
      o[2] = pk2(a1[0] * r2 + (c1[0] + bflo(xres[2])), a1[1] * r2 + (c1[1] + bfhi(xres[2])));
      o[3] = pk2(a1[2] * r2 + (c1[2] + bflo(xres[3])), a1[3] * r2 + (c1[3] + bfhi(xres[3])));
      asm volatile("buffer_store_dwordx4 %0, %1, %2, 0 offen\n\ts_nop 1" ::"v"(o), "v"(ovo), "s"(ro) : "memory");
    }
    if constexpr (!B && WN == 0) { if (b == 0 && w == 0) st_flag1(rw, OFF_C, tag); }
    if constexpr (B && WN == 1) asm volatile(".Lpf_end:");
    STAMP_END(2 * B + WN)
    __builtin_amdgcn_endpgm();
  };

  run(ic<0>{}, ic<-1>{});
  if (wn == 0) {
    __builtin_amdgcn_s_setprio(3);
    STX(15) run(ic<3>{}, ic<mR>{}); STX(19)
    u32x4 xf[28], wf[RA], eo;
    f32x16 acc[2][4];
    f32x4 sm[2] = {zero, zero};
    sfor<7>([&](auto J) { constexpr int j = J;
      vbar<pos<3>(mX + j) - pos<3>(eX + j)>();
      if constexpr (j == 0) { ST(1) }
      STX(21 + 4 * j)
#pragma unroll
      for (int i = 0; i < 4; ++i) {
        if constexpr (j == 3) { xo[i] += 3 * SL; pin(xo[i]); }
        xf[4 * j + i] = l128_at<(j < 3 ? j : j - 3) * SL>(xo[i]);
      }
      SB();
      run(ic<3>{}, ic<mX + j>{});
      STX(22 + 4 * j)
      if constexpr (j == 4) mk_tag();
#pragma unroll
      for (int i = 0; i < 4; ++i) sm[i & 1] = mfma16(u32x4{ao, ao, ao, ao}, xf[4 * j + i], sm[i & 1]);
      pin(sm[0]); pin(sm[1]);
      SB();
      STX(23 + 4 * j)
    });
    const float nm = rbf((sm[0][0] + sm[1][0]) * (-1.0f / D_));
    auto ctr = [&](auto Dc) { constexpr int d = Dc;
      if constexpr (d < 112) {
        unsigned t = xf[d >> 2][d & 3], u;
        asm volatile("v_lshlrev_b32 %1, 16, %0\n\tv_and_b32 %0, %3, %0\n\tv_add_f32 %1, %1, %2\n\tv_add_f32 %0, %0, %2\n\tv_cvt_pk_bf16_f32 %0, %1, %0"
                     : "+v"(t), "=&v"(u) : "v"(nm), "s"(0xffff0000u));
        xf[d >> 2][d & 3] = t;
      }
    };
    sfor<LEAD>([&](auto I) { ctr(I); });
    STX(48)
    SB();
    hso = ((4 * rb + ws) * 28 + unit1) * 4096;
    float h0;
    auto epi = [&](auto Pc, auto Kc) { constexpr int P = Pc, k = Kc, Q = k >> 4, e = (k >> 1) & 7;
      const float h = fmaxf(acc[P][2 * Q][k & 15], 0.f) * acc[P][2 * Q + 1][k & 15];
      if constexpr (!(k & 1)) { h0 = h; pin(h0); }
      else {
        unsigned t = pk2(h0, h); pin(t);
        eo[e & 3] = t;
        if constexpr ((e & 3) == 3) st16_sc1<Q * 2048 + (e >> 2) * 1024>(eo, lvo, rw, hso);
      }
    };
    unsigned a0, a1;
    auto step = [&](auto Tc, auto Endc) { constexpr int T = Tc, M = T - RA;
      if constexpr (T < Endc && T % 16 == 0) { constexpr int n = T / 16;
        if constexpr (n < 3) asm volatile("s_waitcnt vmcnt(%0)\n\ts_barrier" ::"n"(pos<3>(mP) - pos<3>(eW + n)) : "memory");
        else if constexpr (n == 6) asm volatile("s_cmp_eq_u32 %0, 3\n\ts_cbranch_scc1 .Lnb%=\n\ts_barrier\n.Lnb%=:" ::"s"(ii) : "memory", "scc");
        else asm volatile("s_barrier" ::: "memory");
        if constexpr (n == 0) __builtin_amdgcn_s_setprio(0);
        if constexpr (n == 0) { ST(2) }
        if constexpr (n == 7) { ST(3) }
        pin(wo); a0 = wo + (n % 5) * SL; a1 = a0 ^ 64; pin(a0); pin(a1);
      }
      if constexpr (M >= 0 && M < Endc) { constexpr int n = M / 16, i = M % 16, P = n / 7, q = 2 * (i >> 3) + (i & 1);
        acc[P][q] = mfma32(wf[M % RA], xf[4 * (n % 7) + ((i >> 1) & 3)], (n % 7 == 0 && (i & 6) == 0) ? zero16 : acc[P][q]);
        pin(acc[P][q]);
      }
      SB();
      if constexpr (T < Endc) { constexpr int i = T % 16, o = ((i >> 1) & 3) * 4096 + (i & 1) * 1024; wf[T % RA] = tr_at<o, o + 512>(i >= 8 ? a1 : a0); }
      SB();
      ctr(ic<LEAD + T>{});
      if constexpr (Endc == 224 ? (M >= 105 && M < 137) : M == 105) epi(ic<0>{}, ic<M - 105>{});
      if constexpr (Endc == 112 && M > 105) { epi(ic<0>{}, ic<2 * M - 211>{}); epi(ic<0>{}, ic<2 * M - 210>{}); }
      if constexpr (M >= 217) { epi(ic<1>{}, ic<2 * M - 434>{}); epi(ic<1>{}, ic<2 * M - 433>{}); }
      if constexpr (M == HFAT) { ev(ic<3>{}, ic<eHF>{}); hso += (unit2 - unit1) * 4096; }
      SB();
    };
    sfor<112>([&](auto I) { step(I, ic<224>{}); });
    if (!bcu) {
      sfor<112 + RA>([&](auto I) { step(ic<112 + I>{}, ic<224>{}); });
      sfor<18>([&](auto I) { epi(ic<1>{}, ic<14 + I>{}); });
      ST(4)
      tail_c(ic<0>{}, [&] { ST(5) ev(ic<3>{}, ic<eHF>{}); ST(6) });
      finish(ic<0>{}, ic<0>{});
    } else {
      sfor<RA>([&](auto I) { step(ic<112 + I>{}, ic<112>{}); });
      arrive<1>(lds0);
      ST(3)
      sfor<19>([&](auto I) { epi(ic<0>{}, ic<13 + I>{}); });
      await4<1>(lds0);
      ST(4)
      tail_h(ic<2>{});
      finish(ic<1>{}, ic<0>{});
    }
  } else {
    STX(15) run(ic<0>{}, ic<mR>{}); STX(19)
    accA = zero16; sq[0] = 0.f; sq[1] = 0.f;
    u32x4 xa[2][4], ta[2][4];
    auto dots = [&](auto J) { constexpr int j = J;
#pragma unroll
      for (int e = 0; e < 16; ++e) sq[e & 1] = dot2(xa[j & 1][e >> 2][e & 3], xa[j & 1][e >> 2][e & 3], sq[e & 1]);
      pin(sq[0]); pin(sq[1]);
      SB();
    };
    sfor<7>([&](auto J) { constexpr int j = J;
      vbar<j < 3 ? pos<0>(mX + j) - pos<0>(eX + j) : j == 5 ? pos<0>(mX + j) - pos<0>(eEP) : 63>();
      STX(21 + 4 * j)
      if constexpr (j == 0) { ST(1) }
      if constexpr (j == 3) sfor<4>([&](auto I) { xo[I] += 3 * SL; pin(xo[I]); });
      unsigned to = lvo + (lds0 + L_T1 + kof(j) * 4096); pin(to);
      SB();
      sfor<4>([&](auto I) { constexpr int i = I;
        if constexpr (j > 0) { accA = mfma32(ta[~j & 1][i], xa[~j & 1][i], accA); pin(accA); SB(); }
        xa[j & 1][i] = l128_at<(j < 3 ? j : j - 3) * SL>(xo[i]); ta[j & 1][i] = l128_at<i * 1024>(to);
        SB();
      });
      run(ic<0>{}, ic<mX + j>{});
      STX(22 + 4 * j)
      if constexpr (j == 5) mk_tag();
      if constexpr (j > 0) dots(ic<j - 1>{});
      if constexpr (j == 6) { dots(J); sfor<4>([&](auto I) { accA = mfma32(ta[0][I], xa[0][I], accA); }); pin(accA); }
      STX(23 + 4 * j)
    });
    auto period = [&](auto Rc, auto Nc) { constexpr int R = Rc, n = Nc;
      constexpr int N = n < 3 ? 63 : pos<R>(mP + n) - pos<R>(eW + n + (R && n == 5));
      vbar<N>();
      STP(21 + 3 * n)
      if constexpr (n == 0) { __builtin_amdgcn_s_setprio(2); ST(2) }
      if constexpr (n == 7 - 2 * R) { ST(3) }
      if constexpr (n < 13) run(Rc, ic<mP + n>{});
      STP(22 + 3 * n)
    };
    period(ic<0>{}, ic<0>{});
    if (!bcu) {
      sfor<13>([&](auto I) { period(ic<0>{}, ic<I + 1>{}); });
      ST(4)
      tail_h(ic<0>{});
      finish(ic<0>{}, ic<1>{});
    } else {
      sfor<5>([&](auto I) { period(ic<1>{}, ic<I + 1>{}); });
      wait_vm<0>(); pin(fv);
      ST(4)
      poll(fv, ~0xffffffffull, gvo, rw, tag);
      ST(5)
      const unsigned zo = OFF_Z + gi * 32768 + ws * 8192;
      dma4_sc1<5 * SL>(lds0 + ws * 8192, lvo, rw, zo); dma4_sc1<5 * SL + 4096>(lds0 + ws * 8192, lvo, rw, zo + 4096);
      poll(fv, 0xffffffffull, gvo, rw, tag);
      u32x4 wv[4][2];
      const unsigned wp = OFF_W + ((gi >> 3) * 32 + ((gi >> 2) & 1) * 15 + (gi & 3) * 4 + (gi >= 56)) * 8192 + ws * 2048;
      sfor<4>([&](auto K) { ld_sc1<0>(wv[K][0], lvo, rw, wp + K * 8192); ld_sc1<1024>(wv[K][1], lvo, rw, wp + K * 8192); });
      const int l = lane_id(), v = l & 15, q = l >> 4, vp = 4 * (v & 3) + (v >> 2);
      const unsigned za = lds0 + 5 * SL + q * 4096 + vp * 16, ia = lds0 + 7 * SL + v * 256 + (((4 * ws + q) ^ v) * 16), ib = lds0 + 7 * SL + v * 256 + (((2 * q) ^ v) * 16);
      wait_vm<8>();
      ST(6)
      arrive<2>(lds0); await4<2>(lds0);
      ST(12)
      sfor<2>([&](auto Cp) { constexpr int cp = Cp;
        f32x4 dc[8], ds[8];
        sfor<8>([&](auto J) { constexpr int o = J * 256 + cp * 8;
          const u32x4 z0 = tr_at<o, o + 2048>(za), z1 = tr_at<o + 16384, o + 18432>(za);
          dc[J] = mfma16(z0, C64[0], zero); ds[J] = mfma16(z0, S64[0], zero);
          dc[J] = mfma16(z1, C64[1], dc[J]); ds[J] = mfma16(z1, S64[1], ds[J]);
        });
        if constexpr (cp == 0) { wait_vm<0>(); sfor<8>([&](auto I) { pin(wv[I >> 1][I & 1]); }); }
        sfor<2>([&](auto Cb) { constexpr int c = 2 * cp + Cb, o = 2 * Cb;
          u32x4 fr, fi;
          float a[8], bq[8];
#pragma unroll
          for (int j = 0; j < 8; ++j) {
            float x = dc[j][o] + ds[j][o + 1], y = dc[j][o + 1] - ds[j][o]; opq(x); opq(y);
            a[j] = x * t1c[j]; opq(a[j]); a[j] += y * t1s[j]; opq(a[j]); bq[j] = y * t1c[j]; opq(bq[j]); bq[j] -= x * t1s[j]; opq(bq[j]);
          }
#pragma unroll
          for (int e = 0; e < 4; ++e) { fr[e] = pk2(a[2 * e], a[2 * e + 1]); fi[e] = pk2(bq[2 * e], bq[2 * e + 1]); }
          f32x4 qr[2], qi[2];
#pragma unroll
          for (int t = 0; t < 2; ++t) {
            qr[t] = mfma16(C32[t], fr, zero); qi[t] = mfma16(C32[t], fi, zero);
            qr[t] = mfma16(S32[t], fi, qr[t]); qi[t] = mfma16(N32[t], fr, qi[t]);
          }
#pragma unroll
          for (int j = 0; j < 8; ++j) { const int t = j >> 2, r = j & 3;
            const float wre = (r & 1) ? bfhi(wv[c][t][r >> 1]) : bflo(wv[c][t][r >> 1]), wim = (r & 1) ? bfhi(wv[c][t][2 + (r >> 1)]) : bflo(wv[c][t][2 + (r >> 1)]);
            a[j] = qr[t][r] * wre; opq(a[j]); a[j] -= qi[t][r] * wim; opq(a[j]); bq[j] = qr[t][r] * wim; opq(bq[j]); bq[j] += qi[t][r] * wre; opq(bq[j]);
          }
#pragma unroll
          for (int e = 0; e < 4; ++e) { fr[e] = pk2(a[2 * e], a[2 * e + 1]); fi[e] = pk2(bq[2 * e], bq[2 * e + 1]); }
          sfor<2>([&](auto Tc) { constexpr int t = Tc;
            f32x4 rr = mfma16(fr, C32[t], zero), ri = mfma16(fi, C32[t], zero);
            rr = mfma16(fi, N32[t], rr); ri = mfma16(fr, S32[t], ri);
            u32x4 o4;
#pragma unroll
            for (int r = 0; r < 4; ++r) {
              float x = rr[r] * u2c[4 * t + r]; opq(x); x -= ri[r] * u2s[4 * t + r]; opq(x);
              float y = rr[r] * -u2s[4 * t + r]; opq(y); y -= ri[r] * u2c[4 * t + r]; opq(y);
              o4[r] = pk2(x, y);
            }
            lds_st<u32x4, c * 8192 + t * 4096>(ia, o4);
          });
        });
      });
      arrive<3>(lds0); await4<3>(lds0);
      ST(7)
      const unsigned mz = (unsigned)((gi - 60) >> 31), m3 = mz & (unsigned)(((gi & 3) - 3) >> 31);
      const unsigned yv = (unsigned)((v >> 2) * 2048 + q * 64 + (v & 3) * 16);
      sfor<2>([&](auto Tc) { constexpr int t = Tc;
        f32x4 yr[4], yi[4];
        sfor<4>([&](auto Ks) { constexpr int ks = Ks;
          const unsigned a = ib ^ ((ks >> 1) * 128 + (ks & 1) * 16);
          sfor<4>([&](auto K) {
            const u32x4 rf = l128_at<K * 8192 + t * 4096>(a);
            yr[K] = mfma16(rf, M1[ks], ks == 0 ? zero : yr[K]); yi[K] = mfma16(rf, M2[ks], ks == 0 ? zero : yi[K]);
          });
        });
        sfor<4>([&](auto Rc) { constexpr int R = Rc;
          st16_sc1<t * 1024 + R * 256>(u32x4{mz & pk2(yr[0][R], yi[0][R]), mz & pk2(yr[1][R], yi[1][R]), mz & pk2(yr[2][R], yi[2][R]),
                                             m3 & pk2(yr[3][R], yi[3][R])}, yv, rw, OFF_Y + gi * 32768 + ws * 8192);
        });
      });
      ST(13)
      tail_c(ic<1>{}, [&] { wait_vm<0>(); st_flag1(rw, OFF_C + (FY0 + ws * 64 + gi) * 8, tag); });
      finish(ic<1>{}, ic<1>{});
    }
  }
}

extern "C" void kernel_launch(void* const* d_in, const int* in_sizes, int n_in, void* d_out, int out_size, void* d_ws, size_t ws_size,
                              hipStream_t stream) {
  (void)hipFuncSetAttribute((const void*)fwd, hipFuncAttributeMaxDynamicSharedMemorySize, 163840);
  constexpr unsigned FM = 0x00020000u, NX = S_ * D_ * 2, NW = D_ * F_ * 2, NF = S_ * DF_ * 2;
  fwd<<<256, 512, 163840, stream>>>(d_in[0], NX, FM, d_in[4], NW, FM, d_in[5], NW + 1024, FM, d_ws, 16u << 20, FM,
                                    d_in[6], NW, FM, d_in[1], NF, FM, d_in[2], NF, FM, d_out, NX, FM STAMP_PASS);
}
```

```cpp
#include <hip/hip_runtime.h>
#include <hip/hip_bf16.h>
#include <stdint.h>
#include <utility>

typedef __attribute__((ext_vector_type(4))) short s16x4;
typedef __attribute__((ext_vector_type(8))) __bf16 bf16x8;
typedef __attribute__((ext_vector_type(2))) __bf16 bf16x2;
typedef __attribute__((ext_vector_type(4))) float f32x4;
typedef __attribute__((ext_vector_type(16))) float f32x16;
typedef __attribute__((ext_vector_type(2))) float f32x2;
typedef __attribute__((ext_vector_type(4))) unsigned u32x4;
typedef __attribute__((ext_vector_type(2))) unsigned u32x2;
typedef __attribute__((address_space(3))) s16x4* lds_s16x4_p;
typedef unsigned long long u64;
typedef __attribute__((address_space(1))) u64 gu64;
typedef __attribute__((address_space(3))) unsigned lu32;
#define DI __device__ __forceinline__
#define SB() __builtin_amdgcn_sched_barrier(0)
template <int N> using ic = std::integral_constant<int, N>;

constexpr int S_ = 2048, D_ = 448, F_ = 1792, DF_ = 225;

template <class Fn, int... I> DI void sfor_impl(Fn&& f, std::integer_sequence<int, I...>) { (f(ic<I>{}), ...); }
template <int N, class Fn> DI void sfor(Fn&& f) { sfor_impl(f, std::make_integer_sequence<int, N>{}); }

DI f32x4 mfma16(u32x4 a, u32x4 b, f32x4 c) {
  return __builtin_amdgcn_mfma_f32_16x16x32_bf16(__builtin_bit_cast(bf16x8, a), __builtin_bit_cast(bf16x8, b), c, 0, 0, 0);
}
DI f32x16 mfma32(u32x4 a, u32x4 b, f32x16 c) {
  return __builtin_amdgcn_mfma_f32_32x32x16_bf16(__builtin_bit_cast(bf16x8, a), __builtin_bit_cast(bf16x8, b), c, 0, 0, 0);
}
DI unsigned pk2(float lo, float hi) { bf16x2 p; p[0] = (__bf16)lo; p[1] = (__bf16)hi; return __builtin_bit_cast(unsigned, p); }
DI float bflo(unsigned u) { return __builtin_bit_cast(float, u << 16); }
DI float bfhi(unsigned u) { return __builtin_bit_cast(float, u & 0xffff0000u); }
DI float rbf(float f) { return (float)(__bf16)f; }
typedef __attribute__((address_space(3))) const char* lds_cp;
typedef __attribute__((address_space(3))) const u32x4* lds_u32x4_p;
template <int O0, int O1> DI u32x4 tr_at(unsigned a) {
  s16x4 x = __builtin_amdgcn_ds_read_tr16_b64_v4i16((lds_s16x4_p)((lds_cp)(uintptr_t)a + O0));
  s16x4 y = __builtin_amdgcn_ds_read_tr16_b64_v4i16((lds_s16x4_p)((lds_cp)(uintptr_t)a + O1));
  return __builtin_bit_cast(u32x4, __builtin_shufflevector(x, y, 0, 1, 2, 3, 4, 5, 6, 7));
}
template <int O> DI u32x4 l128_at(unsigned a) { return *(lds_u32x4_p)((lds_cp)(uintptr_t)a + O); }
DI unsigned lds_addr(const void* p) { return (unsigned)(uintptr_t)(__attribute__((address_space(3))) const char*)p; }
template <int N> DI void wait_vm() { asm volatile("s_waitcnt vmcnt(%0)" ::"n"(N) : "memory"); }
DI void bar() { asm volatile("s_waitcnt lgkmcnt(0)" ::: "memory"); __builtin_amdgcn_s_barrier(); asm volatile("" ::: "memory"); }
DI u32x4 lds128(const char* p) { return *(const u32x4*)p; }
DI int lane_id() { int r; asm volatile("v_mbcnt_lo_u32_b32 %0, -1, 0\n\tv_mbcnt_hi_u32_b32 %0, -1, %0" : "=v"(r)); return r; }
DI float vcos(float a) { return __builtin_amdgcn_cosf(a); }
DI float vsin(float a) { return __builtin_amdgcn_sinf(a); }

#define STAMP_BEGIN
#define ST(i)
#define STP(i)
#define STX(i)
#define STAMP_END(kid)
#define STAMP_ARG
#define STAMP_PASS

constexpr unsigned ONES = 0x3f803f80u;
constexpr int SL = 16384;
constexpr int L_T1 = 8 * SL, L_P = 9 * SL, L_CT = 9 * SL + 13312;
constexpr unsigned OFF_Z = 8u << 20, OFF_W = 10u << 20, OFF_Y = 12u << 20, OFF_C = 14u << 20;
constexpr int FY0 = 256, FW0 = 512, FZ0 = 1536, FH0 = 2560;
#define SPINC 200000
constexpr int SPIN_CAP = SPINC;
constexpr int LEAD = 10, HFAT = 180;
constexpr int RA = 6;

enum : int { eX = 0, eW = 8, eD = 24, eDR = 40, eH = 48, eTB = 77, eEP = 81, ePF, eT1, ePRE, eDV, eF, eFA, eFB, eFC, eZ, eZF, eWF, eWL,
             eHF, eFL1, eP1, eFL2, eP2, eRES, mX = 200, mR = 209, mP = 210, mK = 230, mEnd = 500 };
struct Ev { int id, n; };
#define PRO0 {ePF, 1}, {eX + 0, 2}, {eX + 1, 2}, {eX + 2, 2}, {eT1, 0}, {mR, 0}, {ePRE, 0}
#define PRO PRO0, {mX + 0, 0}, {eEP, 1}, {eF, 2}, {mX + 1, 0}, {mX + 2, 0}, {mX + 3, 0}, {mX + 4, 0}, {mX + 5, 0}, {mX + 6, 0}, {mP + 0, 0}, {eZ, 2}, {eW + 3, 4}
constexpr Ev SEQ_R[] = {PRO,
  {mP + 1, 0}, {eFA, 0}, {eW + 4, 4}, {eFB, 0}, {eDV, 0}, {eD + 0, 4}, {eFC, 2},
  {mP + 2, 0}, {eW + 5, 4}, {eZF, 1}, {eD + 1, 4},
  {mP + 3, 0}, {eW + 6, 4}, {eD + 2, 4},
  {mP + 4, 0}, {eW + 7, 4}, {eWF, 1}, {eD + 3, 4}, {eDR + 0, 0},
  {mP + 5, 0}, {eW + 8, 4}, {eD + 4, 4},
  {mP + 6, 0}, {eW + 9, 4}, {eD + 5, 4}, {eDR + 1, 0},
  {mP + 7, 0}, {eW + 10, 4},
  {mP + 8, 0}, {eW + 11, 4}, {eD + 6, 4},
  {mP + 9, 0}, {eW + 12, 4}, {eD + 7, 4}, {eDR + 2, 0},
  {mP + 10, 0}, {eW + 13, 4}, {eD + 8, 4},
  {mP + 11, 0}, {eD + 9, 4}, {eDR + 3, 0},
  {mP + 12, 0}, {eD + 10, 4}, {eD + 11, 4}, {eFL1, 1},
  {mP + 13, 0}, {eP1, 0}, {eH + 0, 2}, {eH + 1, 2}, {eH + 2, 2}, {eH + 3, 2}, {eH + 4, 2},
  {mK + 0, 0}, {eH + 5, 2}, {eH + 6, 2}, {mK + 1, 0}, {eH + 7, 2}, {eH + 8, 2}, {mK + 2, 0}, {eH + 9, 2}, {eH + 10, 2}, {mK + 3, 0}, {eH + 11, 2}, {eH + 12, 2},
  {mK + 4, 0}, {eH + 13, 2}, {eH + 14, 2}, {mK + 5, 0}, {eH + 15, 2}, {mK + 6, 0}, {mK + 7, 0}, {eDR + 4, 0},
  {mK + 8, 0}, {eD + 12, 4}, {mK + 9, 0}, {eD + 13, 4}, {mK + 10, 0}, {eRES, 2}, {mK + 11, 0}, {eDR + 5, 0},
  {mK + 12, 0}, {mK + 13, 0}, {eFL2, 1}, {mK + 14, 0}, {mK + 15, 0}, {eP2, 0},
  {eH + 16, 2}, {eH + 17, 2}, {eH + 18, 2}, {eH + 19, 2}, {eH + 20, 2}, {eH + 21, 2}, {eH + 22, 2}, {eH + 23, 2}, {eH + 24, 2}, {eH + 25, 2}, {eH + 26, 2}, {eH + 27, 2},
  {mK + 16, 0}, {mK + 17, 0}, {mK + 18, 0}, {mK + 19, 0}, {eDR + 6, 0}, {mK + 20, 0}, {mK + 21, 0}, {mK + 22, 0}, {mK + 23, 0},
  {mK + 24, 0}, {mK + 25, 0}, {mK + 26, 0}, {mK + 27, 0}, {mEnd, 0}};
constexpr Ev SEQ_B[] = {PRO,
  {mP + 1, 0}, {eFA, 0}, {eW + 4, 4}, {eFB, 0}, {eTB + 0, 0}, {eFC, 2},
  {mP + 2, 0}, {eW + 5, 4}, {eZF, 1}, {eTB + 1, 0},
  {mP + 3, 0}, {eW + 6, 4}, {eTB + 2, 0},
  {mP + 4, 0}, {eWF, 1}, {eTB + 3, 0}, {eWL, 1}, {mP + 5, 0}, {mEnd, 0}};
constexpr Ev SEQ_T[] = {
  {mP + 13, 0}, {eDV, 0}, {eD + 0, 4}, {eD + 1, 4}, {eD + 2, 4}, {eD + 3, 4}, {eHF, 1}, {eDR + 0, 0}, {eD + 4, 4}, {eD + 5, 4}, {eDR + 1, 0}, {eD + 6, 4}, {eD + 7, 4},
  {eFL1, 1}, {eP1, 0},
  {eH + 0, 1}, {eH + 1, 1}, {eH + 2, 1}, {eH + 3, 1}, {eH + 4, 1}, {eH + 5, 1}, {eH + 6, 1}, {eH + 7, 1},
  {eH + 8, 1}, {eH + 9, 1}, {eH + 10, 1}, {eH + 11, 1}, {eH + 12, 1}, {eH + 13, 1}, {eH + 14, 1}, {eH + 15, 1}, {eDR + 2, 0}, {eD + 8, 4}, {eD + 9, 4},
  {mK + 0, 0}, {mK + 1, 0}, {mK + 2, 0}, {mK + 3, 0}, {mK + 4, 0}, {mK + 5, 0}, {mK + 6, 0}, {mK + 7, 0}, {eDR + 3, 0}, {eD + 10, 4}, {eD + 11, 4},
  {mK + 8, 0}, {mK + 9, 0}, {mK + 10, 0}, {mK + 11, 0}, {mK + 12, 0}, {mK + 13, 0}, {mK + 14, 0}, {mK + 15, 0}, {eDR + 4, 0}, {eD + 12, 4}, {eD + 13, 4}, {eFL2, 1}, {eDR + 5, 0}, {eP2, 0},
  {eH + 16, 1}, {eH + 17, 1}, {eH + 18, 1}, {eH + 19, 1}, {eH + 20, 1}, {eH + 21, 1}, {eH + 22, 1}, {eH + 23, 1}, {eH + 24, 1}, {eH + 25, 1}, {eH + 26, 1}, {eH + 27, 1},
  {eDR + 6, 0},
  {mK + 16, 0}, {mK + 17, 0}, {mK + 18, 0}, {mK + 19, 0}, {mK + 20, 0}, {mK + 21, 0}, {mK + 22, 0}, {mK + 23, 0},
  {mK + 24, 0}, {mK + 25, 0}, {mK + 26, 0}, {mK + 27, 0}, {mEnd, 0}};
constexpr Ev SEQ_C[] = {PRO0, {eX + 3, 4}, {mX + 0, 0}, {eEP, 1}, {eX + 4, 4}, {mX + 1, 0}, {eX + 5, 4}, {mX + 2, 0}, {eX + 6, 4}, {mX + 3, 0}, {eW + 0, 4},
  {mX + 4, 0}, {eW + 1, 4}, {mX + 5, 0}, {eW + 2, 4}, {mX + 6, 0}, {mP + 0, 0}, {mEnd, 0}};
template <int R> constexpr Ev seq(int i) { return R == 0 ? SEQ_R[i] : R == 1 ? SEQ_B[i] : R == 2 ? SEQ_T[i] : SEQ_C[i]; }
template <int R> constexpr int sidx(int id) { int i = 0; while (seq<R>(i).id != id && seq<R>(i).id != mEnd) ++i; return i; }
template <int R> constexpr bool has(int id) { return seq<R>(sidx<R>(id)).id == id; }
template <int R> constexpr int pos(int id) { int p = has<R>(id) ? 0 : 1000; for (int i = 0; i <= sidx<R>(id); ++i) p += seq<R>(i).n; return p; }
template <int R> constexpr int before(int id) { return pos<R>(id) - seq<R>(sidx<R>(id)).n; }
template <int R> constexpr int seglen(int i0) { int n = 0; while (seq<R>(i0 + n).id < mX) ++n; return n; }
constexpr int cmax(int a, int b) { return a > b ? a : b; }

DI float dot2(unsigned a, unsigned b, float c) { return __builtin_amdgcn_fdot2_f32_bf16(__builtin_bit_cast(bf16x2, a), __builtin_bit_cast(bf16x2, b), c, false); }
typedef u32x4 srd_t;
typedef __attribute__((ext_vector_type(16))) unsigned u32x16;
template <int I> DI srd_t srd_of(u32x16 a) { return __builtin_shufflevector(a, a, 4 * I, 4 * I + 1, 4 * I + 2, 4 * I + 3); }
template <int L> DI void dma2(unsigned lw, unsigned v0, srd_t r0, unsigned v1, srd_t r1, unsigned so) {
  asm volatile("s_add_u32 m0, %0, %1\n\ts_nop 0\n\tbuffer_load_dwordx4 %2, %3, %6 offen lds\n\tbuffer_load_dwordx4 %4, %5, %6 offen offset:1024 lds"
               ::"s"(lw), "n"(L), "v"(v0), "s"(r0), "v"(v1), "s"(r1), "s"(so) : "memory", "scc");
}
template <int L> DI void dma4(unsigned lw, unsigned v0, unsigned v1, unsigned v2, unsigned v3, srd_t r0, srd_t r1, unsigned so) {
  asm volatile("s_add_u32 m0, %0, %1\n\ts_nop 0\n\tbuffer_load_dwordx4 %2, %6, %8 offen lds\n\tbuffer_load_dwordx4 %3, %7, %8 offen offset:1024 lds\n\t"
               "buffer_load_dwordx4 %4, %6, %8 offen offset:2048 lds\n\tbuffer_load_dwordx4 %5, %7, %8 offen offset:3072 lds"
               ::"s"(lw), "n"(L), "v"(v0), "v"(v1), "v"(v2), "v"(v3), "s"(r0), "s"(r1), "s"(so) : "memory", "scc");
}
template <int L> DI void dma4_sc1(unsigned lw, unsigned v, srd_t r, unsigned so) {
  asm volatile("s_add_u32 m0, %0, %1\n\ts_nop 0\n\tbuffer_load_dwordx4 %2, %3, %4 offen sc1 lds\n\tbuffer_load_dwordx4 %2, %3, %4 offen offset:1024 sc1 lds\n\t"
               "buffer_load_dwordx4 %2, %3, %4 offen offset:2048 sc1 lds\n\tbuffer_load_dwordx4 %2, %3, %4 offen offset:3072 sc1 lds"
               ::"s"(lw), "n"(L), "v"(v), "s"(r), "s"(so) : "memory", "scc");
}
template <int OFF> DI void ld_sc1(u32x4& d, unsigned voff, srd_t r, unsigned so) { asm volatile("buffer_load_dwordx4 %0, %1, %2, %3 offen offset:%4 sc1" : "=v"(d) : "v"(voff), "s"(r), "s"(so), "n"(OFF) : "memory"); }
template <int OFF> DI void ld_sc1_over(u32x4& d, unsigned voff, srd_t r, unsigned so) { asm volatile("buffer_load_dwordx4 %0, %1, %2, %3 offen offset:%4 sc1" : "+v"(d) : "v"(voff), "s"(r), "s"(so), "n"(OFF) : "memory"); }
DI void ld_pl(u32x4& d, unsigned voff, srd_t r, unsigned so) { asm volatile("buffer_load_dwordx4 %0, %1, %2, %3 offen" : "=v"(d) : "v"(voff), "s"(r), "s"(so) : "memory"); }
DI void ld_u32_sc1(unsigned& d, unsigned voff, srd_t r, unsigned so) { asm volatile("buffer_load_dword %0, %1, %2, %3 offen sc1" : "=v"(d) : "v"(voff), "s"(r), "s"(so) : "memory"); }
DI void ld_flag(u64& d, unsigned voff, srd_t r) { asm volatile("buffer_load_dwordx2 %0, %1, %2, %3 offen sc1" : "=v"(d) : "v"(voff), "s"(r), "s"(OFF_C) : "memory"); }
template <int OFF = 0> DI void st16_sc1(u32x4 v, unsigned voff, srd_t r, unsigned so) {
  asm volatile("buffer_store_dwordx4 %0, %1, %2, %3 offen offset:%4 sc1\n\ts_nop 1" ::"v"(v), "v"(voff), "s"(r), "s"(so), "n"(OFF) : "memory");
}
DI void st_flag1(srd_t r, unsigned so, u64 tag) {
  asm volatile("s_mov_b64 exec, 1\n\tbuffer_store_dwordx2 %0, off, %1, %2 sc1\n\ts_mov_b64 exec, -1" ::"v"(tag), "s"(r), "s"(so) : "memory");
}
template <class T> DI void pin(T& a) { asm volatile("" : "+v"(a)); }
template <class T> DI void pins(T& a) { asm volatile("" : "+s"(a)); }
template <class T> DI void opq(T& a) { asm("" : "+v"(a)); }
DI unsigned selm(u64 m, unsigned a, unsigned b) { unsigned r; asm("v_cndmask_b32_e64 %0, %2, %1, %3" : "=v"(r) : "v"(a), "v"(b), "s"(m)); return r; }
DI unsigned selz(u64 m, unsigned a) { unsigned r; asm("v_cndmask_b32_e64 %0, 0, %1, %2" : "=v"(r) : "v"(a), "s"(m)); return r; }
template <int N> DI void vbar() {
  static_assert(N >= 0, "wait");
  asm volatile("s_waitcnt vmcnt(%0) lgkmcnt(0)\n\ts_barrier" ::"n"(N > 63 ? 63 : N) : "memory");
}
template <int N> DI void wvm() { static_assert(N >= 0, "wait"); wait_vm<(N > 63 ? 63 : N)>(); }
DI void poll(u64& fv, u64 mask, unsigned voff, srd_t sb, u64 tag) {
  if ((~__builtin_amdgcn_ballot_w64(fv == tag) & mask) != 0) {
#pragma unroll 1
    for (int it = 0; it < SPIN_CAP; ++it) {
      __builtin_amdgcn_s_sleep(1);
      ld_flag(fv, voff, sb); wait_vm<0>(); pin(fv);
      if ((~__builtin_amdgcn_ballot_w64(fv == tag) & mask) == 0) break;
    }
  }
  asm volatile("" ::: "memory");
}
typedef __attribute__((address_space(3))) char* lds_p;
template <class T, int O> DI T lds_ld(unsigned a) { return *(__attribute__((address_space(3))) const T*)((lds_cp)(uintptr_t)a + O); }
template <class T, int O> DI void lds_st(unsigned a, T v) { *(__attribute__((address_space(3))) T*)((lds_p)(uintptr_t)a + O) = v; }
template <int I> DI void arrive(unsigned lds0) {
  asm volatile("s_waitcnt lgkmcnt(0)\n\ts_mov_b64 exec, 1\n\tds_add_u32 %0, %1 offset:%2\n\ts_mov_b64 exec, -1" ::"v"(lds0 + L_CT), "v"(1u), "n"(4 * I) : "memory");
}
template <int I> DI void await4(unsigned lds0) {
#pragma unroll 1
  for (int it = 0; it < SPIN_CAP * 8; ++it) {
    unsigned c;
    asm volatile("ds_read_b32 %0, %1 offset:%2\n\ts_waitcnt lgkmcnt(0)" : "=v"(c) : "v"(lds0 + L_CT), "n"(4 * I) : "memory");
    if (__builtin_amdgcn_readfirstlane(c) >= 4) break;
    __builtin_amdgcn_s_sleep(1);
  }
}
template <int N> DI void trig(float a, float d, float k, float m, float* c, float* s) {
  c[0] = vcos(a) * m; s[0] = vsin(a) * m; c[1] = vcos(a + d) * m; s[1] = vsin(a + d) * m;
#pragma unroll
  for (int j = 2; j < N; ++j) { c[j] = __builtin_fmaf(k, c[j - 1], -c[j - 2]); opq(c[j]); s[j] = __builtin_fmaf(k, s[j - 1], -s[j - 2]); opq(s[j]); }
}
DI void put_part(unsigned a, int l, const f32x16& c0, const f32x16& c1) {
  sfor<8>([&](auto K) { constexpr int k = K; const f32x16& c = k < 4 ? c0 : c1;
    lds_st<f32x4, k * 1024>(a + (l >> 5) * 512 + (((l & 31) ^ (2 * k)) * 16), f32x4{c[4 * (k & 3)], c[4 * (k & 3) + 1], c[4 * (k & 3) + 2], c[4 * (k & 3) + 3]});
  });
}

#define BUF(n) const void* n##p, unsigned n##b, unsigned n##f
__global__ __launch_bounds__(512, 2) void fwd(BUF(x), BUF(g), BUF(u), BUF(w), BUF(d), BUF(a), BUF(c), BUF(o) STAMP_ARG) {
  extern __shared__ __attribute__((aligned(16))) char smem[];
  u32x16 ka, kb;
  unsigned b = blockIdx.x, tid = threadIdx.x;
  STAMP_BEGIN
  {
    u64 pc; unsigned t, pf;
    asm volatile("s_load_dwordx16 %0, %7, 0\n\ts_load_dwordx16 %1, %7, 64\n\ts_getpc_b64 %2\n.Lpf_here:\n\ts_lshr_b32 %3, %5, 3\n\ts_lshl_b32 %3, %3, 11\n\t"
                 "v_lshl_add_u32 %4, %6, 2, %3\n\ts_mov_b32 m0, 0\n\tv_min_u32 %4, .Lpf_end-.Lpf_here, %4\n\tglobal_load_lds_dword %4, %2"
                 : "=&s"(ka), "=&s"(kb), "=&s"(pc), "=&s"(t), "=&v"(pf), "+s"(b), "+v"(tid) : "s"(__builtin_amdgcn_kernarg_segment_ptr()) : "memory", "scc");
  }
  SB();
  const int w = __builtin_amdgcn_readfirstlane(tid >> 6), ws = w & 3, wn = w >> 2;
  const int xcd = b & 7, idx = b >> 3;
  const unsigned lds0 = lds_addr(smem), lw = lds0 + w * 2048;
  const int rb = (xcd & 1) * 8 + (idx & 7), g = xcd >> 1, ii = idx >> 3;
  const int r0 = (unsigned)((idx & 7) + 2 * ii) % 7;
  const int unit1 = 4 * g + ii, unit2 = 16 + 3 * g + ii;
  unsigned xvo[2];
  auto mk_xv = [&](unsigned t) {
    xvo[0] = (unsigned)(128 * rb + 32 * ((t >> 3) & 3) + ((t >> 4) & 28) + ((t >> 5) & 1)) * (D_ * 2) + ((t ^ ((t >> 4) & 3)) & 7) * 16;
    xvo[1] = (xvo[0] ^ 64) + (2 * D_ * 2 - 1024);
    pin(xvo[0]); pin(xvo[1]);
  };
  mk_xv(tid);
  unsigned xs0 = r0 * 128; pins(xs0);
  auto t1gen = [&](int s) {
    const int l = lane_id(), hh = l >> 5, fi = ((l >> 1) & 9) | (l & 4) | ((l >> 2) & 2), f = (15 * unit1 + fi) & ((fi - 15) >> 31);
    const float cf_ = (float)f * (1.0f / 448), a0 = (float)(16 * s + 8 * hh) * cf_ + (float)(l & 1) * 0.25f;
    const unsigned sg = (unsigned)(f & 1) * 0x80008000u, a = lds0 + L_T1 + s * 1024 + l * 16;
    u32x4 o;
#pragma unroll
    for (int e = 0; e < 4; ++e) o[e] = pk2(vcos(a0 + (float)(2 * e) * cf_), vcos(a0 + (float)(2 * e + 1) * cf_));
    lds_st<u32x4, 0>(a, o); lds_st<u32x4, 14336>(a, o ^ sg);
  };
  t1gen(w);
  SB();
  asm volatile("s_waitcnt lgkmcnt(0)" : "+s"(ka), "+s"(kb));
  const srd_t rx = srd_of<0>(ka), rg = srd_of<1>(ka), rw = srd_of<3>(ka), rd = srd_of<0>(kb), ra = srd_of<1>(kb), rc = srd_of<2>(kb), ro = srd_of<3>(kb);
  STX(50)
  dma2<SL>(lw, xvo[0], rx, xvo[1], rx, xs0);
  STX(51)
  SB();
  srd_t ru = srd_of<2>(ka);
  { const u64 p = ((u64)ru[1] << 32 | ru[0]) - 1024; ru[0] = (unsigned)p; ru[1] = (unsigned)(p >> 32); }
  const bool bcu = ii == 3;
  auto kof = [&](int st) { int k = st + r0; return k >= 7 ? k - 7 : k; };
  constexpr unsigned CH = 64 * F_ * 2;
  const int rt = 4 * xcd + (idx & 3), dt = bcu ? 6 : (idx >> 2);
  const int rot = dt + (idx & 3);
  const int rot3 = (unsigned)rot % 3;
  auto rnd = [&](int i) { if (i < 4) return (i + rot) & 3; const int k = i - 4 + rot3; return 4 + (k >= 3 ? k - 3 : k); };
  const int rtb = 2 * rt + (bcu ? (idx >> 2) & 1 : 0);
  const int gi = b & 63;
  const f32x4 zero = {0.f, 0.f, 0.f, 0.f};
  const f32x16 zero16 = {};

  u64 ep, fv, tag;
  unsigned lvo, wvo, wo, xo[4], gvo, ovo, r2u, hso, wv1, dv[4], ao;
  float sq[2], t1c[8], t1s[8], u2c[8], u2s[8];
  u32x4 xres, fl[2], wdf[7][8], hf[28][2], C64[2], S64[2], M1[4], M2[4], C32[2], S32[2], N32[2];
  f32x16 accA, ah[2][2];
  auto res = [&](int R, int u) {
    const int l = lane_id(), pp = 32 * (rtb & 3) + 8 * (R ? ws : w) + (l >> 3);
    ld_u32_sc1(r2u, (unsigned)(pp * 16), rw, OFF_Z + 12 + (4 * u + 3) * 32768 + (rt >> 1) * 2048);
    ovo = (unsigned)((128 * (rt >> 1) + 32 * (pp & 3) + (pp >> 2)) * D_ + dt * 64 + (l & 7) * 8) * 2;
    ld_pl(xres, ovo, rx, 0);
  };
  auto ev = [&](auto Rc, auto Ic) { constexpr int R = Rc, id = Ic;
    if constexpr (id >= eX && id < eX + 7) { constexpr int j = id - eX;
      if constexpr (j > 2) dma4<(j + 1) * SL>(lds0 + ws * 4096, xvo[0], xvo[1], dv[0], dv[1], rx, rx, kof(j) * 128);
      else if constexpr (j > 0) dma2<(j + 1) * SL>(lw, xvo[0], rx, xvo[1], rx, kof(j) * 128);
    } else if constexpr (id >= eW && id < eW + 14) { constexpr int j = id - eW;
      dma4<(j % 5) * SL>(lds0 + ws * 4096, wvo, wvo, wv1, wv1, rg, ru, kof(j % 7) * CH + (j >= 7 ? (unit2 - unit1) * 128 : 0));
    } else if constexpr (id >= eD && id < eD + 14) { constexpr int i = id - eD;
      if constexpr (i >= 4 && !(i & 1)) asm volatile("s_waitcnt lgkmcnt(0)" ::: "memory");
      dma4<(R ? 0 : 5 * SL) + (i & 3) * 4096>(lds0 + ws * SL, dv[0], dv[1], dv[2], dv[3], rd, rd, rnd(i >> 1) * CH + (i & 1) * (32 * D_ * 2));
    } else if constexpr (id >= eDR && id < eDR + 7) { constexpr int i = id - eDR;
      if constexpr (has<R>(eD + 2 * i + 1)) wvm<pos<R>(id) - pos<R>(eD + 2 * i + 1)>();
      unsigned a1 = ao ^ 64; pin(a1);
      sfor<8>([&](auto K) { constexpr int o = (i & 1) * 8192 + (K >> 1) * 2048; wdf[i][K] = tr_at<o, o + 1024>((K & 1) ? a1 : ao); });
    } else if constexpr (id >= eH && id < eH + 28) { constexpr int s = id - eH;
      const unsigned so = (rtb * 28 + ws) * 4096 + rnd(s >> 2) * 16384;
      if constexpr (R) ld_sc1<(s & 3) * 1024>(hf[s][0], lvo, rw, so);
      else sfor<2>([&](auto C) { constexpr int c = C, k = (s - 5) >> 1;
        if constexpr (s < 5) ld_sc1<(s & 3) * 1024>(hf[s][c], lvo, rw, so + c * 28 * 4096);
        else {
          if constexpr (s >= 26) hf[s][c] = wdf[3][2 * (s - 26) + c];
          else if constexpr (s >= 16) hf[s][c] = hf[s - 10][c];
          else if constexpr (s & 1) hf[s][c] = hf[k][c];
          else hf[s][c] = wdf[k >> 2][2 * (k & 3) + c];
          ld_sc1_over<(s & 3) * 1024>(hf[s][c], lvo, rw, so + c * 28 * 4096);
        }
      });
    } else if constexpr (id == eEP) {
      ld_flag(ep, 0u, rw);
    } else if constexpr (id == eT1) {
      STX(52)
      lds_st<u32x4, 0>(lds0 + L_CT, u32x4{0u, 0u, 0u, 0u});
      if (w >= 2) t1gen(w + 6);
      STX(53)
    } else if constexpr (id == ePRE) {
      STX(54)
      const int l = lane_id(), r8 = l >> 3, ch = l & 7, r32 = l & 31, hh = l >> 5;
      lvo = (unsigned)l * 16;
      if constexpr (R == 3) {
        mk_xv(128 * ws + l); dv[0] = xvo[0] + (4 * D_ * 2 - 2048); dv[1] = xvo[1] + (4 * D_ * 2 - 2048); pin(dv[0]); pin(dv[1]);
        wo = lds0 + hh * 2048 + ((l & 15) >> 2) * 128 + (r8 & 1) * 64 + ((l >> 4) & 1) * 32 + (l & 3) * 8; pin(wo);
        ao = ONES & (unsigned)((((l >> 2) ^ (l >> 4)) & 1) - 1); pin(ao);
      }
      wvo = (unsigned)((16 * ws + r8) * F_ * 2 + (ch ^ ((r8 & 2) << 1)) * 16 + unit1 * 128); wv1 = wvo + (8 * F_ * 2 - 2048); pin(wvo); pin(wv1);
#pragma unroll
      for (int i = 0; i < 4; ++i) { xo[i] = lds0 + SL + (32 * ws + r32) * 128 + (((2 * i + hh) ^ ((r32 >> 1) & 7)) * 16); pin(xo[i]); }
      pin(lvo);
    } else if constexpr (id == eDV) {
      const int l = lane_id();
      dv[0] = (unsigned)((64 * ws + (l >> 3)) * D_ * 2 + dt * 128 + (((l & 7) ^ ((l >> 2) & 4)) * 16));
#pragma unroll
      for (int i = 1; i < 4; ++i) { dv[i] = dv[0] + i * (8 * D_ * 2 - 1024); pin(dv[i]); }
      ao = lds0 + ((R ? 0 : 5) + ws) * SL + (4 * (l >> 5) + ((l & 15) >> 2)) * 128 + ((l >> 3) & 1) * 64 + ((l >> 4) & 1) * 32 + (l & 3) * 8;
      pin(ao);
    } else if constexpr (id == eZ) {
      const unsigned su = __float_as_uint(accA[14]), qu = __float_as_uint(sq[0] + sq[1]);
      const float sm = __uint_as_float(__builtin_amdgcn_permlane32_swap(su, su, false, false)[1]);
      const u32x2 q2 = __builtin_amdgcn_permlane32_swap(qu, qu, false, false);
      const float mean = sm * (1.0f / D_);
      const unsigned nm = pk2(-mean, -mean);
      const float rs = __builtin_amdgcn_rcpf(rbf(__builtin_amdgcn_sqrtf(rbf(rbf(fmaxf(__uint_as_float(q2[0]) + __uint_as_float(q2[1]) - sm * mean, 0.f) * (1.0f / D_)) + 1e-5f))));
      const u64 m0 = unit1 == 0 ? 0xffffffffull : 0ull, m6 = unit1 == 14 ? ~0xffffffffull : 0ull;
      accA[0] += __uint_as_float(selz(m0, nm << 16)) * 448.f;
      const unsigned rh = __float_as_uint(0.5f * rs);
      const unsigned zv = (lvo & 511) + (lvo & 512) * 64;
      u32x4 t;
      sfor<8>([&](auto E) { constexpr int e = E;
        const float r = e == 0 ? __uint_as_float(selm(m0, rh, __float_as_uint(rs))) : e == 6 ? __uint_as_float(selm(m6, rh, __float_as_uint(rs))) : rs;
        float p = accA[2 * e] * r, q = accA[2 * e + 1] * r; opq(p); opq(q);
        t[e & 3] = pk2(p, q);
        if constexpr (e == 7) t[3] = selm(0xffffffffull, t[3], __float_as_uint(rs * rs));
        if constexpr ((e & 3) == 3) st16_sc1(t, zv, rw, OFF_Z + unit1 * 131072 + (e >> 2) * 65536 + rb * 2048 + ws * 512);
      });
    } else if constexpr (id == eF) {
      const int l = lane_id(), fc = idx & 7;
      const unsigned fo = (unsigned)(((l >> 4) * 256 + ((l >> 2) & 3)) * DF_ + min(30 * fc - (fc == 7) + 8 * (l & 3), DF_ - 8)) * 2;
      const unsigned fs = (unsigned)(16 * ii + 4 * ws + 64 * (16 * (xcd >> 2) + (xcd & 3))) * DF_ * 2;
      ld_pl(fl[0], fo, ra, fs); ld_pl(fl[1], fo, rc, fs);
    } else if constexpr (id == eFA) {
      wvm<pos<R>(eFA) - pos<R>(eF)>(); pin(fl[0]); pin(fl[1]);
      const unsigned a = lvo + (lds0 + L_P + ws * 2048); lds_st<u32x4, 0>(a, fl[0]); lds_st<u32x4, 1024>(a, fl[1]);
    } else if constexpr (id == eFB) {
      const unsigned a = ((lvo & 192) << 2) + ((lvo >> 2) & 192) + ((lvo >> 1) & 24) + (lds0 + L_P + ws * 2048);
      fl[0] = tr_at<0, 1024>(a); fl[1] = tr_at<32, 1056>(a);
    } else if constexpr (id == eFC) {
      const unsigned a = ((lvo & 240) << 9) + ((lvo >> 4) & 48), so = OFF_W + (idx & 7) * 262144 + ii * 2048 + xcd * 256 + ws * 64;
      st16_sc1(fl[0], a, rw, so); st16_sc1(fl[1], a, rw, so + 131072);
    } else if constexpr (id == eZF) {
      wvm<before<R>(eZF) - pos<R>(eZ)>();
      st_flag1(rw, OFF_C + (FZ0 + unit1 * 64 + rb * 4 + ws) * 8, tag);
    } else if constexpr (id == eWF) {
      wvm<before<R>(eWF) - pos<R>(eFC)>();
      st_flag1(rw, OFF_C + (FW0 + (idx & 7) * 128 + ii * 32 + xcd * 4 + ws) * 8, tag);
    } else if constexpr (id == eWL) {
      gvo = selm(0xffffffffull, (FW0 + (gi >> 3) * 128 + 32 * ws) * 8 + (lvo >> 1), (FZ0 + (gi >> 2) * 64 + 16 * ws) * 8 + ((lvo >> 1) & 127));
      ld_flag(fv, gvo, rw);
    } else if constexpr (id >= eTB && id < eTB + 4) { constexpr int j = id - eTB;
      const int l = lane_id(), v = l & 15, q = l >> 4, vp = 4 * (v & 3) + (v >> 2);
      float c[8], s[8];
      if constexpr (j == 0) {
        const float d = (float)(16 * ws + v) * (1.0f / 64);
        const unsigned sg = (v & 1) ? 0x80008000u : 0u;
        trig<8>((float)(8 * q) * d, d, 2.f * vcos(d), 1.f, c, s);
#pragma unroll
        for (int e = 0; e < 4; ++e) {
          C64[0][e] = pk2(c[2 * e], c[2 * e + 1]); S64[0][e] = pk2(s[2 * e], s[2 * e + 1]);
          M1[0][e] = pk2(c[e], s[e]); M1[1][e] = pk2(c[4 + e], s[4 + e]); M2[0][e] = pk2(-s[e], c[e]); M2[1][e] = pk2(-s[4 + e], c[4 + e]);
        }
        C64[1] = C64[0] ^ sg; S64[1] = S64[0] ^ sg;
        pin(C64[0]); pin(C64[1]); pin(S64[0]); pin(S64[1]); pin(M1[0]); pin(M1[1]); pin(M2[0]); pin(M2[1]);
      } else if constexpr (j == 1) {
        const float d = (float)vp * (1.0f / 32);
        const unsigned sg = (q & 1) ? 0x80008000u : 0u;
        trig<8>((float)q * d, 4.f * d, 2.f * vcos(4.f * d), 1.f, c, s);
#pragma unroll
        for (int e = 0; e < 4; ++e) { C32[0][e] = pk2(c[2 * e], c[2 * e + 1]); S32[0][e] = pk2(s[2 * e], s[2 * e + 1]); }
        C32[1] = C32[0] ^ sg; S32[1] = S32[0] ^ sg; N32[0] = S32[0] ^ 0x80008000u; N32[1] = N32[0] ^ sg;
        sfor<2>([&](auto T) { pin(C32[T]); pin(S32[T]); pin(N32[T]); });
      } else if constexpr (j == 2) {
        const float d = (float)(16 * ws + v) * (1.0f / 2048);
        const unsigned sg = (v & 1) ? 0x80008000u : 0u;
        trig<8>((float)q * d, 4.f * d, 2.f * vcos(4.f * d), 2.0f / (448.0f * 2048.0f), t1c, t1s);
        sfor<8>([&](auto I) { pin(t1c[I]); pin(t1s[I]); });
        sfor<2>([&](auto I) { M1[2 + I] = M1[I] ^ sg; M2[2 + I] = M2[I] ^ sg; pin(M1[2 + I]); pin(M2[2 + I]); });
      } else {
        const float k = (float)(16 * ws + 4 * q);
        sfor<8>([&](auto I) { constexpr int t = I >> 2, r = I & 3;
          const float a = (k + (float)r) * ((float)(16 * t + vp) * (1.0f / 2048)); u2c[I] = vcos(a); u2s[I] = vsin(a); pin(u2c[I]); pin(u2s[I]); });
      }
    } else if constexpr (id == eHF) {
      wvm<has<R>(eHF) ? before<R>(eHF) : 0>(); st_flag1(rw, OFF_C + FH0 * 8 + (hso >> 9), tag);
    } else if constexpr (id == eFL1) {
      const int l = lane_id(), i = l < 14 ? l >> 1 : 0;
      gvo = (unsigned)(FH0 + (rtb + (R ? 0 : l & 1)) * 28 + 4 * i + ws) * 8;
      ld_flag(fv, gvo, rw);
    } else if constexpr (id == eFL2) { ld_flag(fv, gvo, rw);
    } else if constexpr (id == eP1) { wvm<pos<R>(eP1) - pos<R>(eFL1)>(); pin(fv); ST(5) poll(fv, 0xffull, gvo, rw, tag); ST(6)
    } else if constexpr (id == eP2) { wvm<pos<R>(eP2) - pos<R>(eFL2)>(); pin(fv); ST(8) poll(fv, 0x3f00ull, gvo, rw, tag); ST(9)
    } else if constexpr (id == eRES) { res(0, ws);
    }
  };
  auto run = [&](auto Rc, auto Mc) { constexpr int R = Rc, mk = Mc, i0 = mk < 0 ? 0 : sidx<R>(mk) + 1;
    sfor<seglen<R>(i0)>([&](auto K) { ev(Rc, ic<seq<R>(i0 + K).id>{}); SB(); });
  };
  auto mk_tag = [&] { pin(ep);
    tag = (((u64)__builtin_amdgcn_readfirstlane((unsigned)(ep >> 32)) << 32) | (unsigned)__builtin_amdgcn_readfirstlane((unsigned)ep)) + 1; };
  auto tail_h = [&](auto Rc) { constexpr int R = Rc;
    run(Rc, ic<mP + 13>{});
    sfor<28>([&](auto Sc) { constexpr int s = Sc;
      wvm<pos<R>(mK + s) - pos<R>(eH + s)>();
      if constexpr (s == 0) { ST(7) }
      if constexpr (s == 16) { ST(10) }
      sfor<R ? 2 : 4>([&](auto I) { constexpr int a = I & 1, c = I >> 1;
        if constexpr (!a) pin(hf[s][c]);
        ah[c][a] = mfma32(wdf[s >> 2][2 * (s & 3) + a], hf[s][c], s == 0 ? zero16 : ah[c][a]); pin(ah[c][a]); });
      SB();
      run(Rc, ic<mK + s>{});
    });
    ST(11)
    const int l = lane_id();
    put_part(lds0 + ((R ? 0 : 5) + ws) * SL, l, ah[0][0], ah[0][1]);
    if constexpr (!R) { put_part(lds0 + (5 + ws) * SL + 8192, l, ah[1][0], ah[1][1]); pin(xres); pin(r2u); }
  };
  auto tail_c = [&](auto Rc, auto&& mid) { constexpr int R = Rc, NK = R ? 8 : 16;
    const int l = lane_id(), kq = R ? ws : ws >> 1;
    u32x4 t2[NK][2], yf[NK];
    const float c0 = (float)((2 * kq + (l >> 5)) * (15 * NK / 4));
    float dd[2], a0[2], k2[2];
#pragma unroll
    for (int t = 0; t < 2; ++t) { dd[t] = (float)(64 * dt + 32 * t + (l & 31)) * (1.0f / 448); a0[t] = __builtin_amdgcn_fractf(c0 * dd[t]); k2[t] = 2.f * vcos(dd[t]); }
    sfor<NK / 4>([&](auto Kc) { constexpr int k = Kc;
#pragma unroll
      for (int t = 0; t < 2; ++t) {
        float c[15], s[15];
        trig<15>((float)(15 * k) * dd[t] + a0[t], dd[t], k2[t], 1.f, c, s);
#pragma unroll
        for (int e = 0; e < 16; ++e) t2[4 * k + (e >> 2)][t][e & 3] = e < 15 ? pk2(c[e % 15], s[e % 15]) : 0u;
#pragma unroll
        for (int e = 0; e < 4; ++e) pin(t2[4 * k + e][t]);
      }
      SB();
      if constexpr (k == 1 - R) { mid(); SB(); }
      if constexpr (k == NK / 4 - 2) { gvo = (unsigned)(FY0 + (rt >> 3) * 64 + 2 * NK * kq + (l & (2 * NK - 1))) * 8; ld_flag(fv, gvo, rw); SB(); }
    });
    wait_vm<0>(); pin(fv);
    ST(8)
    poll(fv, ~0ull, gvo, rw, tag);
    ST(9)
    const unsigned yo = OFF_Y + (rtb + (R ? 0 : ws & 1)) * 512, yv = (unsigned)((l >> 5) * (NK * 32768) + (l & 31) * 16);
    sfor<NK>([&](auto I) { ld_sc1<0>(yf[I], yv, rw, yo + (2 * NK * kq + I) * 32768); });
    res(R, NK / 2 * kq);
    f32x16 ac[2];
    sfor<NK>([&](auto I) { constexpr int i = I;
      wait_vm<NK + 1 - i>(); pin(yf[i]);
      if constexpr (i == 0) { ST(10) }
      ac[0] = mfma32(t2[i][0], yf[i], i ? ac[0] : zero16); ac[1] = mfma32(t2[i][1], yf[i], i ? ac[1] : zero16);
      SB();
    });
    ST(11)
    put_part(lds0 + (R ? 5 * SL : 0) + ws * 8192, l, ac[0], ac[1]);
    wait_vm<0>(); pin(xres); pin(r2u);
  };

  auto finish = [&](auto Bc, auto Wc) { constexpr int B = Bc, WN = Wc;
    bar();
    if constexpr (!B) { ST(12) }
    if constexpr (!B || WN == 1) {
      const int l = lane_id(), ch = l & 7;
      const unsigned q = lds0 + ch * 1024 + (((8 * ws + (l >> 3)) ^ (2 * ch)) * 16), qh = q + (B ? 0 : 5 * SL + WN * 8192), qc = q + (B ? 5 * SL : WN * 8192);
      f32x4 a0 = lds_ld<f32x4, 0>(qh), a1 = lds_ld<f32x4, 512>(qh), c0 = lds_ld<f32x4, 0>(qc), c1 = lds_ld<f32x4, 512>(qc);
      sfor<3>([&](auto I) { a0 += lds_ld<f32x4, (I + 1) * SL>(qh); a1 += lds_ld<f32x4, (I + 1) * SL + 512>(qh); });
      c0 += lds_ld<f32x4, SL>(qc); c1 += lds_ld<f32x4, SL + 512>(qc);
      if constexpr (B) { c0 += lds_ld<f32x4, 8192>(qc) + lds_ld<f32x4, SL + 8192>(qc); c1 += lds_ld<f32x4, 8704>(qc) + lds_ld<f32x4, SL + 8704>(qc); }
      const float r2 = __uint_as_float(r2u);
      u32x4 o;
      o[0] = pk2(a0[0] * r2 + (c0[0] + bflo(xres[0])), a0[1] * r2 + (c0[1] + bfhi(xres[0])));
      o[1] = pk2(a0[2] * r2 + (c0[2] + bflo(xres[1])), a0[3] * r2 + (c0[3] + bfhi(xres[1])));
      o[2] = pk2(a1[0] * r2 + (c1[0] + bflo(xres[2])), a1[1] * r2 + (c1[1] + bfhi(xres[2])));
      o[3] = pk2(a1[2] * r2 + (c1[2] + bflo(xres[3])), a1[3] * r2 + (c1[3] + bfhi(xres[3])));
      asm volatile("buffer_store_dwordx4 %0, %1, %2, 0 offen\n\ts_nop 1" ::"v"(o), "v"(ovo), "s"(ro) : "memory");
    }
    if constexpr (!B && WN == 0) { if (b == 0 && w == 0) st_flag1(rw, OFF_C, tag); }
    if constexpr (B && WN == 1) asm volatile(".Lpf_end:");
    STAMP_END(2 * B + WN)
    __builtin_amdgcn_endpgm();
  };

  run(ic<0>{}, ic<-1>{});
  if (wn == 0) {
    __builtin_amdgcn_s_setprio(3);
    STX(15) run(ic<3>{}, ic<mR>{}); STX(19)
    u32x4 xf[28], wf[RA], eo;
    f32x16 acc[2][4];
    f32x4 sm[2] = {zero, zero};
    sfor<7>([&](auto J) { constexpr int j = J;
      vbar<pos<3>(mX + j) - pos<3>(eX + j)>();
      if constexpr (j == 0) { ST(1) }
      STX(21 + 4 * j)
#pragma unroll
      for (int i = 0; i < 4; ++i) {
        if constexpr (j == 3) { xo[i] += 3 * SL; pin(xo[i]); }
        xf[4 * j + i] = l128_at<(j < 3 ? j : j - 3) * SL>(xo[i]);
      }
      SB();
      run(ic<3>{}, ic<mX + j>{});
      STX(22 + 4 * j)
      if constexpr (j == 4) mk_tag();
#pragma unroll
      for (int i = 0; i < 4; ++i) sm[i & 1] = mfma16(u32x4{ao, ao, ao, ao}, xf[4 * j + i], sm[i & 1]);
      pin(sm[0]); pin(sm[1]);
      SB();
      STX(23 + 4 * j)
    });
    const float nm = rbf((sm[0][0] + sm[1][0]) * (-1.0f / D_));
    auto ctr = [&](auto Dc) { constexpr int d = Dc;
      if constexpr (d < 112) {
        unsigned t = xf[d >> 2][d & 3], u;
        asm volatile("v_lshlrev_b32 %1, 16, %0\n\tv_and_b32 %0, %3, %0\n\tv_add_f32 %1, %1, %2\n\tv_add_f32 %0, %0, %2\n\tv_cvt_pk_bf16_f32 %0, %1, %0"
                     : "+v"(t), "=&v"(u) : "v"(nm), "s"(0xffff0000u));
        xf[d >> 2][d & 3] = t;
      }
    };
    sfor<LEAD>([&](auto I) { ctr(I); });
    STX(48)
    SB();
    hso = ((4 * rb + ws) * 28 + unit1) * 4096;
    float h0;
    auto epi = [&](auto Pc, auto Kc) { constexpr int P = Pc, k = Kc, Q = k >> 4, e = (k >> 1) & 7;
      const float h = fmaxf(acc[P][2 * Q][k & 15], 0.f) * acc[P][2 * Q + 1][k & 15];
      if constexpr (!(k & 1)) { h0 = h; pin(h0); }
      else {
        unsigned t = pk2(h0, h); pin(t);
        eo[e & 3] = t;
        if constexpr ((e & 3) == 3) st16_sc1<Q * 2048 + (e >> 2) * 1024>(eo, lvo, rw, hso);
      }
    };
    unsigned a0, a1;
    auto step = [&](auto Tc, auto Endc) { constexpr int T = Tc, M = T - RA;
      if constexpr (T < Endc && T % 16 == 0) { constexpr int n = T / 16;
        if constexpr (n < 3) asm volatile("s_waitcnt vmcnt(%0)\n\ts_barrier" ::"n"(pos<3>(mP) - pos<3>(eW + n)) : "memory");
        else if constexpr (n == 6) asm volatile("s_cmp_eq_u32 %0, 3\n\ts_cbranch_scc1 .Lnb%=\n\ts_barrier\n.Lnb%=:" ::"s"(ii) : "memory", "scc");
        else asm volatile("s_barrier" ::: "memory");
        if constexpr (n == 0) __builtin_amdgcn_s_setprio(0);
        if constexpr (n == 0) { ST(2) }
        if constexpr (n == 7) { ST(3) }
        pin(wo); a0 = wo + (n % 5) * SL; a1 = a0 ^ 64; pin(a0); pin(a1);
      }
      if constexpr (M >= 0 && M < Endc) { constexpr int n = M / 16, i = M % 16, P = n / 7, q = 2 * (i >> 3) + (i & 1);
        acc[P][q] = mfma32(wf[M % RA], xf[4 * (n % 7) + ((i >> 1) & 3)], (n % 7 == 0 && (i & 6) == 0) ? zero16 : acc[P][q]);
        pin(acc[P][q]);
      }
      SB();
      if constexpr (T < Endc) { constexpr int i = T % 16, o = ((i >> 1) & 3) * 4096 + (i & 1) * 1024; wf[T % RA] = tr_at<o, o + 512>(i >= 8 ? a1 : a0); }
      SB();
      ctr(ic<LEAD + T>{});
      if constexpr (Endc == 224 ? (M >= 105 && M < 137) : M == 105) epi(ic<0>{}, ic<M - 105>{});
      if constexpr (Endc == 112 && M > 105) { epi(ic<0>{}, ic<2 * M - 211>{}); epi(ic<0>{}, ic<2 * M - 210>{}); }
      if constexpr (M >= 217) { epi(ic<1>{}, ic<2 * M - 434>{}); epi(ic<1>{}, ic<2 * M - 433>{}); }
      if constexpr (M == HFAT) { ev(ic<3>{}, ic<eHF>{}); hso += (unit2 - unit1) * 4096; }
      SB();
    };
    sfor<112>([&](auto I) { step(I, ic<224>{}); });
    if (!bcu) {
      sfor<112 + RA>([&](auto I) { step(ic<112 + I>{}, ic<224>{}); });
      sfor<18>([&](auto I) { epi(ic<1>{}, ic<14 + I>{}); });
      ST(4)
      tail_c(ic<0>{}, [&] { ST(5) ev(ic<3>{}, ic<eHF>{}); ST(6) });
      finish(ic<0>{}, ic<0>{});
    } else {
      sfor<RA>([&](auto I) { step(ic<112 + I>{}, ic<112>{}); });
      arrive<1>(lds0);
      ST(3)
      sfor<19>([&](auto I) { epi(ic<0>{}, ic<13 + I>{}); });
      await4<1>(lds0);
      ST(4)
      tail_h(ic<2>{});
      finish(ic<1>{}, ic<0>{});
    }
  } else {
    STX(15) run(ic<0>{}, ic<mR>{}); STX(19)
    accA = zero16; sq[0] = 0.f; sq[1] = 0.f;
    u32x4 xa[2][4], ta[2][4];
    auto dots = [&](auto J) { constexpr int j = J;
#pragma unroll
      for (int e = 0; e < 16; ++e) sq[e & 1] = dot2(xa[j & 1][e >> 2][e & 3], xa[j & 1][e >> 2][e & 3], sq[e & 1]);
      pin(sq[0]); pin(sq[1]);
      SB();
    };
    sfor<7>([&](auto J) { constexpr int j = J;
      vbar<j < 3 ? pos<0>(mX + j) - pos<0>(eX + j) : j == 5 ? pos<0>(mX + j) - pos<0>(eEP) : 63>();
      STX(21 + 4 * j)
      if constexpr (j == 0) { ST(1) }
      if constexpr (j == 3) sfor<4>([&](auto I) { xo[I] += 3 * SL; pin(xo[I]); });
      unsigned to = lvo + (lds0 + L_T1 + kof(j) * 4096); pin(to);
      SB();
      sfor<4>([&](auto I) { constexpr int i = I;
        if constexpr (j > 0) { accA = mfma32(ta[~j & 1][i], xa[~j & 1][i], accA); pin(accA); SB(); }
        xa[j & 1][i] = l128_at<(j < 3 ? j : j - 3) * SL>(xo[i]); ta[j & 1][i] = l128_at<i * 1024>(to);
        SB();
      });
      run(ic<0>{}, ic<mX + j>{});
      STX(22 + 4 * j)
      if constexpr (j == 5) mk_tag();
      if constexpr (j > 0) dots(ic<j - 1>{});
      if constexpr (j == 6) { dots(J); sfor<4>([&](auto I) { accA = mfma32(ta[0][I], xa[0][I], accA); }); pin(accA); }
      STX(23 + 4 * j)
    });
    auto period = [&](auto Rc, auto Nc) { constexpr int R = Rc, n = Nc;
      constexpr int N = n < 3 ? 63 : pos<R>(mP + n) - pos<R>(eW + n + (R && n == 5));
      vbar<N>();
      STP(21 + 3 * n)
      if constexpr (n == 0) { __builtin_amdgcn_s_setprio(2); ST(2) }
      if constexpr (n == 7 - 2 * R) { ST(3) }
      if constexpr (n < 13) run(Rc, ic<mP + n>{});
      STP(22 + 3 * n)
    };
    period(ic<0>{}, ic<0>{});
    if (!bcu) {
      sfor<13>([&](auto I) { period(ic<0>{}, ic<I + 1>{}); });
      ST(4)
      tail_h(ic<0>{});
      finish(ic<0>{}, ic<1>{});
    } else {
      sfor<5>([&](auto I) { period(ic<1>{}, ic<I + 1>{}); });
      wait_vm<0>(); pin(fv);
      ST(4)
      poll(fv, ~0xffffffffull, gvo, rw, tag);
      ST(5)
      const unsigned zo = OFF_Z + gi * 32768 + ws * 8192;
      dma4_sc1<5 * SL>(lds0 + ws * 8192, lvo, rw, zo); dma4_sc1<5 * SL + 4096>(lds0 + ws * 8192, lvo, rw, zo + 4096);
      poll(fv, 0xffffffffull, gvo, rw, tag);
      u32x4 wv[4][2];
      const unsigned wp = OFF_W + ((gi >> 3) * 32 + ((gi >> 2) & 1) * 15 + (gi & 3) * 4 + (gi >= 56)) * 8192 + ws * 2048;
      sfor<4>([&](auto K) { ld_sc1<0>(wv[K][0], lvo, rw, wp + K * 8192); ld_sc1<1024>(wv[K][1], lvo, rw, wp + K * 8192); });
      const int l = lane_id(), v = l & 15, q = l >> 4, vp = 4 * (v & 3) + (v >> 2);
      const unsigned za = lds0 + 5 * SL + q * 4096 + vp * 16, ia = lds0 + 7 * SL + v * 256 + (((4 * ws + q) ^ v) * 16), ib = lds0 + 7 * SL + v * 256 + (((2 * q) ^ v) * 16);
      wait_vm<8>();
      ST(6)
      arrive<2>(lds0); await4<2>(lds0);
      ST(12)
      sfor<2>([&](auto Cp) { constexpr int cp = Cp;
        f32x4 dc[8], ds[8];
        sfor<8>([&](auto J) { constexpr int o = J * 256 + cp * 8;
          const u32x4 z0 = tr_at<o, o + 2048>(za), z1 = tr_at<o + 16384, o + 18432>(za);
          dc[J] = mfma16(z0, C64[0], zero); ds[J] = mfma16(z0, S64[0], zero);
          dc[J] = mfma16(z1, C64[1], dc[J]); ds[J] = mfma16(z1, S64[1], ds[J]);
        });
        if constexpr (cp == 0) { wait_vm<0>(); sfor<8>([&](auto I) { pin(wv[I >> 1][I & 1]); }); }
        sfor<2>([&](auto Cb) { constexpr int c = 2 * cp + Cb, o = 2 * Cb;
          u32x4 fr, fi;
          float a[8], bq[8];
#pragma unroll
          for (int j = 0; j < 8; ++j) {
            float x = dc[j][o] + ds[j][o + 1], y = dc[j][o + 1] - ds[j][o]; opq(x); opq(y);
            a[j] = x * t1c[j]; opq(a[j]); a[j] += y * t1s[j]; opq(a[j]); bq[j] = y * t1c[j]; opq(bq[j]); bq[j] -= x * t1s[j]; opq(bq[j]);
          }
#pragma unroll
          for (int e = 0; e < 4; ++e) { fr[e] = pk2(a[2 * e], a[2 * e + 1]); fi[e] = pk2(bq[2 * e], bq[2 * e + 1]); }
          f32x4 qr[2], qi[2];
#pragma unroll
          for (int t = 0; t < 2; ++t) {
            qr[t] = mfma16(C32[t], fr, zero); qi[t] = mfma16(C32[t], fi, zero);
            qr[t] = mfma16(S32[t], fi, qr[t]); qi[t] = mfma16(N32[t], fr, qi[t]);
          }
#pragma unroll
          for (int j = 0; j < 8; ++j) { const int t = j >> 2, r = j & 3;
            const float wre = (r & 1) ? bfhi(wv[c][t][r >> 1]) : bflo(wv[c][t][r >> 1]), wim = (r & 1) ? bfhi(wv[c][t][2 + (r >> 1)]) : bflo(wv[c][t][2 + (r >> 1)]);
            a[j] = qr[t][r] * wre; opq(a[j]); a[j] -= qi[t][r] * wim; opq(a[j]); bq[j] = qr[t][r] * wim; opq(bq[j]); bq[j] += qi[t][r] * wre; opq(bq[j]);
          }
#pragma unroll
          for (int e = 0; e < 4; ++e) { fr[e] = pk2(a[2 * e], a[2 * e + 1]); fi[e] = pk2(bq[2 * e], bq[2 * e + 1]); }
          sfor<2>([&](auto Tc) { constexpr int t = Tc;
            f32x4 rr = mfma16(fr, C32[t], zero), ri = mfma16(fi, C32[t], zero);
            rr = mfma16(fi, N32[t], rr); ri = mfma16(fr, S32[t], ri);
            u32x4 o4;
#pragma unroll
            for (int r = 0; r < 4; ++r) {
              float x = rr[r] * u2c[4 * t + r]; opq(x); x -= ri[r] * u2s[4 * t + r]; opq(x);
              float y = rr[r] * -u2s[4 * t + r]; opq(y); y -= ri[r] * u2c[4 * t + r]; opq(y);
              o4[r] = pk2(x, y);
            }
            lds_st<u32x4, c * 8192 + t * 4096>(ia, o4);
          });
        });
      });
      arrive<3>(lds0); await4<3>(lds0);
      ST(7)
      const unsigned mz = (unsigned)((gi - 60) >> 31), m3 = mz & (unsigned)(((gi & 3) - 3) >> 31);
      const unsigned yv = (unsigned)((v >> 2) * 2048 + q * 64 + (v & 3) * 16);
      sfor<2>([&](auto Tc) { constexpr int t = Tc;
        f32x4 yr[4], yi[4];
        sfor<4>([&](auto Ks) { constexpr int ks = Ks;
          const unsigned a = ib ^ ((ks >> 1) * 128 + (ks & 1) * 16);
          sfor<4>([&](auto K) {
            const u32x4 rf = l128_at<K * 8192 + t * 4096>(a);
            yr[K] = mfma16(rf, M1[ks], ks == 0 ? zero : yr[K]); yi[K] = mfma16(rf, M2[ks], ks == 0 ? zero : yi[K]);
          });
        });
        sfor<4>([&](auto Rc) { constexpr int R = Rc;
          st16_sc1<t * 1024 + R * 256>(u32x4{mz & pk2(yr[0][R], yi[0][R]), mz & pk2(yr[1][R], yi[1][R]), mz & pk2(yr[2][R], yi[2][R]),
                                             m3 & pk2(yr[3][R], yi[3][R])}, yv, rw, OFF_Y + gi * 32768 + ws * 8192);
        });
      });
      ST(13)
      tail_c(ic<1>{}, [&] { wait_vm<0>(); st_flag1(rw, OFF_C + (FY0 + ws * 64 + gi) * 8, tag); });
      finish(ic<1>{}, ic<1>{});
    }
  }
}

extern "C" void kernel_launch(void* const* d_in, const int* in_sizes, int n_in, void* d_out, int out_size, void* d_ws, size_t ws_size,
                              hipStream_t stream) {
  (void)hipFuncSetAttribute((const void*)fwd, hipFuncAttributeMaxDynamicSharedMemorySize, 163840);
  constexpr unsigned FM = 0x00020000u, NX = S_ * D_ * 2, NW = D_ * F_ * 2, NF = S_ * DF_ * 2;
  fwd<<<256, 512, 163840, stream>>>(d_in[0], NX, FM, d_in[4], NW, FM, d_in[5], NW + 1024, FM, d_ws, 16u << 20, FM,
                                    d_in[6], NW, FM, d_in[1], NF, FM, d_in[2], NF, FM, d_out, NX, FM STAMP_PASS);
}
```

```cpp
#include <hip/hip_runtime.h>
#include <hip/hip_bf16.h>
#include <stdint.h>
#include <utility>

typedef __attribute__((ext_vector_type(4))) short s16x4;
typedef __attribute__((ext_vector_type(8))) __bf16 bf16x8;
typedef __attribute__((ext_vector_type(2))) __bf16 bf16x2;
typedef __attribute__((ext_vector_type(4))) float f32x4;
typedef __attribute__((ext_vector_type(16))) float f32x16;
typedef __attribute__((ext_vector_type(2))) float f32x2;
typedef __attribute__((ext_vector_type(4))) unsigned u32x4;
typedef __attribute__((ext_vector_type(2))) unsigned u32x2;
typedef __attribute__((address_space(3))) s16x4* lds_s16x4_p;
typedef unsigned long long u64;
typedef __attribute__((address_space(1))) u64 gu64;
typedef __attribute__((address_space(3))) unsigned lu32;
#define DI __device__ __forceinline__
#define SB() __builtin_amdgcn_sched_barrier(0)
template <int N> using ic = std::integral_constant<int, N>;

constexpr int S_ = 2048, D_ = 448, F_ = 1792, DF_ = 225;

template <class Fn, int... I> DI void sfor_impl(Fn&& f, std::integer_sequence<int, I...>) { (f(ic<I>{}), ...); }
template <int N, class Fn> DI void sfor(Fn&& f) { sfor_impl(f, std::make_integer_sequence<int, N>{}); }

DI f32x4 mfma16(u32x4 a, u32x4 b, f32x4 c) {
  return __builtin_amdgcn_mfma_f32_16x16x32_bf16(__builtin_bit_cast(bf16x8, a), __builtin_bit_cast(bf16x8, b), c, 0, 0, 0);
}
DI f32x16 mfma32(u32x4 a, u32x4 b, f32x16 c) {
  return __builtin_amdgcn_mfma_f32_32x32x16_bf16(__builtin_bit_cast(bf16x8, a), __builtin_bit_cast(bf16x8, b), c, 0, 0, 0);
}
DI unsigned pk2(float lo, float hi) { bf16x2 p; p[0] = (__bf16)lo; p[1] = (__bf16)hi; return __builtin_bit_cast(unsigned, p); }
DI float bflo(unsigned u) { return __builtin_bit_cast(float, u << 16); }
DI float bfhi(unsigned u) { return __builtin_bit_cast(float, u & 0xffff0000u); }
DI float rbf(float f) { return (float)(__bf16)f; }
typedef __attribute__((address_space(3))) const char* lds_cp;
typedef __attribute__((address_space(3))) const u32x4* lds_u32x4_p;
template <int O0, int O1> DI u32x4 tr_at(unsigned a) {
  s16x4 x = __builtin_amdgcn_ds_read_tr16_b64_v4i16((lds_s16x4_p)((lds_cp)(uintptr_t)a + O0));
  s16x4 y = __builtin_amdgcn_ds_read_tr16_b64_v4i16((lds_s16x4_p)((lds_cp)(uintptr_t)a + O1));
  return __builtin_bit_cast(u32x4, __builtin_shufflevector(x, y, 0, 1, 2, 3, 4, 5, 6, 7));
}
template <int O> DI u32x4 l128_at(unsigned a) { return *(lds_u32x4_p)((lds_cp)(uintptr_t)a + O); }
DI unsigned lds_addr(const void* p) { return (unsigned)(uintptr_t)(__attribute__((address_space(3))) const char*)p; }
template <int N> DI void wait_vm() { asm volatile("s_waitcnt vmcnt(%0)" ::"n"(N) : "memory"); }
DI void bar() { asm volatile("s_waitcnt lgkmcnt(0)" ::: "memory"); __builtin_amdgcn_s_barrier(); asm volatile("" ::: "memory"); }
DI u32x4 lds128(const char* p) { return *(const u32x4*)p; }
DI int lane_id() { int r; asm volatile("v_mbcnt_lo_u32_b32 %0, -1, 0\n\tv_mbcnt_hi_u32_b32 %0, -1, %0" : "=v"(r)); return r; }
DI float vcos(float a) { return __builtin_amdgcn_cosf(a); }
DI float vsin(float a) { return __builtin_amdgcn_sinf(a); }

#define STAMP_BEGIN
#define ST(i)
#define STP(i)
#define STX(i)
#define STC(i)
#define STE(mk, i)
#define STF(mk, n)
#define SQD
#define SQ(n, k)
#define SQW
#define STAMP_END(kid)
#define STAMP_ARG
#define STAMP_PASS

constexpr int SL = 16384;
constexpr int XS[7] = {3, 4, 5, 6, 7, 4, 0}, XP[7] = {0, 1, 2, 2, 3, 3, 4};
constexpr int WS[14] = {0, 1, 2, 3, 1, 5, 2, 4, 0, 3, 1, 2, 4, 0}, DS[14] = {6, 7, 8, 6, 7, 5, 6, 7, 5, 6, 4, 7, 5, 6};
constexpr int L_T1 = 8 * SL, L_W = 8 * SL, L_J = L_W + 8192, L_I = 9 * SL, L_ST = L_I + 12288, L_CS = L_ST + 512, L_R2 = L_ST + 1536, L_CT = L_ST + 2560, L_PF = L_ST + 3072;
constexpr int L_TB = 3 * SL, L_TW = SL;
constexpr unsigned OFF_R = 7u << 20, OFF_S = 15u << 19, OFF_Z = 8u << 20, OFF_W = 10u << 20, OFF_Y = 12u << 20, OFF_C = 14u << 20, OFF_END = 16u << 20;
constexpr int FW0 = 256, FZ0 = 1024, FH0 = 2048, FY0 = 16384;
constexpr unsigned ONES = 0x3f803f80u, NEG2 = 0x80008000u;
#define SPINC 200000
constexpr int SPIN_CAP = SPINC;
#define HFAT_ 174
#define PRC_ 3
#define PRH_ 2
constexpr int HFAT = HFAT_, PRC = PRC_, PRH = PRH_;
constexpr int RA = 6;

enum : int { eX = 0, eW = 8, eD = 24, eDR = 40, eH = 56, eXR = 84, eTM = 92, eEP = 100, ePF, eTAG, eDV, eG0, eG1, eG2, eGW, eZ, eZF, eCL0, eCL1, eCW0, eCW1,
             eFLZ, ePZ, eLZ, eFL1, eP1, eR2, eRW, eFL2, eP2, eRES, eAW, mP = 210, mK = 230, mEnd = 500 };
struct Ev { int id, n; };
#define M_(m) {m, 0},
#define X_(j) {eX + j, 4},
#define W_(j, p) {eW + j + 1024 * (3 - p), 1},
#define D_(j, p) {eD + j + 1024 * (3 - p), 1},
#define W4(j) W_(j, 0) W_(j, 1) W_(j, 2) W_(j, 3)
#define D4(j) D_(j, 0) D_(j, 1) D_(j, 2) D_(j, 3)
#define E_(e, p) {e + 1024 * p, 0},
#define V_(e, p, n) {e + 1024 * p, n},
#define H_(s) {eH + s, 2},
#define T_(j, p) E_(eTM + j, p)
#define T4(j) T_(j, 0) T_(j, 1) T_(j, 2) T_(j, 3)
#define WE(j, e) W_(j, 0) E_(e, 0) W_(j, 1) E_(e, 1) W_(j, 2) E_(e, 2) W_(j, 3) E_(e, 3)
#define DE(j, e) D_(j, 0) E_(e, 0) D_(j, 1) E_(e, 1) D_(j, 2) E_(e, 2) D_(j, 3) E_(e, 3)
constexpr Ev SEQ_R[] = {{ePF, 1}, {eW + 0, 4}, {eW + 1, 4},
  M_(mP + 0) E_(eXR + 0, 0) X_(2) V_(eEP, 0, 1) WE(2, eTM + 0) X_(3)
  M_(mP + 1) E_(eXR + 1, 0) X_(4) WE(3, eTM + 1) E_(eG0, 0) E_(eG0, 1)
  M_(mP + 2) E_(eXR + 2, 0) E_(eXR + 3, 0) X_(5) T4(2) X_(6) T4(3) E_(eDV, 0) E_(eTAG, 0) E_(eG0, 2) E_(eG0, 3)
  M_(mP + 3) E_(eXR + 4, 0) E_(eXR + 5, 0) WE(4, eTM + 4) WE(5, eTM + 5) D4(0)
  M_(mP + 4) E_(eXR + 6, 0) WE(6, eTM + 6) V_(eCL0, 0, 1) W4(7) D4(1)
  M_(mP + 5) E_(eZ, 1) E_(eDR + 0, 0) W_(8, 0) W_(8, 1) E_(eZ, 2) W_(8, 2) W_(8, 3) E_(eZ, 3) D_(2, 0) D_(2, 1) E_(eZ, 4) D_(2, 2) D_(2, 3) E_(eCW0, 0)
  M_(mP + 6) V_(eZ, 0, 8) E_(eDR + 1, 0) W4(9) D4(3)
  M_(mP + 7) E_(eDR + 2, 0) WE(10, eG1) D4(4)
  M_(mP + 8) V_(eZF, 0, 1) E_(eDR + 3, 0) WE(11, eG2) D4(5)
  M_(mP + 9) E_(eDR + 4, 0) W4(12) D4(6)
  M_(mP + 10) V_(eFLZ, 0, 1) V_(eCL1, 0, 1) E_(eDR + 5, 0) W4(13) D4(7)
  M_(mP + 11) E_(ePZ, 0) V_(eLZ, 0, 4) E_(eDR + 6, 0) E_(eGW, 0) D4(8) E_(eCW1, 0)
  M_(mP + 12) V_(eFL1, 0, 1) E_(eGW, 1) D4(9)
  M_(mP + 13) E_(eP1, 0) V_(eR2, 0, 1) H_(0) H_(1) H_(2) H_(3) H_(4) H_(5) H_(6)
  M_(mK + 0) H_(7) E_(eRW, 0) M_(mK + 1) H_(8) M_(mK + 2) H_(9) M_(mK + 3) H_(10) M_(mK + 4) H_(11) M_(mK + 5) H_(12) M_(mK + 6) H_(13)
  M_(mK + 7) V_(eFL2, 0, 1) V_(eRES, 0, 1) E_(eDR + 7, 0) E_(eAW, 0) D4(10) M_(mK + 8) D4(11) M_(mK + 9) E_(eDR + 8, 0) M_(mK + 10) D4(12) M_(mK + 11) E_(eDR + 9, 0) M_(mK + 12) D4(13)
  M_(mK + 13) E_(eP2, 0) H_(14) H_(15) H_(16) H_(17) H_(18) H_(19) H_(20)
  M_(mK + 14) H_(21) H_(22) M_(mK + 15) E_(eDR + 10, 0) H_(23) H_(24) M_(mK + 16) H_(25) H_(26) M_(mK + 17) E_(eDR + 11, 0) H_(27) M_(mK + 18) M_(mK + 19) E_(eDR + 12, 0)
  M_(mK + 20) M_(mK + 21) E_(eDR + 13, 0) M_(mK + 22) M_(mK + 23) M_(mK + 24) M_(mK + 25) M_(mK + 26) M_(mK + 27) M_(mEnd)};
constexpr int sidx(int id) { int i = 0; while (SEQ_R[i].id != id && SEQ_R[i].id != mEnd) ++i; return i; }
constexpr int pos(int id) { int p = SEQ_R[sidx(id)].id == id ? 0 : 1000; for (int i = 0, n = sidx(id); i <= n; ++i) p += SEQ_R[i].n; return p; }
constexpr int before(int id) { return pos(id) - SEQ_R[sidx(id)].n; }
constexpr int seglen(int i0) { int n = 0; while (SEQ_R[i0 + n].id % 1024 < mP) ++n; return n; }
constexpr int need(int n) {
  int p = pos(eW + n);
  for (int j = 2; j < 7; ++j) if (XP[j] == n && pos(eX + j) > p) p = pos(eX + j);
  return n == 13 && pos(eLZ) > p ? pos(eLZ) : p;
}

DI unsigned pka(float lo, float hi) { unsigned r; asm volatile("v_cvt_pk_bf16_f32 %0, %1, %2" : "=v"(r) : "v"(lo), "v"(hi)); return r; }
typedef u32x4 srd_t;
typedef __attribute__((ext_vector_type(10))) unsigned u32x10;
DI f32x4 mfma4(u32x2 a, f32x4 c) { return __builtin_amdgcn_mfma_f32_4x4x4bf16_1k(__builtin_bit_cast(s16x4, a), __builtin_bit_cast(s16x4, a), c, 0, 0, 0); }
typedef __attribute__((ext_vector_type(16))) unsigned u32x16;
template <int I> DI srd_t srd_of(u32x16 a) { return __builtin_shufflevector(a, a, 4 * I, 4 * I + 1, 4 * I + 2, 4 * I + 3); }
template <int L> DI void dma2s(unsigned lw, unsigned v0, unsigned v1, srd_t r, unsigned so) {
  asm volatile("s_add_u32 m0, %0, %1\n\ts_nop 0\n\tbuffer_load_dwordx4 %2, %4, %5 offen sc1 lds\n\tbuffer_load_dwordx4 %3, %4, %5 offen offset:1024 sc1 lds"
               ::"s"(lw), "n"(L), "v"(v0), "v"(v1), "s"(r), "s"(so) : "memory", "scc");
}
template <int L> DI void dma4(unsigned lw, unsigned v0, unsigned v1, unsigned v2, unsigned v3, srd_t r0, srd_t r1, unsigned so) {
  asm volatile("s_add_u32 m0, %0, %1\n\ts_nop 0\n\tbuffer_load_dwordx4 %2, %6, %8 offen lds\n\tbuffer_load_dwordx4 %3, %7, %8 offen offset:1024 lds\n\t"
               "buffer_load_dwordx4 %4, %6, %8 offen offset:2048 lds\n\tbuffer_load_dwordx4 %5, %7, %8 offen offset:3072 lds"
               ::"s"(lw), "n"(L), "v"(v0), "v"(v1), "v"(v2), "v"(v3), "s"(r0), "s"(r1), "s"(so) : "memory", "scc");
}
template <int L, int P> DI void dma1(unsigned lw, unsigned v, srd_t r, unsigned so) {
  if constexpr (P == 3) asm volatile("s_add_u32 m0, %0, %1\n\ts_nop 0" ::"s"(lw), "n"(L) : "memory", "scc");
  asm volatile("buffer_load_dwordx4 %0, %1, %2 offen offset:%3 lds" ::"v"(v), "s"(r), "s"(so), "n"(P * 1024) : "memory");
}
template <int OFF> DI void ld_sc1(u32x4& d, unsigned voff, srd_t r, unsigned so) { asm volatile("buffer_load_dwordx4 %0, %1, %2, %3 offen offset:%4 sc1" : "=v"(d) : "v"(voff), "s"(r), "s"(so), "n"(OFF) : "memory"); }
template <int OFF> DI void ld_sc1_over(u32x4& d, unsigned voff, srd_t r, unsigned so) { asm volatile("buffer_load_dwordx4 %0, %1, %2, %3 offen offset:%4 sc1" : "+v"(d) : "v"(voff), "s"(r), "s"(so), "n"(OFF) : "memory"); }
template <int OFF> DI void ld_pl(u32x4& d, unsigned voff, srd_t r, unsigned so) { asm volatile("buffer_load_dwordx4 %0, %1, %2, %3 offen offset:%4" : "=v"(d) : "v"(voff), "s"(r), "s"(so), "n"(OFF) : "memory"); }
template <int OFF> DI void ld8_sc1(u32x2& d, unsigned voff, srd_t r, unsigned so) { asm volatile("buffer_load_dwordx2 %0, %1, %2, %3 offen offset:%4 sc1" : "=v"(d) : "v"(voff), "s"(r), "s"(so), "n"(OFF) : "memory"); }
DI void st4_sc1(unsigned v, unsigned voff, srd_t r, unsigned so) { asm volatile("buffer_store_dword %0, %1, %2, %3 offen sc1" ::"v"(v), "v"(voff), "s"(r), "s"(so) : "memory"); }
DI void ld_u32_sc1(unsigned& d, unsigned voff, srd_t r, unsigned so) { asm volatile("buffer_load_dword %0, %1, %2, %3 offen sc1" : "=v"(d) : "v"(voff), "s"(r), "s"(so) : "memory"); }
DI void ld_flag(u64& d, unsigned voff, srd_t r) { asm volatile("buffer_load_dwordx2 %0, %1, %2, %3 offen sc1" : "=v"(d) : "v"(voff), "s"(r), "s"(OFF_C) : "memory"); }
template <int OFF = 0> DI void st16_sc1(u32x4 v, unsigned voff, srd_t r, unsigned so) {
  asm volatile("buffer_store_dwordx4 %0, %1, %2, %3 offen offset:%4 sc1\n\ts_nop 1" ::"v"(v), "v"(voff), "s"(r), "s"(so), "n"(OFF) : "memory");
}
DI void st_flag1(srd_t r, unsigned so, u64 tag) {
  asm volatile("s_mov_b64 exec, 1\n\tbuffer_store_dwordx2 %0, off, %1, %2 sc1\n\ts_mov_b64 exec, -1" ::"v"(tag), "s"(r), "s"(so) : "memory");
}
template <int M> DI void st_flags(unsigned voff, srd_t r, unsigned so, u64 tag) {
  asm volatile("s_mov_b64 exec, %4\n\tbuffer_store_dwordx2 %0, %1, %2, %3 offen sc1\n\ts_mov_b64 exec, -1" ::"v"(tag), "v"(voff), "s"(r), "s"(so), "n"(M) : "memory");
}
template <class T> DI void pin(T& a) { asm volatile("" : "+v"(a)); }
template <class T> DI void pins(T& a) { asm volatile("" : "+s"(a)); }
template <class T> DI void fresh(T& a) { asm volatile("" : "=v"(a)); }
template <bool K> DI unsigned vmov(unsigned a) { unsigned r; if constexpr (K) asm volatile("v_mov_b32 %0, %1" : "=v"(r) : "v"(a)); else asm volatile("v_mov_b32 %0, 0" : "=v"(r)); return r; }
DI unsigned nb(unsigned a) { return __builtin_amdgcn_mov_dpp(a, 0xb1, 0xf, 0xf, true); }
DI float nb(float a) { return __uint_as_float(nb(__float_as_uint(a))); }
DI u32x4 rot90(u32x4 a, unsigned sg) { u32x4 r = {nb(a[0]) ^ sg, nb(a[1]) ^ sg, nb(a[2]) ^ sg, nb(a[3]) ^ sg}; pin(r); return r; }
DI unsigned selm(u64 m, unsigned a, unsigned b) { unsigned r; asm("v_cndmask_b32_e64 %0, %2, %1, %3" : "=v"(r) : "v"(a), "v"(b), "s"(m)); return r; }
template <int N> DI void vbar() {
  static_assert(N >= 0, "wait");
  asm volatile("s_waitcnt vmcnt(%0) lgkmcnt(0)\n\ts_barrier" ::"n"(N > 63 ? 63 : N) : "memory");
}
template <int N> DI void wvm() { static_assert(N >= 0, "wait"); wait_vm<(N > 63 ? 63 : N)>(); }
#define SLP 1
DI int poll(u64& fv, u64 mask, unsigned voff, srd_t sb, u64 tag) {
  int it = 0;
  if ((~__builtin_amdgcn_ballot_w64(fv == tag) & mask) != 0) {
#pragma unroll 1
    for (it = 1; it < SPIN_CAP; ++it) {
      __builtin_amdgcn_s_sleep(SLP);
      ld_flag(fv, voff, sb); wait_vm<0>(); pin(fv);
      if ((~__builtin_amdgcn_ballot_w64(fv == tag) & mask) == 0) break;
    }
  }
  asm volatile("" ::: "memory");
  return it;
}
#define SN(i, n) (void)(n);
typedef __attribute__((address_space(3))) char* lds_p;
template <class T, int O> DI T lds_ld(unsigned a) { return *(__attribute__((address_space(3))) const T*)((lds_cp)(uintptr_t)a + O); }
template <class T, int O> DI void lds_st(unsigned a, T v) { *(__attribute__((address_space(3))) T*)((lds_p)(uintptr_t)a + O) = v; }
DI void arrive(unsigned ct) { asm volatile("s_mov_b64 exec, 1\n\tds_add_u32 %0, %1\n\ts_mov_b64 exec, -1" ::"v"(ct), "v"(1u) : "memory"); }
DI void await4(unsigned ct) {
#pragma unroll 1
  for (int it = 0; it < SPIN_CAP * 8; ++it) {
    unsigned c;
    asm volatile("ds_read_b32 %0, %1\n\ts_waitcnt lgkmcnt(0)" : "=v"(c) : "v"(ct) : "memory");
    if (__builtin_amdgcn_readfirstlane(c) >= 4) break;
    __builtin_amdgcn_s_sleep(1);
  }
}
template <int N> DI void trig(float a, float d, float k, float* c, float* s) {
  c[0] = vcos(a); s[0] = vsin(a); c[1] = vcos(a + d); s[1] = vsin(a + d);
#pragma unroll
  for (int j = 2; j < N; ++j) { c[j] = __builtin_fmaf(k, c[j - 1], -c[j - 2]); s[j] = __builtin_fmaf(k, s[j - 1], -s[j - 2]); }
}
template <int S> DI void put_part(unsigned a, int l, const f32x16& c0, const f32x16& c1) {
  sfor<8>([&](auto K) { constexpr int k = K; const f32x16& c = k < 4 ? c0 : c1;
    lds_st<f32x4, (k >> 2) * S + (k & 3) * 1024>(a + (l >> 5) * 512 + (((l & 31) ^ (2 * k)) * 16), f32x4{c[4 * (k & 3)], c[4 * (k & 3) + 1], c[4 * (k & 3) + 2], c[4 * (k & 3) + 3]});
  });
}

#define BUF(n) const void* n##p, unsigned n##b, unsigned n##f
__global__ __launch_bounds__(512, 2) void fwd(BUF(x), BUF(g), BUF(u), BUF(w), BUF(d), BUF(a), BUF(c), BUF(o) STAMP_ARG) {
  extern __shared__ __attribute__((aligned(16))) char smem[];
  u32x16 ka, kb;
  unsigned b = blockIdx.x, tid = threadIdx.x;
  STAMP_BEGIN
  {
    u64 pc; unsigned t, pf;
    asm volatile("s_load_dwordx16 %0, %5, 0\n\ts_load_dwordx16 %1, %5, 64\n\ts_getpc_b64 %2\n.Lpf_here:\n\ts_lshr_b32 %3, %4, 3\n\ts_lshl_b32 %3, %3, 11\n\ts_mov_b32 m0, %6"
                 : "=&s"(ka), "=&s"(kb), "=&s"(pc), "=&s"(t), "+s"(b) : "s"(__builtin_amdgcn_kernarg_segment_ptr()), "n"(L_PF) : "memory", "scc");
    asm volatile("v_lshl_add_u32 %0, %1, 2, %2\n\tv_min_u32 %0, .Lpf_end-.Lpf_here, %0\n\tglobal_load_lds_dword %0, %3" : "=&v"(pf), "+v"(tid) : "s"(t), "s"(pc) : "memory");
  }
  SB();
  const int w = __builtin_amdgcn_readfirstlane(tid >> 6), ws = w & 3, wn = w >> 2;
  const int xcd = b & 7, idx = b >> 3;
  const unsigned lds0 = lds_addr(smem), lw = lds0 + ws * 4096;
  const int rb = (xcd & 1) * 8 + (idx & 7), g = xcd >> 1, ii = idx >> 3;
  int i3 = (ii + 1) >> 2, col = i3 & (g ^ idx);
  const int r0 = (unsigned)((idx & 7) + 2 * ii) % 7;
  int unit = (int)(0xdba9d876c543c210ull >> (16 * g + 4 * ii)) & 15;
  pins(col); pins(unit);
  constexpr unsigned CH = 64 * F_ * 2;
  unsigned xvo[4], wvo, wv1, fvo[4];
  xvo[0] = (unsigned)(4 * rb + ws + ((tid & 56) << 3)) * (D_ * 2) + ((tid ^ ((tid >> 4) & 3)) & 7) * 16;
  xvo[1] = (xvo[0] ^ 64) + (512 * D_ * 2 - 1024); xvo[2] = xvo[0] + (1024 * D_ * 2 - 2048); xvo[3] = xvo[1] + (1024 * D_ * 2 - 2048);
  wvo = (unsigned)((16 * ws + ((tid >> 3) & 7)) * F_ * 2 + ((tid ^ ((tid >> 2) & 4)) & 7) * 16 + unit * 128); wv1 = wvo + (8 * F_ * 2 - 2048);
  u64 hm = -(u64)wn; pins(hm);
#pragma unroll
  for (int i = 0; i < 4; ++i) { pin(xvo[i]); fvo[i] = selm(hm, i < 2 ? wvo : wv1, xvo[i]); pin(fvo[i]); }
  pin(wvo); pin(wv1);
  auto kof = [&](int st) { int k = st + r0; return k >= 7 ? k - 7 : k; };
  unsigned fs0 = r0 * (wn ? CH : 128), fs1 = kof(1) * (wn ? CH : 128), fl = lw + (wn ? 0 : 3 * SL); pins(fs0); pins(fs1); pins(fl);
  static_assert(XS[0] == 3 && XS[1] == 4 && WS[0] == 0 && WS[1] == 1, "first loads");
  auto t1gen = [&](int s) {
    const int l = lane_id(), hh = l >> 5, f = 16 * unit + 1 + ((l >> 1) & 15);
    const float cf_ = (float)f * (1.0f / 448), a0 = (float)(16 * s + 8 * hh) * cf_ + (float)(l & 1) * 0.25f;
    const unsigned sg = (unsigned)(f & 1) * NEG2, a = lds0 + L_T1 + s * 1024 + l * 16;
    u32x4 o;
#pragma unroll
    for (int e = 0; e < 4; ++e) o[e] = pk2(vcos(a0 + (float)(2 * e) * cf_), vcos(a0 + (float)(2 * e + 1) * cf_));
    lds_st<u32x4, 0>(a, o); lds_st<u32x4, 14336>(a, o ^ sg);
  };
  SB();
  asm volatile("s_waitcnt lgkmcnt(0)" : "+s"(ka), "+s"(kb));
  const srd_t rx = srd_of<0>(ka), rg = srd_of<1>(ka), rw = srd_of<3>(ka), rd = srd_of<0>(kb), ra = srd_of<1>(kb), rc = srd_of<2>(kb), ro = srd_of<3>(kb);
  const f32x4 zero = {0.f, 0.f, 0.f, 0.f};
  const f32x16 zero16 = {};
  u64 ep, fv, tag;
  auto mk_tag = [&]() __attribute__((always_inline)) { pin(ep);
    tag = (((u64)__builtin_amdgcn_readfirstlane((unsigned)(ep >> 32)) << 32) | (unsigned)__builtin_amdgcn_readfirstlane((unsigned)ep)) + 1; };

  if (!col) {
  srd_t ru = srd_of<2>(ka);
  { const u64 p = ((u64)ru[1] << 32 | ru[0]) - 1024; ru[0] = (unsigned)p; ru[1] = (unsigned)(p >> 32); }
  {
    srd_t f0 = wn ? rg : rx, f1 = wn ? ru : rx; pins(f0); pins(f1);
    STX(50)
    dma4<0>(fl, fvo[0], fvo[1], fvo[2], fvo[3], f0, f1, fs0);
    STX(51)
    SB();
    t1gen(w);
    SB();
    dma4<SL>(fl, fvo[0], fvo[1], fvo[2], fvo[3], f0, f1, fs1);
    STX(52)
    SB();
    lds_st<u32x2, L_CT>(lds0, u32x2{0u, 0u});
    if (w < 6) t1gen(w + 8);
    STX(53)
    SB();
  }
#define XM 1
  const int rt = 4 * (XM == 0 ? xcd : XM == 1 ? g + 4 * (~xcd & 1) : g + 4 * (xcd & 1)) + ((idx >> i3) & 3), dt = min(idx >> 2, 6);
  const int fi = 16 * unit + rb;
  unsigned lvo, wo, xo[4], gvo, ovo, hso, ao, sgf;
  u32x4 xres, z[2], zr[2];
  f32x4 bo[4];
  { const int l = lane_id(), r32 = l & 31, hh = l >> 5;
    lvo = (unsigned)l * 16; pin(lvo);
#pragma unroll
    for (int i = 0; i < 4; ++i) { xo[i] = lw + r32 * 128 + (((2 * i + hh) ^ ((r32 >> 1) & 7)) * 16); pin(xo[i]); } }
  auto xread = [&](auto J, u32x4* d) { constexpr int j = J;
    sfor<4>([&](auto I) { if constexpr (j == 1 || j == 6) { xo[I] += j == 1 ? 4 * SL : -4 * SL; pin(xo[I]); } d[I] = l128_at<(XS[j] & 3) * SL>(xo[I]); }); };
  auto res = [&] {
    const int l = lane_id(), m = 8 * ws + (l >> 3);
    ovo = (unsigned)((64 * m + 2 * rt + wn) * D_ + dt * 64 + (l & 7) * 8) * 2;
    ld_pl<0>(xres, ovo, rx, 0);
  };
  auto finish = [&](auto Wc) { constexpr int WN = Wc;
    bar();
    ST(12)
    const int l = lane_id(), ch = l & 7;
    const unsigned qo = ((8 * ws + (l >> 3)) ^ (2 * ch)) * 16, qc = lds0 + WN * 8192 + ch * 1024 + qo, qh = lds0 + (4 + 2 * WN) * SL + (ch >> 2) * SL + (ch & 3) * 1024 + qo;
    f32x4 a0 = lds_ld<f32x4, 0>(qh), a1 = lds_ld<f32x4, 512>(qh), c0 = lds_ld<f32x4, 0>(qc), c1 = lds_ld<f32x4, 512>(qc);
    const float r = lds_ld<float, 0>(lds0 + L_R2 + WN * 128 + ws * 32 + (l >> 3) * 4);
    sfor<3>([&](auto I) { a0 += lds_ld<f32x4, (I + 1) * 4096>(qh); a1 += lds_ld<f32x4, (I + 1) * 4096 + 512>(qh);
                          c0 += lds_ld<f32x4, (I + 1) * SL>(qc); c1 += lds_ld<f32x4, (I + 1) * SL + 512>(qc); });
    u32x4 o;
    o[0] = pk2(a0[0] * r + (c0[0] + bflo(xres[0])), a0[1] * r + (c0[1] + bfhi(xres[0])));
    o[1] = pk2(a0[2] * r + (c0[2] + bflo(xres[1])), a0[3] * r + (c0[3] + bfhi(xres[1])));
    o[2] = pk2(a1[0] * r + (c1[0] + bflo(xres[2])), a1[1] * r + (c1[1] + bfhi(xres[2])));
    o[3] = pk2(a1[2] * r + (c1[2] + bflo(xres[3])), a1[3] * r + (c1[3] + bfhi(xres[3])));
    asm volatile("buffer_store_dwordx4 %0, %1, %2, 0 offen sc1\n\ts_nop 1" ::"v"(o), "v"(ovo), "s"(ro) : "memory");
    if constexpr (WN == 0) { if (b == 0 && w == 0) st_flag1(rw, OFF_C, tag); }
    else asm volatile(".Lpf_end:");
    STAMP_END(WN)
    __builtin_amdgcn_endpgm();
  };

  if (wn == 0) {
    ld_flag(ep, 0u, rw);
    { const int l = lane_id();
      wo = lds0 + (l >> 5) * 2048 + ((l & 15) >> 2) * 128 + ((l >> 3) & 1) * 64 + ((l >> 4) & 1) * 32 + (l & 3) * 8; pin(wo);
      ao = ONES & (unsigned)((((l >> 2) ^ (l >> 4)) & 1) - 1); pin(ao); }
    u32x4 xf[28], wf[RA], eo;
    f32x16 acc[2][4];
    f32x4 sm = zero, sq[2] = {zero, zero};
    hso = ((4 * rb + ws) * 28 + unit) * 4096;
    float u0, u1, rr;
    unsigned nm;
    auto stats = [&](auto Tc) { constexpr int T = Tc, n = T / 16, k = T % 16 - 2, S = T - 71;
      constexpr int j0 = n < 2 ? n : 2 * n - 2, i = (n == 2 || n == 3) ? (k >= 0 && k < 12 && k % 3 != 2 ? 4 * j0 + k - k / 3 : -1) : (n < 5 && k >= 0 && k < 4 ? 4 * j0 + k : -1);
      if constexpr (i >= 0) { sq[0] = mfma4(xf[i].lo, sq[0]); pin(sq[0]); sm = mfma16(u32x4{ao, ao, ao, ao}, xf[i], sm); pin(sm); sq[1] = mfma4(xf[i].hi, sq[1]); pin(sq[1]); }
      if constexpr (S == 0) { float p[4]; sfor<4>([&](auto I) { p[I] = sq[0][+I] + sq[1][+I]; pin(p[I]); });
        u0 = __uint_as_float(selm(0xaaaaaaaaaaaaaaaaull, __float_as_uint(p[1]), __float_as_uint(p[0]))); u1 = __uint_as_float(selm(0xaaaaaaaaaaaaaaaaull, __float_as_uint(p[3]), __float_as_uint(p[2]))); pin(u0); pin(u1); }
      if constexpr (S == 1) { const unsigned qu = selm(0xccccccccccccccccull, __float_as_uint(u1), __float_as_uint(u0)); const u32x2 q2 = __builtin_amdgcn_permlane32_swap(qu, qu, false, false);
        const float mean = sm[0] * (-1.0f / D_); nm = pk2(mean, mean); u1 = __builtin_fmaf(sm[0], mean, __uint_as_float(q2[0]) + __uint_as_float(q2[1])); pin(nm); pin(u1); }
      if constexpr (S == 2) { u1 = rbf(rbf(fmaxf(u1, 0.f) * (1.0f / D_)) + 1e-5f); pin(u1); }
      if constexpr (S == 3) { u1 = rbf(__builtin_amdgcn_sqrtf(u1)); pin(u1); }
      if constexpr (S == 4) { u1 = __builtin_amdgcn_rcpf(u1); lds_st<float, 0>(lds0 + L_ST + ws * 128 + ((lvo >> 2) & 127), u1); rr = u1 * u1; pin(rr); }
    };
    auto epi = [&](auto Pc, auto Kc) { constexpr int P = Pc, k = Kc, Q = k >> 4, e = (k >> 1) & 7;
      if constexpr (!(k & 1)) asm volatile("v_max_i32 %0, 0, %2\n\tv_max_i32 %1, 0, %3" : "=&v"(u0), "=v"(u1) : "v"(acc[P][2 * Q][2 * e]), "v"(acc[P][2 * Q][2 * e + 1]));
      else { unsigned o;
        asm volatile("v_mul_f32 %1, %1, %3\n\tv_mul_f32 %2, %2, %4\n\tv_cvt_pk_bf16_f32 %0, %1, %2" : "=v"(o), "+v"(u0), "+v"(u1) : "v"(acc[P][2 * Q + 1][2 * e]), "v"(acc[P][2 * Q + 1][2 * e + 1]));
        eo[e & 3] = o;
        if constexpr ((e & 3) == 3) st16_sc1<Q * 2048 + (e >> 2) * 1024>(eo, lvo, rw, hso);
      }
    };
    auto hflag = [&] { st_flags<0x7f>(lvo << 10, rw, OFF_C + FH0 * 8 + (hso >> 9), tag); };
    unsigned a0, a1;
    u32x10 nv;
    SQD
    sfor<232 + RA>([&](auto Tc) { constexpr int T = Tc, M = T - RA, tu = T % 116, mu = M % 116;
      if constexpr (T < 232 && tu % 16 == 0 && tu < 100) { constexpr int n = tu / 16 + 7 * (T / 116);
        SQ(n, 0)
        if constexpr (n == 0) vbar<5>();
        else if constexpr (n == 1) asm volatile("s_waitcnt vmcnt(0)\n\ts_barrier" ::: "memory");
        else if constexpr (n == 5) asm volatile("s_waitcnt lgkmcnt(8)\n\ts_barrier" ::: "memory");
        else asm volatile("s_barrier" ::: "memory");
        SQ(n, 1)
        if constexpr (n == 0) { ST(1) }
        if constexpr (n == 5) { ST(2) }
        if constexpr (n == 7) { ST(3) }
        if constexpr (n == 13) __builtin_amdgcn_s_setprio(PRC);
      }
      if constexpr (T < 232 && ((tu % 16 == 0 && tu < 96) || tu == 100)) { pin(wo); a0 = wo + WS[tu / 16 + 7 * (T / 116)] * SL; a1 = a0 ^ 64; pin(a0); pin(a1); }
      if constexpr (M >= 0) { constexpr int P = M / 116, n = mu / 16 > 6 ? 6 : mu / 16, i = mu < 96 ? mu % 16 : mu - 100, q = i < 0 ? i + 4 : 2 * (i >> 3) + (i & 1);
        if constexpr (i < 0) { constexpr int o = q == 0 ? 2 : q == 1 ? 6 : q == 2 ? 0 : 4; acc[P][q] = mfma32(eo, __builtin_shufflevector(nv, nv, o, o + 1, o + 2, o + 3), acc[P][q]); }
        else acc[P][q] = mfma32(wf[M % RA], xf[4 * n + ((i >> 1) & 3)], (n == 0 && (i & 6) == 0) ? zero16 : acc[P][q]);
        pin(acc[P][q]);
      }
      SB();
      sfor<7>([&](auto J) { if constexpr (T == 16 * XP[J] + (J > 0 && XP[J] == XP[J > 0 ? J - 1 : 0] ? 4 : 0)) xread(J, xf + 4 * J); });
      if constexpr (T < 232) { constexpr int i = tu < 96 ? tu % 16 : tu - 100, o = ((i >> 1) & 3) * 4096 + (i & 1) * 1024;
        if constexpr (i == -4) { pin(lvo); eo = l128_at<0>(lvo + (lds0 + L_CS)); }
        if constexpr (i >= 0) wf[T % RA] = tr_at<o, o + 512>(i >= 8 ? a1 : a0);
      }
      SB();
      stats(Tc);
      if constexpr (T == 26) mk_tag();
      if constexpr (M >= 109 && M < 141) epi(ic<0>{}, ic<M - 109>{});
      if constexpr (M == 117) st4_sc1(__float_as_uint(rr), (lvo >> 2) & 127, rw, OFF_R + (hso >> 5));
      if constexpr (M >= 225) { epi(ic<1>{}, ic<2 * M - 450>{}); epi(ic<1>{}, ic<2 * M - 449>{}); }
      if constexpr (M == HFAT) { wait_vm<0>(); hflag(); hso += 14 * 4096; }
      if constexpr (M > 0 && mu == 90) fresh(nv);
      if constexpr (M > 0 && mu >= 90 && mu < 94) { sfor<10>([&](auto I) { if constexpr (I / 3 == mu - 90) nv[+I] = vmov<I == 2 || I == 7>(nm); }); pin(nv); }
      SB();
    });
    arrive(lds0 + L_CT);
    {
      u32x4 tb[4][4], fw[4];
      f32x4 tw[4];
      { const int l = lane_id();
        const unsigned a = lds0 + L_I + (l >> 2) * 256 + ((ws ^ ((l >> 2) & 7)) << 5) + (l & 3) * 8, c = lvo + lds0, d = lds0 + L_W + ws * 128 + (lvo & 768) * 2 + ((lvo >> 1) & 112);
        z[0] = tr_at<0, 4096>(a); z[1] = tr_at<0, 4096>(a ^ 128);
        sfor<8>([&](auto I) { tb[I >> 2][I & 3] = l128_at<L_TB + I * 1024>(c); });
        sfor<4>([&](auto I) { fw[I] = l128_at<I * 2048>(d); });
        sgf = (unsigned)(l & 1) * NEG2; pin(sgf); }
      SB();
      sfor<18>([&](auto I) { epi(ic<1>{}, ic<14 + I>{}); });
      ST(4)
      SQW
      SB();
      auto step = [&](auto Bc, auto Hc) { constexpr int B = Bc, H = Hc;
        sfor<4>([&](auto I) { const u32x4 t = tb[B ? (I & 2) : (I >> 1)][2 * H + (I & 1)], y = H ? zr[I >> 1] : z[I >> 1];
          bo[I] = B ? mfma16(y, t, H ? bo[I] : zero) : mfma16(t, y, H ? bo[I] : zero); pin(bo[I]); });
        if constexpr (!H) { zr[0] = rot90(z[0], sgf); zr[1] = rot90(z[1], sgf); }
        SB(); };
      step(ic<0>{}, ic<0>{}); step(ic<0>{}, ic<1>{});
      { const unsigned c = lvo + lds0, d = c + ws * 4096;
        sfor<8>([&](auto I) { tb[2 + (I >> 2)][I & 3] = l128_at<L_TB + 8192 + I * 1024>(c); });
        sfor<4>([&](auto I) { tw[I] = lds_ld<f32x4, L_TW + I * 1024>(d); }); }
      sgf ^= NEG2; pin(sgf);
      SB();
      sfor<4>([&](auto Kc) { constexpr int k = Kc, t = k >> 1, i = 2 * (k & 1);
        float x1 = bo[t][i] + bo[2 + t][i], x2 = bo[t][i] - bo[2 + t][i], y1 = bo[t][i + 1] + bo[2 + t][i + 1], y2 = bo[t][i + 1] - bo[2 + t][i + 1];
        unsigned a1 = fw[t][i], a2 = fw[2 + t][i], b1 = fw[t][i + 1], b2 = fw[2 + t][i + 1], p, q, c, d, sg = sgf;
#define NB " quad_perm:[1,0,3,2] row_mask:0xf bank_mask:0xf\n\t"
        asm volatile("v_lshlrev_b32 %0, 16, %4\n\tv_lshlrev_b32 %1, 16, %6\n\tv_bitop3_b32 %4, %12, %13, %4 bitop3:0x48\n\tv_bitop3_b32 %6, %12, %13, %6 bitop3:0x48\n\t"
                     "v_mul_f32 %0, %8, %0\n\tv_mul_f32 %1, %10, %1\n\tv_lshlrev_b32 %2, 16, %5\n\tv_lshlrev_b32 %3, 16, %7\n\t"
                     "v_fmac_f32_dpp %0, %8, %4" NB "v_fmac_f32_dpp %1, %10, %6" NB "v_bitop3_b32 %5, %12, %13, %5 bitop3:0x48\n\tv_bitop3_b32 %7, %12, %13, %7 bitop3:0x48\n\t"
                     "v_fma_f32 %2, %9, %2, %0\n\tv_fma_f32 %3, %11, %3, %1\n\tv_fmac_f32_dpp %2, %9, %5" NB "v_fmac_f32_dpp %3, %11, %7" NB
                     "v_fma_f32 %4, %0, 2.0, -%2\n\tv_fma_f32 %6, %1, 2.0, -%3\n\tv_cvt_pk_bf16_f32 %0, %2, %3\n\tv_cvt_pk_bf16_f32 %1, %4, %6"
                     : "=&v"(p), "=&v"(q), "=&v"(c), "=&v"(d), "+v"(a1), "+v"(a2), "+v"(b1), "+v"(b2) : "v"(x1), "v"(x2), "v"(y1), "v"(y2), "v"(sg), "s"(0xffff0000u));
        z[0][k] = p; z[1][k] = q;
        SB(); });
      asm volatile("s_nop 1");
      step(ic<1>{}, ic<0>{}); step(ic<1>{}, ic<1>{});
      const int l = lane_id();
      unsigned e = lds0 + L_J + (l & 15) * 256 + (((2 * ws + (l >> 5)) ^ (l & 7)) << 4) + ((l >> 1) & 8); pin(e);
      sfor<4>([&](auto Kc) { constexpr int k = Kc;
        float v[4];
#pragma unroll
        for (int u = 0; u < 4; u += 2) { const float re = bo[k][u], im = bo[k][u + 1], c = tw[k][u], s = tw[k][u + 1];
          v[u] = __builtin_fmaf(-im, s, re * c); v[u + 1] = __builtin_fmaf(re, s, im * c); }
        pin(v[0]); pin(v[1]); pin(v[2]); pin(v[3]);
        lds_st<u32x2, (k & 1) * 4096 + (k >> 1) * 128>(e, u32x2{pk2(v[0], v[1]), pk2(v[2], v[3])}); });
      arrive(lds0 + L_CT + 4);
      ST(5)
      wait_vm<0>(); hflag();
      ST(6)
      e = lds0 + L_J + ws * 2048 + (l & 15) * 128 + (((l >> 4) ^ ((l >> 1) & 7)) << 4); pin(e);
      await4(lds0 + L_CT + 4);
      z[0] = l128_at<0>(e); z[1] = l128_at<0>(e ^ 64);
      sfor<4>([&](auto I) { bo[I] = mfma16(z[0], tb[3][I], zero); });
      zr[0] = z[1] ^ NEG2;
      sfor<4>([&](auto I) { bo[I] = mfma16(I < 2 ? z[1] : zr[0], tb[3][I], bo[I]); });
      const unsigned vo = (lvo >> 8) * (2 * 57344) + (lvo & 255);
      sfor<2>([&](auto Y) {
        const u32x4 o = {pk2(bo[0][2 * Y], bo[1][2 * Y]), pk2(bo[0][2 * Y + 1], bo[1][2 * Y + 1]), pk2(bo[2][2 * Y], bo[3][2 * Y]), pk2(bo[2][2 * Y + 1], bo[3][2 * Y + 1])};
        st16_sc1(o, vo, rw, OFF_Y + (8 * ws + Y) * 57344 + fi * 256); });
      ST(7)
      SB();
    }
    {
      const int l = lane_id();
      u32x4 t2[7][2];
      u32x2 y[28];
      const float f0 = (float)(56 * ws + 1 + 28 * (l >> 5));
      float dd[2], ab[2], k2[2];
#pragma unroll
      for (int t = 0; t < 2; ++t) { dd[t] = (float)(64 * dt + 32 * t + (l & 31)) * (-1.0f / 448); ab[t] = __builtin_amdgcn_fractf(f0 * dd[t]); k2[t] = 2.f * vcos(dd[t]); }
      sfor<2>([&](auto Kc) { constexpr int k = Kc;
#pragma unroll
        for (int t = 0; t < 2; ++t) {
          float c[14], s[14];
          trig<14>((float)(14 * k) * dd[t] + ab[t], dd[t], k2[t], c, s);
#pragma unroll
          for (int e = 0; e < 14; ++e) { const int x = 14 * k + e;
            unsigned v = e < 2 ? pk2(c[e], s[e]) : pka(c[e], s[e]); if (e < 2) pin(v); t2[x >> 2][t][2 * (x & 1) + ((x >> 1) & 1)] = v; }
        }
        SB();
        wait_vm<0>();
        if constexpr (k == 0) {
          st_flags<0xff>(lvo << 7, rw, OFF_C + (FY0 + ws * 2048 + fi) * 8, tag);
          ST(13)
          gvo = (unsigned)(FY0 + rt * 256 + 56 * ws) * 8 + (lvo >> 1); ld_flag(fv, gvo, rw);
          res();
        } else {
          pin(fv); pin(xres);
          ST(8)
          SN(15, poll(fv, ~0ull >> 8, gvo, rw, tag))
          ST(9)
          const unsigned yv = (unsigned)((l >> 5) * 7168 + (l & 31) * 8);
          sfor<28>([&](auto I) { ld8_sc1<(I & 15) * 256>(y[I], yv, rw, OFF_Y + rt * 57344 + ws * 14336 + (I >> 4) * 4096); });
        }
        SB();
      });
      f32x16 ac[2][2];
      sfor<7>([&](auto I) { constexpr int i = I;
        wait_vm<24 - 4 * i>();
        if constexpr (i == 0) { ST(10) }
        sfor<4>([&](auto K) { pin(y[4 * i + K]); });
        unsigned p0 = y[4 * i][1], p1 = y[4 * i + 1][1], q0 = y[4 * i + 2][0], q1 = y[4 * i + 3][0];
        asm volatile("v_swap_b32 %0, %2\n\tv_swap_b32 %1, %3\n\ts_nop 1" : "+v"(p0), "+v"(p1), "+v"(q0), "+v"(q1));
        const u32x4 yf[2] = {{y[4 * i][0], p0, y[4 * i + 1][0], p1}, {q0, y[4 * i + 2][1], q1, y[4 * i + 3][1]}};
        sfor<4>([&](auto K) { ac[K >> 1][K & 1] = mfma32(t2[i][K & 1], yf[K >> 1], i ? ac[K >> 1][K & 1] : zero16); });
        SB();
      });
      ST(11)
      put_part<4096>(lds0 + ws * SL, l, ac[0][0], ac[0][1]);
      put_part<4096>(lds0 + ws * SL + 8192, l, ac[1][0], ac[1][1]);
    }
    finish(ic<0>{});
  } else {
    __builtin_amdgcn_s_setprio(2);
    const int rot = (unsigned)(dt + 2 * (rt & 3)) % 7;
    unsigned urot = (unsigned)(0xca86420ca86420ull >> (4 * rot)); pins(urot);
    auto uof = [&](int i) { return 14 * (i / 7) + ((urot >> (4 * (i % 7))) & 15); };
    unsigned dv[4], r2;
    u32x2 cg;
    u32x4 wdf[14][4], hf[28][2], tb[4], xa[2][4], ta[2][4];
    f32x4 tw[4];
    f32x16 accA, ah[2][2];
    float gd, gx;
    auto ev = [&](auto Ic) { constexpr int id = Ic % 1024, pt = Ic / 1024;
      if constexpr (id >= eX && id < eX + 7) { constexpr int j = id - eX;
        dma4<XS[j] * SL>(lw, xvo[0], xvo[1], xvo[2], xvo[3], rx, rx, kof(j) * 128);
      } else if constexpr (id >= eW && id < eW + 14) { constexpr int j = id - eW;
        dma1<WS[j] * SL, pt>(lw, pt < 2 ? wvo : wv1, (pt & 1) ? ru : rg, kof(j % 7) * CH + (j >= 7 ? 14 * 128 : 0));
      } else if constexpr (id >= eD && id < eD + 14) { constexpr int i = id - eD;
        if constexpr (i > 10 && pt == 3) asm volatile("s_waitcnt lgkmcnt(0)" ::: "memory");
        dma1<DS[i] * SL, pt>(lw, dv[pt], rd, uof(i) * (64 * D_ * 2));
      } else if constexpr (id >= eDR && id < eDR + 14) { constexpr int i = id - eDR;
        wvm<pos(id) - pos(eD + i)>();
        if constexpr (i == 3) { ao -= SL; wo -= SL; pin(ao); pin(wo); }
        sfor<4>([&](auto K) { constexpr int o = (DS[i] - (i < 3 ? 5 : 4)) * SL + (K >> 1) * 2048; wdf[i][K] = tr_at<o, o + 1024>((K & 1) ? wo : ao); });
      } else if constexpr (id >= eH && id < eH + 28) { constexpr int s = id - eH;
        const unsigned so = hso + uof(s >> 1) * 4096;
        sfor<2>([&](auto C) { constexpr int c = C;
          if constexpr (s < 7) ld_sc1<(s & 1) * 2048>(hf[s][c], lvo, rw, so + c * 28 * 4096);
          else {
            if constexpr (s < 21) hf[s][c] = hf[s - 7][c];
            else hf[s][c] = wdf[(s - 14) >> 1][2 * (s & 1) + c];
            ld_sc1_over<(s & 1) * 2048>(hf[s][c], lvo, rw, so + c * 28 * 4096);
          }
        });
      } else if constexpr (id >= eXR && id < eXR + 7) { constexpr int j = id - eXR;
        unsigned to = lvo + (lds0 + L_T1 + kof(j) * 4096); pin(to);
        xread(ic<j>{}, xa[j & 1]);
        sfor<4>([&](auto I) { ta[j & 1][I] = l128_at<I * 1024>(to); });
      } else if constexpr (id >= eTM && id < eTM + 7) { constexpr int j = id - eTM;
        accA = mfma32(xa[j & 1][pt], ta[j & 1][pt], j + pt ? accA : zero16); pin(accA);
      } else if constexpr (id == eEP) { ld_flag(ep, 0u, rw);
      } else if constexpr (id == eTAG) { wvm<pos(eTAG) - pos(eEP)>(); mk_tag();
      } else if constexpr (id == eDV) {
        const int l = lane_id();
        dv[0] = (unsigned)((64 * (ws >> 1) + 16 * (ws & 1) + (l >> 3)) * D_ * 2 + dt * 128 + (((l & 7) ^ ((l >> 2) & 4)) * 16));
#pragma unroll
        for (int i = 1; i < 4; ++i) { dv[i] = dv[0] + ((8 * (i & 1) + 16 * (i & 2)) * D_ * 2 - 1024 * i); pin(dv[i]); }
        ao = lw + 5 * SL + (4 * (l >> 5) + ((l & 15) >> 2)) * 128 + ((l >> 3) & 1) * 64 + ((l >> 4) & 1) * 32 + (l & 3) * 8;
        wo = ao ^ 64; pin(ao); pin(wo);
      } else if constexpr (id == eG0) {
        if constexpr (pt == 0) { const int l = lane_id(); sgf = (unsigned)(l & 1) * NEG2; gd = (float)(l & 31) * (1.0f / 32); gx = (float)(8 * (l & 32) + 4 * rb + ws) * (1.0f / 64) * gd; pin(sgf); pin(gd); pin(gx);
          sfor<4>([&](auto I) { fresh(tb[I]); }); }
        const float x = (float)(8 * (pt >> 1) + 2 * (pt & 1)) * gd + gx, y = x + gd;
        tb[0][pt] = pk2(vcos(x), vcos(y)); tb[2][pt] = pk2(vsin(x), vsin(y)); tb[1][pt] = tb[0][pt] ^ sgf; tb[3][pt] = tb[2][pt] ^ sgf;
        sfor<4>([&](auto I) { pin(tb[I]); });
      } else if constexpr (id == eG1) {
        if constexpr (pt == 0) { const int l = lane_id(); gd = (float)(l & 15); gx = (float)((l >> 2) & 12); pin(gd); pin(gx); sfor<4>([&](auto I) { fresh(tb[I]); }); }
        if (ws < 3) {
          const float a = gd * (1.0f / 32), d = a + (float)(ws >> 1) * (1.0f / 64), x = (gx + (float)(16 * (pt >> 1) + 2 * (pt & 1))) * d + a * ((float)(ws & 1) * 0.5f), y = x + d;
          tb[0][pt] = pk2(vcos(x), vcos(y)); tb[2][pt] = pk2(vsin(x), vsin(y)); tb[1][pt] = tb[0][pt] ^ 0x80000000u; tb[3][pt] = tb[2][pt] ^ (0x80000000u >> (16 * (ws & 1)));
        } else {
          sfor<2>([&](auto U) { const float x = (gx + (float)pt) * (gd * (1.0f / 16) + (float)U * (1.0f / 32)), p = vcos(x), q = vsin(x);
            tb[2 * U][pt] = pk2(p, q) ^ 0x80000000u; tb[2 * U + 1][pt] = pk2(q, p); });
        }
        sfor<4>([&](auto I) { pin(tb[I]); });
      } else if constexpr (id == eG2) {
        const float m = (fi == 223 ? 1.0f : 2.0f) / (448 * 2048), n = gd * (1.0f / 1024) + (float)(32 * (pt & 1) + (pt >> 1)) * (1.0f / 2048), x = n * (gx * 0.5f + (float)(8 * ws)), y = x + n;
        float t[4] = {vcos(x) * m, vsin(x) * m, vcos(y) * m, vsin(y) * m}; pin(t[0]); pin(t[1]); pin(t[2]); pin(t[3]);
        tw[pt] = f32x4{t[0], t[1], t[2], t[3]}; pin(tw[pt]);
      } else if constexpr (id == eGW) {
        const unsigned a = lw + lvo, o = a + (ws == 1 ? 2048 : 0);
        if constexpr (pt == 0) { lds_st<u32x4, L_TB>(a, tb[0]); lds_st<u32x4, L_TB + 2048>(a, tb[2]); lds_st<u32x4, L_TB + 1024>(o, tb[1]); lds_st<u32x4, L_TB + 1024>(o ^ 2048, tb[3]); }
        else sfor<4>([&](auto I) { lds_st<f32x4, L_TW + I * 1024>(a, tw[I]); });
      } else if constexpr (id == eCL0 || id == eCL1) {
        ld8_sc1<0>(cg, lvo >> 1, rw, OFF_S + (unit + 14 * (id - eCL0)) * 2048 + ws * 512);
      } else if constexpr (id == eCW0 || id == eCW1) {
        wvm<pos(id) - pos(id - 2)>(); pin(cg);
        if (__builtin_amdgcn_ballot_w64(cg[1] != (unsigned)tag) != 0) {
#pragma unroll 1
          for (int it = 0; it < SPIN_CAP; ++it) {
            __builtin_amdgcn_s_sleep(1);
            ld8_sc1<0>(cg, lvo >> 1, rw, OFF_S + (unit + 14 * (id - eCW0)) * 2048 + ws * 512); wait_vm<0>(); pin(cg);
            if (__builtin_amdgcn_ballot_w64(cg[1] != (unsigned)tag) == 0) break;
          }
        }
        lds_st<unsigned, 0>(lds0 + L_CS + ws * 256 + (lvo >> 2), cg[0]);
      } else if constexpr (id == eZ) {
        if constexpr (pt == 1) { const unsigned a = lds0 + L_ST + ws * 128 + ((lvo >> 5) & 16); sfor<4>([&](auto J) { bo[J] = lds_ld<f32x4, 32 * J>(a); }); }
        if constexpr (pt == 2 || pt == 3) { constexpr int q = pt - 2;
          float v[8];
#pragma unroll
          for (int e = 0; e < 8; ++e) { v[e] = accA[8 * q + e] * bo[2 * q + (e >> 2)][e & 3]; pin(v[e]); }
          z[q] = u32x4{pk2(v[0], v[1]), pk2(v[2], v[3]), pk2(v[4], v[5]), pk2(v[6], v[7])};
          ah[0][0] = mfma32(z[q], tb[q], q ? ah[0][0] : zero16); pin(ah[0][0]);
          zr[q] = rot90(z[q], sgf);
        }
        if constexpr (pt == 4) { ah[0][0] = mfma32(zr[1], tb[3], mfma32(zr[0], tb[2], ah[0][0])); pin(ah[0][0]); }
        if constexpr (pt == 0) {
          const unsigned so = OFF_Z + unit * 131072 + (4 * rb + ws) * 128, vo = (lvo & 512) * 32 + ((lvo >> 2) & 127);
          sfor<8>([&](auto I) { st4_sc1(pk2(ah[0][0][2 * I], ah[0][0][2 * I + 1]), vo, rw, so + ((I >> 1) * 4 + (I & 1)) * 8192); });
        }
      } else if constexpr (id == eZF) {
        wvm<before(eZF) - pos(eZ)>();
        st_flag1(rw, OFF_C + (FZ0 + unit * 64 + rb * 4 + ws) * 8, tag);
      } else if constexpr (id == eFLZ) {
        gvo = selm(0xffffull, (FZ0 + unit * 64 + 16 * ws) * 8, (FW0 + (fi * 2341 >> 16) * 32 + 8 * ws - 16) * 8) + (lvo >> 1);
        ld_flag(fv, gvo, rw);
      } else if constexpr (id == ePZ) { wvm<pos(ePZ) - pos(eFLZ)>(); pin(fv); ST(13) poll(fv, 0xffffffull, gvo, rw, tag); ST(14)
      } else if constexpr (id == eLZ) {
        const unsigned v = lvo ^ ((lvo >> 3) & 96);
        dma2s<L_I>(lds0 + ws * 2048, v, v ^ 128, rw, OFF_Z + fi * 8192 + ws * 2048);
        dma2s<L_W>(lds0 + ws * 2048, lvo, lvo, rw, OFF_W + fi * 8192 + ws * 2048);
      } else if constexpr (id == eR2) {
        ld_u32_sc1(r2, (lvo & 512) * 7 + ((lvo >> 2) & 127), rw, OFF_R + (2 * rt * 28 + (ws >> 1)) * 128);
      } else if constexpr (id == eRW) {
        wvm<pos(eRW) - pos(eR2)>(); pin(r2);
        lds_st<unsigned, 0>(lds0 + L_R2 + ws * 256 + (lvo >> 2), r2);
      } else if constexpr (id == eFL1) {
        const int l = lane_id(), i = min(l >> 1, 13);
        gvo = (unsigned)(FH0 + dt * 2048 + (2 * rt + (l & 1)) * 28 + 2 * i + (ws >> 1)) * 8;
        hso = (2 * rt * 28 + (ws >> 1)) * 4096 + (ws & 1) * 1024;
        ld_flag(fv, gvo, rw);
      } else if constexpr (id == eFL2) { ld_flag(fv, gvo, rw);
      } else if constexpr (id == eP1) { wvm<pos(eP1) - pos(eFL1)>(); pin(fv); ST(5) SN(19, poll(fv, 0x3fffull, gvo, rw, tag)) ST(6)
      } else if constexpr (id == eP2) { wvm<pos(eP2) - pos(eFL2)>(); pin(fv); ST(8) SN(15, poll(fv, 0xfffc000ull, gvo, rw, tag)) ST(9)
      } else if constexpr (id == eRES) { res();
      } else if constexpr (id == eAW) { await4(lds0 + L_CT);
      }
    };
    auto run = [&](auto Mc) { constexpr int R = 0, mk = Mc, i0 = sidx(mk) + 1;
      [[maybe_unused]] u64 _q[10];
      STE(mk, 0)
      sfor<seglen(i0)>([&](auto K) { ev(ic<SEQ_R[i0 + K].id>{}); SB(); STE(mk, 1 + K) });
      STF(mk, seglen(i0))
    };
    SQD
    sfor<14>([&](auto Nc) { constexpr int n = Nc;
      SQ(n, 0)
      vbar<pos(mP + n) - need(n)>();
      SQ(n, 1)
      if constexpr (n == 0) { ST(1) }
      if constexpr (n == 5) { ST(2) }
      if constexpr (n == 7) { ST(3) }
      if constexpr (n == 13) { ST(4) __builtin_amdgcn_s_setprio(PRH); }
      run(ic<mP + n>{});
    });
    SQW
    sfor<28>([&](auto Sc) { constexpr int s = Sc;
      wvm<pos(mK + s) - pos(eH + s)>();
      if constexpr (s == 0) { ST(7) }
      if constexpr (s == 14) { ST(10) }
      sfor<4>([&](auto I) { constexpr int a = I & 1, c = I >> 1;
        if constexpr (!a) pin(hf[s][c]);
        ah[c][a] = mfma32(wdf[s >> 1][2 * (s & 1) + a], hf[s][c], s == 0 ? zero16 : ah[c][a]); pin(ah[c][a]); });
      SB();
      run(ic<mK + s>{});
    });
    ST(11)
    const int l = lane_id();
    put_part<SL>(lw + 4 * SL, l, ah[0][0], ah[0][1]);
    put_part<SL>(lw + 6 * SL, l, ah[1][0], ah[1][1]); pin(xres);
    finish(ic<1>{});
  }
  } else {
    constexpr int RP = 8224, L1 = 0, L2 = 65 * 1024, L3 = 140 * 1024;
    const int t = 4 * xcd + ((idx >> 1) & 3), u2 = (t & 7) < 6 ? 6 * g + (t & 7) : 24 + g;
    const int l = lane_id(), v = l & 15, gq = l >> 4;
    const unsigned lvo = (unsigned)l * 16, so = (unsigned)(128 * (t >> 1) + 32 * (w >> 1) + 16 * (t & 1)) * (DF_ * 2);
    const unsigned cvo = (unsigned)((l >> 3) * F_ * 2 + (l & 7) * 16), cso = (unsigned)((224 * (u2 & 1) + 56 * ws) * F_ * 2 + (u2 >> 1) * 128);
    srd_t r = (w & 1) ? rc : ra, rs = wn ? srd_of<2>(ka) : rg; pins(r); pins(rs);
    u32x4 in[8], wv[2][7], sel;
    ld_flag(ep, 0u, rw);
    sfor<7>([&](auto I) { ld_pl<0>(wv[0][I], cvo, rs, cso + I * (8 * F_ * 2)); });
    dma4<L1>(lds0 + w * RP, lvo, lvo, lvo, lvo, r, r, so); dma4<L1 + 4096>(lds0 + w * RP, lvo, lvo, lvo, lvo, r, r, so + 4096);
    sfor<7>([&](auto I) { ld_pl<14 * 128>(wv[1][I], cvo, rs, cso + I * (8 * F_ * 2)); });
    const int fp = 3 - (v >> 2) + (v & 3);
    unsigned p0 = lds0 + L1 + (v >> 2) * RP + (4 * gq + (v & 3)) * 448 + 8 * gq + 64 * w, p1 = lds0 + L2 + w * 8192 + fp * 256 + (((l >> 2) ^ (4 * (fp & 3))) << 4);
    unsigned p2 = lds0 + L2 + w * 7168 + 1024 + (lvo ^ (gq << 6)), vo = (unsigned)(gq * 8192 + v * 16);
    unsigned p3 = lds0 + L3 + (4 * (l & 1) + (l >> 4) + 2 * (l & 8)) * 16 + (l & 6) * 2;
#pragma unroll
    for (int i = 0; i < 4; ++i) sel[i] = selm(0x0003000300030003ull << (2 * i), 0x3f80u << ((l & 1) * 16), 0u);
    pin(p0); pin(p1); pin(p2); pin(p3); pin(vo); pin(sel);
    auto sums = [&](auto Pc) { constexpr int P = Pc;
      f32x4 c = zero;
      sfor<7>([&](auto I) { pin(wv[P][I]); c = mfma16(sel, wv[P][I], c); });
      lds_st<f32x4, L3 + P * 8192>(lds0 + w * 1024 + lvo, c);
    };
    auto total = [&](auto Pc) { constexpr int P = Pc;
      float s[2][8];
      sfor<16>([&](auto I) { s[I >> 3][I & 7] = lds_ld<float, P * 8192 + (I >> 1) * 1024 + (I & 1) * 128>(p3); });
      u32x4 o;
      sfor<2>([&](auto I) { const float y = ((s[I][0] + s[I][1]) + (s[I][2] + s[I][3])) + ((s[I][4] + s[I][5]) + (s[I][6] + s[I][7])), hi = bfhi(__float_as_uint(y)); o[2 * I] = pk2(hi, y - hi); o[2 * I + 1] = (unsigned)tag; });
      st16_sc1(o, lvo, rw, OFF_S + (u2 + 28 * P) * 1024);
    };
    ST(5)
    wait_vm<15>();
    ST(6)
    sums(ic<0>{});
    bar();
    mk_tag();
    ST(7)
    if (w == 0) { total(ic<0>{}); wait_vm<8>(); } else wait_vm<7>();
    bar();
    ST(1)
    sfor<8>([&](auto I) { in[I] = tr_at<8 * I, 8 * I + 4 * RP>(p0); });
    SB();
    sfor<8>([&](auto I) { lds_st<u32x4, I * 1024>(p1, in[I]); });
    if (w == 0) wait_vm<1>(); else wait_vm<0>();
    sums(ic<1>{});
    bar();
    ST(2)
    sfor<7>([&](auto I) { in[I] = l128_at<I * 1024>(p2); });
    SB();
    sfor<7>([&](auto I) { st16_sc1(in[I], vo, rw, OFF_W + (28 * w + 4 * I) * 8192 + t * 256); });
    if (w == 1) total(ic<1>{});
    ST(3)
    wait_vm<0>();
    st_flag1(rw, OFF_C + (FW0 + w * 32 + t) * 8, tag);
    STAMP_END(2 + wn)
    __builtin_amdgcn_endpgm();
  }
}

extern "C" void kernel_launch(void* const* d_in, const int* in_sizes, int n_in, void* d_out, int out_size, void* d_ws, size_t ws_size,
                              hipStream_t stream) {
  (void)hipFuncSetAttribute((const void*)fwd, hipFuncAttributeMaxDynamicSharedMemorySize, 163840);
  constexpr unsigned FM = 0x00020000u, NX = S_ * D_ * 2, NW = D_ * F_ * 2, NF = S_ * DF_ * 2;
  fwd<<<256, 512, 163840, stream>>>(d_in[0], NX, FM, d_in[4], NW, FM, d_in[5], NW + 1024, FM, d_ws, OFF_END, FM,
                                    d_in[6], NW, FM, d_in[1], NF, FM, d_in[2], NF, FM, d_out, NX, FM STAMP_PASS);
}
```

```cpp
#include <hip/hip_runtime.h>
#include <hip/hip_bf16.h>
#include <stdint.h>
#include <utility>

typedef __attribute__((ext_vector_type(4))) short s16x4;
typedef __attribute__((ext_vector_type(8))) __bf16 bf16x8;
typedef __attribute__((ext_vector_type(2))) __bf16 bf16x2;
typedef __attribute__((ext_vector_type(4))) float f32x4;
typedef __attribute__((ext_vector_type(16))) float f32x16;
typedef __attribute__((ext_vector_type(2))) float f32x2;
typedef __attribute__((ext_vector_type(4))) unsigned u32x4;
typedef __attribute__((ext_vector_type(2))) unsigned u32x2;
typedef __attribute__((address_space(3))) s16x4* lds_s16x4_p;
typedef unsigned long long u64;
typedef __attribute__((address_space(1))) u64 gu64;
typedef __attribute__((address_space(3))) unsigned lu32;
#define DI __device__ __forceinline__
#define SB() __builtin_amdgcn_sched_barrier(0)
template <int N> using ic = std::integral_constant<int, N>;

constexpr int S_ = 2048, D_ = 448, F_ = 1792, DF_ = 225;

template <class Fn, int... I> DI void sfor_impl(Fn&& f, std::integer_sequence<int, I...>) { (f(ic<I>{}), ...); }
template <int N, class Fn> DI void sfor(Fn&& f) { sfor_impl(f, std::make_integer_sequence<int, N>{}); }

DI f32x4 mfma16(u32x4 a, u32x4 b, f32x4 c) {
  return __builtin_amdgcn_mfma_f32_16x16x32_bf16(__builtin_bit_cast(bf16x8, a), __builtin_bit_cast(bf16x8, b), c, 0, 0, 0);
}
DI f32x16 mfma32(u32x4 a, u32x4 b, f32x16 c) {
  return __builtin_amdgcn_mfma_f32_32x32x16_bf16(__builtin_bit_cast(bf16x8, a), __builtin_bit_cast(bf16x8, b), c, 0, 0, 0);
}
DI unsigned pk2(float lo, float hi) { bf16x2 p; p[0] = (__bf16)lo; p[1] = (__bf16)hi; return __builtin_bit_cast(unsigned, p); }
DI float bflo(unsigned u) { return __builtin_bit_cast(float, u << 16); }
DI float bfhi(unsigned u) { return __builtin_bit_cast(float, u & 0xffff0000u); }
DI float rbf(float f) { return (float)(__bf16)f; }
typedef __attribute__((address_space(3))) const char* lds_cp;
typedef __attribute__((address_space(3))) const u32x4* lds_u32x4_p;
template <int O0, int O1> DI u32x4 tr_at(unsigned a) {
  s16x4 x = __builtin_amdgcn_ds_read_tr16_b64_v4i16((lds_s16x4_p)((lds_cp)(uintptr_t)a + O0));
  s16x4 y = __builtin_amdgcn_ds_read_tr16_b64_v4i16((lds_s16x4_p)((lds_cp)(uintptr_t)a + O1));
  return __builtin_bit_cast(u32x4, __builtin_shufflevector(x, y, 0, 1, 2, 3, 4, 5, 6, 7));
}
template <int O> DI u32x4 l128_at(unsigned a) { return *(lds_u32x4_p)((lds_cp)(uintptr_t)a + O); }
DI unsigned lds_addr(const void* p) { return (unsigned)(uintptr_t)(__attribute__((address_space(3))) const char*)p; }
template <int N> DI void wait_vm() { asm volatile("s_waitcnt vmcnt(%0)" ::"n"(N) : "memory"); }
DI void bar() { asm volatile("s_waitcnt lgkmcnt(0)" ::: "memory"); __builtin_amdgcn_s_barrier(); asm volatile("" ::: "memory"); }
DI u32x4 lds128(const char* p) { return *(const u32x4*)p; }
DI int lane_id() { int r; asm volatile("v_mbcnt_lo_u32_b32 %0, -1, 0\n\tv_mbcnt_hi_u32_b32 %0, -1, %0" : "=v"(r)); return r; }
DI float vcos(float a) { return __builtin_amdgcn_cosf(a); }
DI float vsin(float a) { return __builtin_amdgcn_sinf(a); }

#define STAMP_BEGIN
#define ST(i)
#define STP(i)
#define STX(i)
#define STC(i)
#define STE(mk, i)
#define STF(mk, n)
#define SQD
#define SQ(n, k)
#define SQW
#define STAMP_END(kid)
#define STAMP_ARG
#define STAMP_PASS

constexpr int SL = 16384;
constexpr int XS[7] = {3, 4, 5, 6, 7, 4, 0}, XP[7] = {0, 1, 2, 2, 3, 3, 4};
constexpr int WS[14] = {0, 1, 2, 3, 1, 5, 2, 4, 0, 3, 1, 2, 4, 0}, DS[14] = {6, 7, 8, 6, 7, 5, 6, 7, 5, 6, 4, 1, 2, 0};
constexpr int L_T1 = 8 * SL, L_W = 8 * SL, L_J = L_W + 8192, L_I = 9 * SL, L_ST = L_I + 12288, L_CS = L_ST + 512, L_R2 = L_ST + 1536, L_CT = L_ST + 2560, L_PF = L_ST + 3072;
constexpr int L_TB = 3 * SL, L_C0 = L_CT, L_C1 = L_CT + 4, L_C2 = L_CT + 8, L_C3 = L_CT + 12, L_C4 = L_CT + 16, L_C7 = L_CT + 28;
constexpr unsigned OFF_R = 7u << 20, OFF_S = 15u << 19, OFF_Z = 8u << 20, OFF_W = 10u << 20, OFF_Y = 12u << 20, OFF_C = 14u << 20, OFF_END = 16u << 20;
constexpr int FW0 = 256, FZ0 = 1024, FH0 = 2048, FY0 = 32768;
constexpr unsigned ONES = 0x3f803f80u, NEG2 = 0x80008000u;
#define SPINC 200000
constexpr int SPIN_CAP = SPINC;
#define HFAT_ 154
#define PRC_ 3
#define PRH_ 2
#define PRN 12
constexpr int HFAT = HFAT_, PRC = PRC_, PRH = PRH_;
#define EXP 0
#define TWAT 196
#define M2 8
#define MY 14
#define TD 1
#define PRE 3
#define SQV 1
#define EPS 2
#define M1 0
constexpr int RA = 6;

struct Cos { double v[2048]; };
constexpr Cos mk_cos() {
  Cos c{};
  for (int k = 0; k <= 512; ++k) { const double x = k * (3.14159265358979323846 / 1024); double t = 1, s = 1;
    for (int i = 1; i < 14; ++i) { t *= -x * x / ((2 * i - 1) * (2 * i)); s += t; }
    if (k == 512) s = 0;
    c.v[k] = c.v[(2048 - k) & 2047] = s; c.v[1024 - k] = c.v[1024 + k] = -s; }
  return c;
}
constexpr Cos COS = mk_cos();
constexpr float cs(int k) { return (float)COS.v[k & 2047]; }
constexpr float sn(int k) { return (float)COS.v[(k - 512) & 2047]; }
constexpr unsigned bfr(float f) { const unsigned u = __builtin_bit_cast(unsigned, f); return (u + 0x7fff + ((u >> 16) & 1)) >> 16; }
constexpr unsigned pkc(float lo, float hi) { return bfr(lo) | bfr(hi) << 16; }
struct Tab { unsigned tb[16][64][4]; float tw[16][64][4]; };
constexpr Tab mk_tab() {
  Tab t{};
  for (int g = 0; g < 4; ++g) for (int l = 0; l < 64; ++l) for (int p = 0; p < 4; ++p) { const int a = l & 15, b = (l >> 2) & 12;
    if (g < 3) {
      const int d = 32 * (2 * a + (g >> 1)), x = (b + 16 * (p >> 1) + 2 * (p & 1)) * d + 32 * a * (g & 1), o = 2 * (g & 1);
      t.tb[4 * g][l][p] = pkc(cs(x), cs(x + d)); t.tb[4 * g + 2][l][p] = pkc(sn(x), sn(x + d));
      t.tb[4 * g + 1 + o][l][p] = t.tb[4 * g][l][p] ^ 0x80000000u; t.tb[4 * g + 3 - o][l][p] = t.tb[4 * g + 2][l][p] ^ (0x80000000u >> (8 * o));
    } else for (int u = 0; u < 2; ++u) {
      const int x = 64 * (b + p) * (2 * a + u);
      t.tb[12 + 2 * u][l][p] = pkc(cs(x), -sn(x)); t.tb[13 + 2 * u][l][p] = pkc(sn(x), cs(x));
    }
    const int n = 2 * a + 32 * (p & 1) + (p >> 1), x = n * (b / 2 + 8 * g);
    const float m = 2.0f / (448 * 2048);
    float* w = t.tw[4 * g + p][l]; w[0] = cs(x) * m; w[1] = sn(x) * m; w[2] = cs(x + n) * m; w[3] = sn(x + n) * m;
  }
  return t;
}
static __device__ __attribute__((aligned(256))) const Tab TAB = mk_tab();

enum : int { eX = 0, eW = 8, eD = 24, eDR = 40, eH = 56, eXR = 84, eTM = 92, eEP = 100, ePF, eTAG, eDV, eG0, eTB, eTD, eZ, eZF, eCL0, eCL1, eCW0, eCW1,
             eFLZ, ePZ, eLZ, eFL1, eP1, eR2, eRW, eFL2, eP2, eRES, eAW, eAR, eTH, mP = 210, mK = 230, mEnd = 500 };
struct Ev { int id, n; };
#define M_(m) {m, 0},
#define X_(j) {eX + j, 4},
#define W_(j, p) {eW + j + 1024 * (3 - p), 1},
#define D_(j, p) {eD + j + 1024 * (3 - p), 1},
#define W4(j) W_(j, 0) W_(j, 1) W_(j, 2) W_(j, 3)
#define D4(j) D_(j, 0) D_(j, 1) D_(j, 2) D_(j, 3)
#define E_(e, p) {e + 1024 * p, 0},
#define V_(e, p, n) {e + 1024 * p, n},
#define H_(s) {eH + s, 2},
#define T_(j, p) E_(eTM + j, p)
#define T4(j) T_(j, 0) T_(j, 1) T_(j, 2) T_(j, 3)
#define WE(j, e) W_(j, 0) E_(e, 0) W_(j, 1) E_(e, 1) W_(j, 2) E_(e, 2) W_(j, 3) E_(e, 3)
#define DE(j, e) D_(j, 0) E_(e, 0) D_(j, 1) E_(e, 1) D_(j, 2) E_(e, 2) D_(j, 3) E_(e, 3)
constexpr Ev SEQ_R[] = {{ePF, 1}, {eW + 0, 4}, {eW + 1, 4},
  M_(mP + 0) E_(eXR + 0, 0) X_(2) V_(eEP, 0, 1) WE(2, eTM + 0) X_(3)
  M_(mP + 1) E_(eXR + 1, 0) X_(4) WE(3, eTM + 1) V_(eTD, 0, TD) E_(eG0, 0) E_(eG0, 1)
  M_(mP + 2) E_(eXR + 2, 0) E_(eXR + 3, 0) X_(5) T4(2) X_(6) T4(3) E_(eDV, 0) E_(eTAG, 0) E_(eG0, 2) E_(eG0, 3)
  M_(mP + 3) E_(eXR + 4, 0) E_(eXR + 5, 0) WE(4, eTM + 4) WE(5, eTM + 5) D4(0)
  M_(mP + 4) E_(eXR + 6, 0) WE(6, eTM + 6) W4(7) D4(1)
  M_(mP + 5) V_(eCL0, 0, 1) E_(eZ, 1) E_(eDR + 0, 0) W_(8, 0) W_(8, 1) E_(eZ, 2) W_(8, 2) W_(8, 3) E_(eZ, 3) D_(2, 0) D_(2, 1) E_(eZ, 4) D_(2, 2) D_(2, 3) E_(eCW0, 0)
  M_(mP + 6) V_(eZ, 0, 8) E_(eDR + 1, 0) W4(9) D4(3)
  M_(mP + 7) E_(eDR + 2, 0) W4(10) D4(4)
  M_(mP + 8) V_(eZF, 0, 1) E_(eDR + 3, 0) W4(11) D4(5)
  M_(mP + 9) E_(eDR + 4, 0) W4(12) D4(6)
  M_(mP + 10) V_(eFLZ, 0, 1) V_(eCL1, 0, 1) W4(13) E_(eDR + 5, 0) D4(7)
  M_(mP + 11) E_(ePZ, 0) V_(eLZ, 0, 4) V_(eTB, 0, 4) E_(eCW1, 0) E_(eAR, 1) E_(eDR + 6, 0)
  V_(eFL1, 0, 1) D4(8) D4(9) E_(eAR, 0) E_(eP1, 0) V_(eR2, 0, 1) H_(0) H_(1) H_(2) H_(3) H_(4) H_(5) H_(6)
  M_(mK + 0) H_(7) E_(eRW, 0) M_(mK + 1) H_(8) M_(mK + 2) H_(9) M_(mK + 3) H_(10) M_(mK + 4) H_(11) M_(mK + 5) H_(12) M_(mK + 6) H_(13)
  M_(mK + 7) V_(eRES, 0, 1) E_(eDR + 7, 0) E_(eAW, 0) D4(10) M_(mK + 8) D4(11) M_(mK + 9) E_(eDR + 8, 0) D4(12) M_(mK + 10) V_(eFL2, 0, 1) D4(13)
  M_(mK + 11) E_(eDR + 9, 0) M_(mK + 12) E_(eP2, 0) H_(14) H_(15) H_(16) H_(17) H_(18) H_(19) E_(eAR, 2) M_(mK + 13) H_(20)
  M_(mK + 14) H_(21) H_(22) M_(mK + 15) E_(eDR + 10, 0) H_(23) H_(24) M_(mK + 16) H_(25) H_(26) M_(mK + 17) E_(eDR + 11, 0) H_(27) M_(mK + 18) M_(mK + 19) E_(eDR + 12, 0)
  M_(mK + 20) M_(mK + 21) E_(eDR + 13, 0) M_(mK + 22) M_(mK + 23) M_(mK + 24) M_(mK + 25) M_(mK + 26) M_(mK + 27) M_(mEnd)};
constexpr int sidx(int id) { int i = 0; while (SEQ_R[i].id != id && SEQ_R[i].id != mEnd) ++i; return i; }
constexpr int pos(int id) { int p = SEQ_R[sidx(id)].id == id ? 0 : 1000; for (int i = 0, n = sidx(id); i <= n; ++i) p += SEQ_R[i].n; return p; }
constexpr int before(int id) { return pos(id) - SEQ_R[sidx(id)].n; }
constexpr int seglen(int i0) { int n = 0; while (SEQ_R[i0 + n].id % 1024 < mP) ++n; return n; }
constexpr int need(int n) {
  int p = pos(eW + n);
  for (int j = 2; j < 7; ++j) if (XP[j] == n && pos(eX + j) > p) p = pos(eX + j);
  return n == 11 ? pos(eW + 12) : p;
}

DI unsigned pka(float lo, float hi) { unsigned r; asm volatile("v_cvt_pk_bf16_f32 %0, %1, %2" : "=v"(r) : "v"(lo), "v"(hi)); return r; }
typedef u32x4 srd_t;
typedef __attribute__((ext_vector_type(10))) unsigned u32x10;
DI f32x4 mfma4(u32x2 a, f32x4 c) { return __builtin_amdgcn_mfma_f32_4x4x4bf16_1k(__builtin_bit_cast(s16x4, a), __builtin_bit_cast(s16x4, a), c, 0, 0, 0); }
typedef __attribute__((ext_vector_type(16))) unsigned u32x16;
template <int I> DI srd_t srd_of(u32x16 a) { return __builtin_shufflevector(a, a, 4 * I, 4 * I + 1, 4 * I + 2, 4 * I + 3); }
template <int L> DI void dma2s(unsigned lw, unsigned v0, unsigned v1, srd_t r, unsigned so) {
  asm volatile("s_add_u32 m0, %0, %1\n\ts_nop 0\n\tbuffer_load_dwordx4 %2, %4, %5 offen sc1 lds\n\tbuffer_load_dwordx4 %3, %4, %5 offen offset:1024 sc1 lds"
               ::"s"(lw), "n"(L), "v"(v0), "v"(v1), "s"(r), "s"(so) : "memory", "scc");
}
template <int L> DI void dma4(unsigned lw, unsigned v0, unsigned v1, unsigned v2, unsigned v3, srd_t r0, srd_t r1, unsigned so) {
  asm volatile("s_add_u32 m0, %0, %1\n\ts_nop 0\n\tbuffer_load_dwordx4 %2, %6, %8 offen lds\n\tbuffer_load_dwordx4 %3, %7, %8 offen offset:1024 lds\n\t"
               "buffer_load_dwordx4 %4, %6, %8 offen offset:2048 lds\n\tbuffer_load_dwordx4 %5, %7, %8 offen offset:3072 lds"
               ::"s"(lw), "n"(L), "v"(v0), "v"(v1), "v"(v2), "v"(v3), "s"(r0), "s"(r1), "s"(so) : "memory", "scc");
}
template <int L, int P> DI void dma1(unsigned lw, unsigned v, srd_t r, unsigned so) {
  if constexpr (P == 3) asm volatile("s_add_u32 m0, %0, %1\n\ts_nop 0" ::"s"(lw), "n"(L) : "memory", "scc");
  asm volatile("buffer_load_dwordx4 %0, %1, %2 offen offset:%3 lds" ::"v"(v), "s"(r), "s"(so), "n"(P * 1024) : "memory");
}
template <int OFF> DI void ld_sc1(u32x4& d, unsigned voff, srd_t r, unsigned so) { asm volatile("buffer_load_dwordx4 %0, %1, %2, %3 offen offset:%4 sc1" : "=v"(d) : "v"(voff), "s"(r), "s"(so), "n"(OFF) : "memory"); }
template <int OFF> DI void ld_sc1_over(u32x4& d, unsigned voff, srd_t r, unsigned so) { asm volatile("buffer_load_dwordx4 %0, %1, %2, %3 offen offset:%4 sc1" : "+v"(d) : "v"(voff), "s"(r), "s"(so), "n"(OFF) : "memory"); }
template <int OFF> DI void ld_pl(u32x4& d, unsigned voff, srd_t r, unsigned so) { asm volatile("buffer_load_dwordx4 %0, %1, %2, %3 offen offset:%4" : "=v"(d) : "v"(voff), "s"(r), "s"(so), "n"(OFF) : "memory"); }
template <int OFF> DI void ld8_sc1(u32x2& d, unsigned voff, srd_t r, unsigned so) { asm volatile("buffer_load_dwordx2 %0, %1, %2, %3 offen offset:%4 sc1" : "=v"(d) : "v"(voff), "s"(r), "s"(so), "n"(OFF) : "memory"); }
DI void st4_sc1(unsigned v, unsigned voff, srd_t r, unsigned so) { asm volatile("buffer_store_dword %0, %1, %2, %3 offen sc1" ::"v"(v), "v"(voff), "s"(r), "s"(so) : "memory"); }
DI void ld_u32_sc1(unsigned& d, unsigned voff, srd_t r, unsigned so) { asm volatile("buffer_load_dword %0, %1, %2, %3 offen sc1" : "=v"(d) : "v"(voff), "s"(r), "s"(so) : "memory"); }
DI void ld_flag(u64& d, unsigned voff, srd_t r) { asm volatile("buffer_load_dwordx2 %0, %1, %2, %3 offen sc1" : "=v"(d) : "v"(voff), "s"(r), "s"(OFF_C) : "memory"); }
template <int OFF = 0> DI void st16_sc1(u32x4 v, unsigned voff, srd_t r, unsigned so) {
  asm volatile("buffer_store_dwordx4 %0, %1, %2, %3 offen offset:%4 sc1\n\ts_nop 1" ::"v"(v), "v"(voff), "s"(r), "s"(so), "n"(OFF) : "memory");
}
DI void st_flag1(srd_t r, unsigned so, u64 tag) {
  asm volatile("s_mov_b64 exec, 1\n\tbuffer_store_dwordx2 %0, off, %1, %2 sc1\n\ts_mov_b64 exec, -1" ::"v"(tag), "s"(r), "s"(so) : "memory");
}
template <int M> DI void st_flags(unsigned voff, srd_t r, unsigned so, u64 tag) {
  asm volatile("s_mov_b64 exec, %4\n\tbuffer_store_dwordx2 %0, %1, %2, %3 offen sc1\n\ts_mov_b64 exec, -1" ::"v"(tag), "v"(voff), "s"(r), "s"(so), "n"(M) : "memory");
}
template <class T> DI void pin(T& a) { asm volatile("" : "+v"(a)); }
template <class T> DI void pins(T& a) { asm volatile("" : "+s"(a)); }
template <class T> DI void fresh(T& a) { asm volatile("" : "=v"(a)); }
template <bool K> DI unsigned vmov(unsigned a) { unsigned r; if constexpr (K) asm volatile("v_mov_b32 %0, %1" : "=v"(r) : "v"(a)); else asm volatile("v_mov_b32 %0, 0" : "=v"(r)); return r; }
DI unsigned nb(unsigned a) { return __builtin_amdgcn_mov_dpp(a, 0xb1, 0xf, 0xf, true); }
DI float nb(float a) { return __uint_as_float(nb(__float_as_uint(a))); }
DI u32x4 rot90(u32x4 a, unsigned sg) { u32x4 r = {nb(a[0]) ^ sg, nb(a[1]) ^ sg, nb(a[2]) ^ sg, nb(a[3]) ^ sg}; pin(r); return r; }
DI unsigned selm(u64 m, unsigned a, unsigned b) { unsigned r; asm("v_cndmask_b32_e64 %0, %2, %1, %3" : "=v"(r) : "v"(a), "v"(b), "s"(m)); return r; }
template <int N> DI void vbar() {
  static_assert(N >= 0, "wait");
  asm volatile("s_waitcnt vmcnt(%0) lgkmcnt(0)\n\ts_barrier" ::"n"(N > 63 ? 63 : N) : "memory");
}
template <int N> DI void wvm() { static_assert(N >= 0, "wait"); wait_vm<(N > 63 ? 63 : N)>(); }
#define SLP 8
DI int poll(u64& fv, u64 mask, unsigned voff, srd_t sb, u64 tag) {
  int it = 0;
  if ((~__builtin_amdgcn_ballot_w64(fv == tag) & mask) != 0) {
#pragma unroll 1
    for (it = 1; it < SPIN_CAP; ++it) {
      __builtin_amdgcn_s_sleep(SLP);
      ld_flag(fv, voff, sb); wait_vm<0>(); pin(fv);
      if ((~__builtin_amdgcn_ballot_w64(fv == tag) & mask) == 0) break;
    }
  }
  asm volatile("" ::: "memory");
  return it;
}
#define SN(i, n) (void)(n);
typedef __attribute__((address_space(3))) char* lds_p;
template <class T, int O> DI T lds_ld(unsigned a) { return *(__attribute__((address_space(3))) const T*)((lds_cp)(uintptr_t)a + O); }
template <class T, int O> DI void lds_st(unsigned a, T v) { *(__attribute__((address_space(3))) T*)((lds_p)(uintptr_t)a + O) = v; }
DI void arrive(unsigned ct) { asm volatile("s_mov_b64 exec, 1\n\tds_add_u32 %0, %1\n\ts_mov_b64 exec, -1" ::"v"(ct), "v"(1u) : "memory"); }
DI void await4(unsigned ct) {
#pragma unroll 1
  for (int it = 0; it < SPIN_CAP * 8; ++it) {
    unsigned c;
    asm volatile("ds_read_b32 %0, %1\n\ts_waitcnt lgkmcnt(0)" : "=v"(c) : "v"(ct) : "memory");
    if (__builtin_amdgcn_readfirstlane(c) >= 4) break;
    __builtin_amdgcn_s_sleep(1);
  }
}
DI void check4(unsigned c, unsigned ct) {
  unsigned s, n;
  asm volatile("s_mov_b32 %2, %4\n1:\n\tv_readfirstlane_b32 %1, %0\n\ts_cmp_ge_u32 %1, 4\n\ts_cbranch_scc1 2f\n\tds_read_b32 %0, %3\n\ts_sub_u32 %2, %2, 1\n\ts_waitcnt lgkmcnt(0)\n\ts_cbranch_scc0 1b\n2:"
               : "+v"(c), "=&s"(s), "=&s"(n) : "v"(ct), "n"(SPIN_CAP * 8) : "memory", "scc");
}
template <int N> DI void trig(float a, float d, float k, float* c, float* s) {
  c[0] = vcos(a); s[0] = vsin(a); c[1] = vcos(a + d); s[1] = vsin(a + d);
#pragma unroll
  for (int j = 2; j < N; ++j) { c[j] = __builtin_fmaf(k, c[j - 1], -c[j - 2]); s[j] = __builtin_fmaf(k, s[j - 1], -s[j - 2]); }
}
template <int S> DI void put_part(unsigned a, int l, const f32x16& c0, const f32x16& c1) {
  sfor<8>([&](auto K) { constexpr int k = K; const f32x16& c = k < 4 ? c0 : c1;
    lds_st<f32x4, (k >> 2) * S + (k & 3) * 1024>(a + (l >> 5) * 512 + (((l & 31) ^ (2 * k)) * 16), f32x4{c[4 * (k & 3)], c[4 * (k & 3) + 1], c[4 * (k & 3) + 2], c[4 * (k & 3) + 3]});
  });
}

#define BUF(n) const void* n##p, unsigned n##b, unsigned n##f
__global__ __launch_bounds__(512, 2) void fwd(BUF(x), BUF(g), BUF(u), BUF(w), BUF(d), BUF(a), BUF(c), BUF(o) STAMP_ARG) {
  extern __shared__ __attribute__((aligned(16))) char smem[];
  u32x16 ka, kb;
  unsigned b = blockIdx.x, tid = threadIdx.x;
  STAMP_BEGIN
  {
    u64 pc; unsigned t, pf;
    asm volatile("s_load_dwordx16 %0, %5, 0\n\ts_load_dwordx16 %1, %5, 64\n\ts_getpc_b64 %2\n.Lpf_here:\n\ts_lshr_b32 %3, %4, 3\n\ts_lshl_b32 %3, %3, 11\n\ts_mov_b32 m0, %6"
                 : "=&s"(ka), "=&s"(kb), "=&s"(pc), "=&s"(t), "+s"(b) : "s"(__builtin_amdgcn_kernarg_segment_ptr()), "n"(L_PF) : "memory", "scc");
    asm volatile("v_lshl_add_u32 %0, %1, 2, %2\n\tv_min_u32 %0, .Lpf_end-.Lpf_here, %0\n\tglobal_load_lds_dword %0, %3" : "=&v"(pf), "+v"(tid) : "s"(t), "s"(pc) : "memory");
  }
  SB();
  const int w = __builtin_amdgcn_readfirstlane(tid >> 6), ws = w & 3, wn = w >> 2;
  const int xcd = b & 7, idx = b >> 3;
  const unsigned lds0 = lds_addr(smem), lw = lds0 + ws * 4096;
  const int rb = (xcd & 1) * 8 + (idx & 7), g = xcd >> 1, ii = idx >> 3;
  int i3 = (ii + 1) >> 2, col = i3 & (g ^ idx);
  const int r0 = (unsigned)((idx & 7) + 2 * ii) % 7;
  int unit = (int)(0xdba9d876c543c210ull >> (16 * g + 4 * ii)) & 15;
  pins(col); pins(unit);
  constexpr unsigned CH = 64 * F_ * 2;
  unsigned xvo[4], wvo, wv1, fvo[4];
  xvo[0] = (unsigned)(4 * rb + ws + ((tid & 56) << 3)) * (D_ * 2) + ((tid ^ ((tid >> 4) & 3)) & 7) * 16;
  xvo[1] = (xvo[0] ^ 64) + (512 * D_ * 2 - 1024); xvo[2] = xvo[0] + (1024 * D_ * 2 - 2048); xvo[3] = xvo[1] + (1024 * D_ * 2 - 2048);
  wvo = (unsigned)((16 * ws + ((tid >> 3) & 7)) * F_ * 2 + ((tid ^ ((tid >> 2) & 4)) & 7) * 16 + unit * 128); wv1 = wvo + (8 * F_ * 2 - 2048);
  u64 hm = -(u64)wn; pins(hm);
#pragma unroll
  for (int i = 0; i < 4; ++i) { pin(xvo[i]); fvo[i] = selm(hm, i < 2 ? wvo : wv1, xvo[i]); pin(fvo[i]); }
  pin(wvo); pin(wv1);
  auto kof = [&](int st) { int k = st + r0; return k >= 7 ? k - 7 : k; };
  unsigned fs0 = r0 * (wn ? CH : 128), fs1 = kof(1) * (wn ? CH : 128), fl = lw + (wn ? 0 : 3 * SL); pins(fs0); pins(fs1); pins(fl);
  static_assert(XS[0] == 3 && XS[1] == 4 && WS[0] == 0 && WS[1] == 1, "first loads");
  auto t1gen = [&](int s) {
    const int l = lane_id(), hh = l >> 5, f = 16 * unit + 1 + ((l >> 1) & 15);
    const float cf_ = (float)f * (1.0f / 448), a0 = (float)(16 * s + 8 * hh) * cf_ + (float)(l & 1) * 0.25f;
    const unsigned sg = (unsigned)(f & 1) * NEG2, a = lds0 + L_T1 + s * 1024 + l * 16;
    u32x4 o;
#pragma unroll
    for (int e = 0; e < 4; ++e) o[e] = pk2(vcos(a0 + (float)(2 * e) * cf_), vcos(a0 + (float)(2 * e + 1) * cf_));
    lds_st<u32x4, 0>(a, o); lds_st<u32x4, 14336>(a, o ^ sg);
  };
  SB();
  asm volatile("s_waitcnt lgkmcnt(0)" : "+s"(ka), "+s"(kb));
  const srd_t rx = srd_of<0>(ka), rg = srd_of<1>(ka), rw = srd_of<3>(ka), rd = srd_of<0>(kb), ra = srd_of<1>(kb), rc = srd_of<2>(kb), ro = srd_of<3>(kb);
  const f32x4 zero = {0.f, 0.f, 0.f, 0.f};
  const f32x16 zero16 = {};
  u64 ep, fv, tag;
  auto mk_tag = [&]() __attribute__((always_inline)) { pin(ep);
    tag = (((u64)__builtin_amdgcn_readfirstlane((unsigned)(ep >> 32)) << 32) | (unsigned)__builtin_amdgcn_readfirstlane((unsigned)ep)) + 1; };

  if (!col) {
  srd_t ru = srd_of<2>(ka);
  { const u64 p = ((u64)ru[1] << 32 | ru[0]) - 1024; ru[0] = (unsigned)p; ru[1] = (unsigned)(p >> 32); }
  {
    srd_t f0 = wn ? rg : rx, f1 = wn ? ru : rx; pins(f0); pins(f1);
    STX(50)
    dma4<0>(fl, fvo[0], fvo[1], fvo[2], fvo[3], f0, f1, fs0);
    STX(51)
    SB();
    t1gen(w);
    SB();
    dma4<SL>(fl, fvo[0], fvo[1], fvo[2], fvo[3], f0, f1, fs1);
    STX(52)
    SB();
    lds_st<u32x4, L_CT>(lds0 + (tid & 16), u32x4{0u, 0u, 0u, 0u});
    if (w < 6) t1gen(w + 8);
    STX(53)
    SB();
  }
#define XM 1
  const int rt = 4 * (XM == 0 ? xcd : XM == 1 ? g + 4 * (~xcd & 1) : g + 4 * (xcd & 1)) + ((idx >> i3) & 3), dt = min(idx >> 2, 6);
  const int fi = 16 * unit + rb;
  unsigned lvo, wo, xo[4], gvo, ovo, hso, ao, sgf;
  u32x4 xres, z[2], zr[2];
  f32x4 bo[4];
  { const int l = lane_id(), r32 = l & 31, hh = l >> 5;
    lvo = (unsigned)l * 16; pin(lvo);
#pragma unroll
    for (int i = 0; i < 4; ++i) { xo[i] = lw + r32 * 128 + (((2 * i + hh) ^ ((r32 >> 1) & 7)) * 16); pin(xo[i]); } }
  auto xread = [&](auto J, u32x4* d) { constexpr int j = J;
    sfor<4>([&](auto I) { if constexpr (j == 1 || j == 6) { xo[I] += j == 1 ? 4 * SL : -4 * SL; pin(xo[I]); } d[I] = l128_at<(XS[j] & 3) * SL>(xo[I]); }); };
  auto res = [&] {
    const int l = lane_id(), m = 8 * ws + (l >> 3);
    ovo = (unsigned)((64 * m + 2 * rt + wn) * D_ + dt * 64 + (l & 7) * 8) * 2;
    ld_pl<0>(xres, ovo, rx, 0);
  };
  auto finish = [&](auto Wc) { constexpr int WN = Wc;
    bar();
    ST(12)
    const int l = lane_id(), ch = l & 7;
    const unsigned qo = ((8 * ws + (l >> 3)) ^ (2 * ch)) * 16, qc = lds0 + 5 * SL + WN * 8192 + ch * 1024 + qo, qh = lds0 + 2 * WN * SL + (ch >> 2) * (SL << WN) + (ch & 3) * 1024 + qo;
    f32x4 a0 = lds_ld<f32x4, 0>(qh), a1 = lds_ld<f32x4, 512>(qh), c0 = lds_ld<f32x4, 0>(qc - 2 * SL), c1 = lds_ld<f32x4, 512>(qc - 2 * SL);
    const float r = lds_ld<float, 0>(lds0 + L_R2 + WN * 128 + ws * 32 + (l >> 3) * 4);
    sfor<3>([&](auto I) { a0 += lds_ld<f32x4, (I + 1) * 4096>(qh); a1 += lds_ld<f32x4, (I + 1) * 4096 + 512>(qh);
                          c0 += lds_ld<f32x4, I * SL>(qc); c1 += lds_ld<f32x4, I * SL + 512>(qc); });
    u32x4 o;
    o[0] = pk2(a0[0] * r + (c0[0] + bflo(xres[0])), a0[1] * r + (c0[1] + bfhi(xres[0])));
    o[1] = pk2(a0[2] * r + (c0[2] + bflo(xres[1])), a0[3] * r + (c0[3] + bfhi(xres[1])));
    o[2] = pk2(a1[0] * r + (c1[0] + bflo(xres[2])), a1[1] * r + (c1[1] + bfhi(xres[2])));
    o[3] = pk2(a1[2] * r + (c1[2] + bflo(xres[3])), a1[3] * r + (c1[3] + bfhi(xres[3])));
    asm volatile("buffer_store_dwordx4 %0, %1, %2, 0 offen sc1\n\ts_nop 1" ::"v"(o), "v"(ovo), "s"(ro) : "memory");
    if constexpr (WN == 1) asm volatile(".Lpf_end:");
    STAMP_END(WN)
    __builtin_amdgcn_endpgm();
  };

  if (wn == 0) {
    ld_flag(ep, 0u, rw);
    { const int l = lane_id();
      wo = lds0 + (l >> 5) * 2048 + ((l & 15) >> 2) * 128 + ((l >> 3) & 1) * 64 + ((l >> 4) & 1) * 32 + (l & 3) * 8; pin(wo);
      ao = ONES & (unsigned)((((l >> 2) ^ (l >> 4)) & 1) - 1); pin(ao); }
    u32x4 xf[28], wf[RA], eo;
    f32x16 acc[2][4];
    f32x4 sm = zero, sq[2] = {zero, zero};
    hso = ((4 * rb + ws) * 28 + unit) * 4096;
    float u0, u1, rr;
    unsigned nm;
    auto stats = [&](auto Tc) { constexpr int T = Tc, n = T / 16, k = T % 16 - 2, S = T - 71;
      constexpr int j0 = n < 2 ? n : 2 * n - 2, i = (n == 2 || n == 3) ? (k >= 0 && k < 12 && k % 3 != 2 ? 4 * j0 + k - k / 3 : -1) : (n < 5 && k >= 0 && k < 4 ? 4 * j0 + k : -1);
      if constexpr (i >= 0) { sq[0] = mfma4(xf[i].lo, sq[0]); pin(sq[0]); sm = mfma16(u32x4{ao, ao, ao, ao}, xf[i], sm); pin(sm); sq[1] = mfma4(xf[i].hi, sq[1]); pin(sq[1]); }
      if constexpr (S == 0) { float p[4]; sfor<4>([&](auto I) { p[I] = sq[0][+I] + sq[1][+I]; pin(p[I]); });
        u0 = __uint_as_float(selm(0xaaaaaaaaaaaaaaaaull, __float_as_uint(p[1]), __float_as_uint(p[0]))); u1 = __uint_as_float(selm(0xaaaaaaaaaaaaaaaaull, __float_as_uint(p[3]), __float_as_uint(p[2]))); pin(u0); pin(u1); }
      if constexpr (S == 1) { const unsigned qu = selm(0xccccccccccccccccull, __float_as_uint(u1), __float_as_uint(u0)); const u32x2 q2 = __builtin_amdgcn_permlane32_swap(qu, qu, false, false);
        const float mean = sm[0] * (-1.0f / D_); nm = pk2(mean, mean); u1 = __builtin_fmaf(sm[0], mean, __uint_as_float(q2[0]) + __uint_as_float(q2[1])); pin(nm); pin(u1); }
      if constexpr (S == 2) { u1 = rbf(rbf(fmaxf(u1, 0.f) * (1.0f / D_)) + 1e-5f); pin(u1); }
      if constexpr (S == 3) { u1 = rbf(__builtin_amdgcn_sqrtf(u1)); pin(u1); }
      if constexpr (S == 4) { u1 = __builtin_amdgcn_rcpf(u1); lds_st<float, 0>(lds0 + L_ST + ws * 128 + ((lvo >> 2) & 127), u1); rr = u1 * u1; pin(rr); }
    };
    auto epi = [&](auto Pc, auto Kc) { constexpr int P = Pc, k = Kc, Q = k >> 4, e = (k >> 1) & 7;
      if constexpr (!(k & 1)) asm volatile("v_max_i32 %0, 0, %2\n\tv_max_i32 %1, 0, %3" : "=&v"(u0), "=v"(u1) : "v"(acc[P][2 * Q][2 * e]), "v"(acc[P][2 * Q][2 * e + 1]));
      else { unsigned o;
        asm volatile("v_mul_f32 %1, %1, %3\n\tv_mul_f32 %2, %2, %4\n\tv_cvt_pk_bf16_f32 %0, %1, %2" : "=v"(o), "+v"(u0), "+v"(u1) : "v"(acc[P][2 * Q + 1][2 * e]), "v"(acc[P][2 * Q + 1][2 * e + 1]));
        eo[e & 3] = o;
        if constexpr ((e & 3) == 3) st16_sc1<Q * 2048 + (e >> 2) * 1024>(eo, lvo, rw, hso);
      }
    };
    auto hflag = [&](int p) { st_flags<0x7f>(lvo << 11, rw, OFF_C + (FH0 + p * 2048 + (4 * rb + ws) * 16 + unit) * 8, tag); };
    unsigned a0, a1, cs;
    f32x4 tw[4];
    u32x10 nv;
    SQD
    sfor<232 + RA>([&](auto Tc) { constexpr int T = Tc, M = T - RA, tu = T % 116, mu = M % 116;
      if constexpr (T < 232 && tu % 16 == 0 && tu < 100) { constexpr int n = tu / 16 + 7 * (T / 116);
        SQ(n, 0)
        if constexpr (n == 0) vbar<5>();
        else if constexpr (n == 1) asm volatile("s_waitcnt vmcnt(0)\n\ts_barrier" ::: "memory");
        else if constexpr (n == 5) asm volatile("s_waitcnt lgkmcnt(8)\n\ts_barrier" ::: "memory");
        else if constexpr (n == 13) check4(cs, lds0 + L_C2);
        else if constexpr (n < 12) asm volatile("s_barrier" ::: "memory");
        SQ(n, 1)
        if constexpr (n == 0) { ST(1) }
        if constexpr (n == 5) { ST(2) }
        if constexpr (n == 7) { ST(3) }
        if constexpr (n == PRN) __builtin_amdgcn_s_setprio(PRC);
      }
      if constexpr (T < 232 && ((tu % 16 == 0 && tu < 96) || tu == 100)) { pin(wo); a0 = wo + WS[tu / 16 + 7 * (T / 116)] * SL; a1 = a0 ^ 64; pin(a0); pin(a1); }
      if constexpr (M >= 0) { constexpr int P = M / 116, n = mu / 16 > 6 ? 6 : mu / 16, i = mu < 96 ? mu % 16 : mu - 100, q = i < 0 ? i + 4 : 2 * (i >> 3) + (i & 1);
        if constexpr (i < 0) { constexpr int o = q == 0 ? 2 : q == 1 ? 6 : q == 2 ? 0 : 4; acc[P][q] = mfma32(eo, __builtin_shufflevector(nv, nv, o, o + 1, o + 2, o + 3), acc[P][q]); }
        else acc[P][q] = mfma32(wf[M % RA], xf[4 * n + ((i >> 1) & 3)], (n == 0 && (i & 6) == 0) ? zero16 : acc[P][q]);
        pin(acc[P][q]);
      }
      SB();
      sfor<7>([&](auto J) { if constexpr (T == 16 * XP[J] + (J > 0 && XP[J] == XP[J > 0 ? J - 1 : 0] ? 4 : 0)) xread(J, xf + 4 * J); });
      if constexpr (T < 232) { constexpr int i = tu < 96 ? tu % 16 : tu - 100, o = ((i >> 1) & 3) * 4096 + (i & 1) * 1024;
        if constexpr (i == -4) { pin(lvo); eo = l128_at<0>(lvo + (lds0 + L_CS)); }
        if constexpr (i >= 0) wf[T % RA] = tr_at<o, o + 512>(i >= 8 ? a1 : a0);
      }
      SB();
      stats(Tc);
      if constexpr (T == 26) mk_tag();
      if constexpr (T == 186 || T == 202) arrive(lds0 + L_C4 + (T - 186) / 4);
      if constexpr (T == 205 || T == 232) { asm volatile("" ::: "memory"); cs = lds_ld<unsigned, T == 205 ? L_C2 : L_C3>(lds0); asm volatile("" ::: "memory"); }
      if constexpr (EPS == 1 && M >= 109 && M < 141) epi(ic<0>{}, ic<M - 109>{});
      if constexpr (EPS == 2 && M >= 109 && M < 125) { epi(ic<0>{}, ic<2 * M - 218>{}); epi(ic<0>{}, ic<2 * M - 217>{}); }
      if constexpr (M == 117) st4_sc1(__float_as_uint(rr), (lvo >> 2) & 127, rw, OFF_R + (hso >> 5));
      if constexpr (M >= 225) { epi(ic<1>{}, ic<2 * M - 450>{}); epi(ic<1>{}, ic<2 * M - 449>{}); }
      if constexpr (M == HFAT) { wait_vm<0>(); hflag(0); hso += 14 * 4096; }
      if constexpr (M >= TWAT && M < TWAT + 8 && !(M & 1)) asm volatile("global_load_dwordx4 %0, %1, %2 offset:%3" : "=v"(tw[(M - TWAT) / 2]) : "v"(lvo), "s"(TAB.tw[4 * ws]), "n"((M - TWAT) * 512) : "memory");
      if constexpr (M > 0 && mu == 90) fresh(nv);
      if constexpr (M > 0 && mu >= 90 && mu < 94) { sfor<10>([&](auto I) { if constexpr (I / 3 == mu - 90) nv[+I] = vmov<I == 2 || I == 7>(nm); }); pin(nv); }
      SB();
    });
    arrive(lds0 + L_C0);
    if constexpr (PRE == 1) __builtin_amdgcn_s_setprio(1);
    check4(cs, lds0 + L_C3);
    {
      u32x4 tb[4][4], fw[4];
      { const int l = lane_id();
        const unsigned a = lds0 + L_I + (l >> 2) * 256 + ((ws ^ ((l >> 2) & 7)) << 5) + (l & 3) * 8, c = lvo + lds0, d = lds0 + L_W + ws * 128 + (lvo & 768) * 2 + ((lvo >> 1) & 112);
        z[0] = tr_at<0, 4096>(a); z[1] = tr_at<0, 4096>(a ^ 128);
        sfor<8>([&](auto I) { tb[I >> 2][I & 3] = l128_at<L_TB + I * 1024>(c); });
        sfor<4>([&](auto I) { fw[I] = l128_at<I * 2048>(d); });
        sgf = (unsigned)(l & 1) * NEG2; pin(sgf); }
      SB();
      sfor<18>([&](auto I) { epi(ic<1>{}, ic<14 + I>{}); });
      ST(4)
      if constexpr (PRE == 2) __builtin_amdgcn_s_setprio(1);
      SQW
      SB();
      auto step = [&](auto Bc, auto Hc) { constexpr int B = Bc, H = Hc;
        sfor<4>([&](auto I) { const u32x4 t = tb[B ? (I & 2) : (I >> 1)][2 * H + (I & 1)], y = H ? zr[I >> 1] : z[I >> 1];
          bo[I] = B ? mfma16(y, t, H ? bo[I] : zero) : mfma16(t, y, H ? bo[I] : zero); pin(bo[I]); });
        if constexpr (!H) { zr[0] = rot90(z[0], sgf); zr[1] = rot90(z[1], sgf); }
        SB(); };
      step(ic<0>{}, ic<0>{}); step(ic<0>{}, ic<1>{});
      sfor<8>([&](auto I) { tb[2 + (I >> 2)][I & 3] = l128_at<L_TB + 8192 + I * 1024>(lvo + lds0); });
      sgf ^= NEG2; pin(sgf);
      SB();
      sfor<4>([&](auto Kc) { constexpr int k = Kc, t = k >> 1, i = 2 * (k & 1);
        float x1 = bo[t][i] + bo[2 + t][i], x2 = bo[t][i] - bo[2 + t][i], y1 = bo[t][i + 1] + bo[2 + t][i + 1], y2 = bo[t][i + 1] - bo[2 + t][i + 1];
        unsigned a1 = fw[t][i], a2 = fw[2 + t][i], b1 = fw[t][i + 1], b2 = fw[2 + t][i + 1], p, q, c, d, sg = sgf;
#define NB " quad_perm:[1,0,3,2] row_mask:0xf bank_mask:0xf\n\t"
        asm volatile("v_lshlrev_b32 %0, 16, %4\n\tv_lshlrev_b32 %1, 16, %6\n\tv_bitop3_b32 %4, %12, %13, %4 bitop3:0x48\n\tv_bitop3_b32 %6, %12, %13, %6 bitop3:0x48\n\t"
                     "v_mul_f32 %0, %8, %0\n\tv_mul_f32 %1, %10, %1\n\tv_lshlrev_b32 %2, 16, %5\n\tv_lshlrev_b32 %3, 16, %7\n\t"
                     "v_fmac_f32_dpp %0, %8, %4" NB "v_fmac_f32_dpp %1, %10, %6" NB "v_bitop3_b32 %5, %12, %13, %5 bitop3:0x48\n\tv_bitop3_b32 %7, %12, %13, %7 bitop3:0x48\n\t"
                     "v_fma_f32 %2, %9, %2, %0\n\tv_fma_f32 %3, %11, %3, %1\n\tv_fmac_f32_dpp %2, %9, %5" NB "v_fmac_f32_dpp %3, %11, %7" NB
                     "v_fma_f32 %4, %0, 2.0, -%2\n\tv_fma_f32 %6, %1, 2.0, -%3\n\tv_cvt_pk_bf16_f32 %0, %2, %3\n\tv_cvt_pk_bf16_f32 %1, %4, %6"
                     : "=&v"(p), "=&v"(q), "=&v"(c), "=&v"(d), "+v"(a1), "+v"(a2), "+v"(b1), "+v"(b2) : "v"(x1), "v"(x2), "v"(y1), "v"(y2), "v"(sg), "s"(0xffff0000u));
        z[0][k] = p; z[1][k] = q;
        SB(); });
      asm volatile("s_nop 1");
      step(ic<1>{}, ic<0>{}); step(ic<1>{}, ic<1>{});
      const int l = lane_id();
      unsigned e = lds0 + L_J + (l & 15) * 256 + (((2 * ws + (l >> 5)) ^ (l & 7)) << 4) + ((l >> 1) & 8); pin(e);
      wait_vm<4>(); sfor<4>([&](auto I) { pin(tw[I]); });
      sfor<4>([&](auto Kc) { constexpr int k = Kc;
        float v[4];
#pragma unroll
        for (int u = 0; u < 4; u += 2) { const float re = bo[k][u], im = bo[k][u + 1], c = tw[k][u], s = tw[k][u + 1];
          v[u] = __builtin_fmaf(-im, s, re * c); v[u + 1] = __builtin_fmaf(re, s, im * c); }
        pin(v[0]); pin(v[1]); pin(v[2]); pin(v[3]);
        lds_st<u32x2, (k & 1) * 4096 + (k >> 1) * 128>(e, u32x2{pk2(v[0], v[1]), pk2(v[2], v[3])}); });
      arrive(lds0 + L_C1);
      ST(5)
      wait_vm<0>(); hflag(1);
      ST(6)
      e = lds0 + L_J + ws * 2048 + (l & 15) * 128 + (((l >> 4) ^ ((l >> 1) & 7)) << 4); pin(e);
      await4(lds0 + L_C1);
      z[0] = l128_at<0>(e); z[1] = l128_at<0>(e ^ 64);
      sfor<4>([&](auto I) { bo[I] = mfma16(z[0], tb[3][I], zero); });
      zr[0] = z[1] ^ NEG2;
      sfor<4>([&](auto I) { bo[I] = mfma16(I < 2 ? z[1] : zr[0], tb[3][I], bo[I]); });
      const unsigned vo = (lvo >> 8) * (2 * 57344) + (lvo & 255);
      sfor<2>([&](auto Y) {
        const u32x4 o = {pk2(bo[0][2 * Y], bo[1][2 * Y]), pk2(bo[0][2 * Y + 1], bo[1][2 * Y + 1]), pk2(bo[2][2 * Y], bo[3][2 * Y]), pk2(bo[2][2 * Y + 1], bo[3][2 * Y + 1])};
        st16_sc1(o, vo, rw, OFF_Y + (8 * ws + Y) * 57344 + fi * 256); });
      ST(7)
      if constexpr (PRE == 3) __builtin_amdgcn_s_setprio(1);
      SB();
    }
    {
      const int l = lane_id();
      u32x4 t2[7][2];
      u32x2 y[28];
      const float f0 = (float)(56 * ws + 1 + 28 * (l >> 5));
      float dd[2], ab[2], k2[2];
#pragma unroll
      for (int t = 0; t < 2; ++t) { dd[t] = (float)(64 * dt + 32 * t + (l & 31)) * (-1.0f / 448); ab[t] = __builtin_amdgcn_fractf(f0 * dd[t]); k2[t] = 2.f * vcos(dd[t]); }
      sfor<2>([&](auto Kc) { constexpr int k = Kc;
#pragma unroll
        for (int t = 0; t < 2; ++t) {
          float c[14], s[14];
          trig<14>((float)(14 * k) * dd[t] + ab[t], dd[t], k2[t], c, s);
          if constexpr (k == 1) { const float m = f0 > 196.f ? 0.5f : 1.f; c[13] *= m; s[13] *= m; }
#pragma unroll
          for (int e = 0; e < 14; ++e) { const int x = 14 * k + e;
            unsigned v = e < 2 ? pk2(c[e], s[e]) : pka(c[e], s[e]); if (e < 2) pin(v); t2[x >> 2][t][2 * (x & 1) + ((x >> 1) & 1)] = v; }
        }
        SB();
        if constexpr (k == 1) { ST(20) __builtin_amdgcn_s_sleep(MY); ld_flag(fv, gvo, rw); }
        wait_vm<0>();
        if constexpr (k == 0) {
          st_flags<0xff>(lvo << 7, rw, OFF_C + (FY0 + ws * 2048 + fi) * 8, tag);
          ST(13)
          gvo = (unsigned)(FY0 + rt * 256 + 56 * ws) * 8 + (lvo >> 1);
          res();
        } else {
          pin(fv); pin(xres);
          ST(8)
          SN(15, poll(fv, ~0ull >> 8, gvo, rw, tag))
          ST(9)
          const unsigned yv = (unsigned)((l >> 5) * 7168 + (l & 31) * 8);
          sfor<28>([&](auto I) { ld8_sc1<(I & 15) * 256>(y[I], yv, rw, OFF_Y + rt * 57344 + ws * 14336 + (I >> 4) * 4096); });
        }
        SB();
      });
      f32x16 ac[2][2];
      sfor<7>([&](auto I) { constexpr int i = I;
        wait_vm<24 - 4 * i>();
        if constexpr (i == 0) { ST(10) }
        sfor<4>([&](auto K) { pin(y[4 * i + K]); });
        unsigned p0 = y[4 * i][1], p1 = y[4 * i + 1][1], q0 = y[4 * i + 2][0], q1 = y[4 * i + 3][0];
        asm volatile("v_swap_b32 %0, %2\n\tv_swap_b32 %1, %3\n\ts_nop 1" : "+v"(p0), "+v"(p1), "+v"(q0), "+v"(q1));
        const u32x4 yf[2] = {{y[4 * i][0], p0, y[4 * i + 1][0], p1}, {q0, y[4 * i + 2][1], q1, y[4 * i + 3][1]}};
        sfor<4>([&](auto K) { ac[K >> 1][K & 1] = mfma32(t2[i][K & 1], yf[K >> 1], i ? ac[K >> 1][K & 1] : zero16); });
        SB();
      });
      ST(11)
      const unsigned pa = lds0 + (ws ? 4 + ws : 3) * SL;
      await4(lds0 + L_C7);
      put_part<4096>(pa, l, ac[0][0], ac[0][1]);
      put_part<4096>(pa + 8192, l, ac[1][0], ac[1][1]);
    }
    finish(ic<0>{});
  } else {
    __builtin_amdgcn_s_setprio(2);
    const int rot = (unsigned)(dt + 2 * (rt & 3)) % 7;
    unsigned urot = (unsigned)(0xca86420ca86420ull >> (4 * rot)); pins(urot);
    auto uof = [&](int i) { return 14 * (i / 7) + ((urot >> (4 * (i % 7))) & 15); };
    unsigned dv[4], r2;
    u32x2 cg;
    u32x4 wdf[14][4], hf[28][2], tb[4], xa[2][4], ta[2][4];
    f32x16 accA, ah[2][2];
    float gd, gx;
    auto ev = [&](auto Ic) { constexpr int id = Ic % 1024, pt = Ic / 1024;
      if constexpr (id >= eX && id < eX + 7) { constexpr int j = id - eX;
        dma4<XS[j] * SL>(lw, xvo[0], xvo[1], xvo[2], xvo[3], rx, rx, kof(j) * 128);
      } else if constexpr (id >= eW && id < eW + 14) { constexpr int j = id - eW;
        dma1<WS[j] * SL, pt>(lw, pt < 2 ? wvo : wv1, (pt & 1) ? ru : rg, kof(j % 7) * CH + (j >= 7 ? 14 * 128 : 0));
      } else if constexpr (id >= eD && id < eD + 14) { constexpr int i = id - eD;
        if constexpr (i == 9 && pt == 3) asm volatile("s_waitcnt lgkmcnt(0)" ::: "memory");
        dma1<DS[i] * SL, pt>(lw, dv[pt], rd, uof(i) * (64 * D_ * 2));
      } else if constexpr (id >= eDR && id < eDR + 14) { constexpr int i = id - eDR;
        wvm<pos(id) - pos(eD + i)>();
        if constexpr (i == 3 || i == 11) { ao -= (i == 3 ? 1 : 4) * SL; wo -= (i == 3 ? 1 : 4) * SL; pin(ao); pin(wo); }
        sfor<4>([&](auto K) { constexpr int o = (DS[i] - (i < 3 ? 5 : i < 11 ? 4 : 0)) * SL + (K >> 1) * 2048; wdf[i][K] = tr_at<o, o + 1024>((K & 1) ? wo : ao); });
      } else if constexpr (id >= eH && id < eH + 28) { constexpr int s = id - eH;
        const unsigned so = hso + uof(s >> 1) * 4096;
        sfor<2>([&](auto C) { constexpr int c = C;
          if constexpr (s < 7) ld_sc1<(s & 1) * 2048>(hf[s][c], lvo, rw, so + c * 28 * 4096);
          else {
            if constexpr (s < 21) hf[s][c] = hf[s - 7][c];
            else hf[s][c] = wdf[(s - 14) >> 1][2 * (s & 1) + c];
            ld_sc1_over<(s & 1) * 2048>(hf[s][c], lvo, rw, so + c * 28 * 4096);
          }
        });
      } else if constexpr (id >= eXR && id < eXR + 7) { constexpr int j = id - eXR;
        unsigned to = lvo + (lds0 + L_T1 + kof(j) * 4096); pin(to);
        xread(ic<j>{}, xa[j & 1]);
        sfor<4>([&](auto I) { ta[j & 1][I] = l128_at<I * 1024>(to); });
      } else if constexpr (id >= eTM && id < eTM + 7) { constexpr int j = id - eTM;
        accA = mfma32(xa[j & 1][pt], ta[j & 1][pt], j + pt ? accA : zero16); pin(accA);
      } else if constexpr (id == eEP) { ld_flag(ep, 0u, rw);
      } else if constexpr (id == eTAG) { wvm<pos(eTAG) - pos(eEP)>(); mk_tag();
      } else if constexpr (id == eDV) {
        const int l = lane_id();
        dv[0] = (unsigned)((64 * (ws >> 1) + 16 * (ws & 1) + (l >> 3)) * D_ * 2 + dt * 128 + (((l & 7) ^ ((l >> 2) & 4)) * 16));
#pragma unroll
        for (int i = 1; i < 4; ++i) { dv[i] = dv[0] + ((8 * (i & 1) + 16 * (i & 2)) * D_ * 2 - 1024 * i); pin(dv[i]); }
        ao = lw + 5 * SL + (4 * (l >> 5) + ((l & 15) >> 2)) * 128 + ((l >> 3) & 1) * 64 + ((l >> 4) & 1) * 32 + (l & 3) * 8;
        wo = ao ^ 64; pin(ao); pin(wo);
      } else if constexpr (id == eG0) {
        if constexpr (pt == 0) { const int l = lane_id(); sgf = (unsigned)(l & 1) * NEG2; gd = (float)(l & 31) * (1.0f / 32); gx = (float)(8 * (l & 32) + 4 * rb + ws) * (1.0f / 64) * gd; pin(sgf); pin(gd); pin(gx);
          sfor<4>([&](auto I) { fresh(tb[I]); }); }
        const float x = (float)(8 * (pt >> 1) + 2 * (pt & 1)) * gd + gx, y = x + gd;
        tb[0][pt] = pk2(vcos(x), vcos(y)); tb[2][pt] = pk2(vsin(x), vsin(y)); tb[1][pt] = tb[0][pt] ^ sgf; tb[3][pt] = tb[2][pt] ^ sgf;
        sfor<4>([&](auto I) { pin(tb[I]); });
      } else if constexpr (id == eTD) {
        if constexpr (TD) asm volatile("s_mov_b32 m0, %0\n\ts_mov_b64 exec, 0x7f\n\tbuffer_load_dword %1, %2, %3 offen lds\n\ts_mov_b64 exec, -1" ::"n"(L_PF), "v"(lvo * 8), "s"(rd), "s"((896 + 4 * fi + ws) * (D_ * 2)) : "memory");
      } else if constexpr (id == eTB) {
        asm volatile("s_add_u32 m0, %0, %1\n\ts_nop 0\n\tglobal_load_lds_dwordx4 %2, %3\n\tglobal_load_lds_dwordx4 %2, %3 offset:1024\n\t"
                     "global_load_lds_dwordx4 %2, %3 offset:2048\n\tglobal_load_lds_dwordx4 %2, %3 offset:3072" ::"s"(lw), "n"(L_TB), "v"(lvo), "s"(TAB.tb[4 * ws]) : "memory", "scc");
      } else if constexpr (id == eCL0 || id == eCL1) {
        ld8_sc1<0>(cg, lvo >> 1, rw, OFF_S + (unit + 14 * (id - eCL0)) * 2048 + ws * 512);
      } else if constexpr (id == eCW0 || id == eCW1) {
        wvm<pos(id) - pos(id - 2)>(); pin(cg);
        if (__builtin_amdgcn_ballot_w64(cg[1] != (unsigned)tag) != 0) {
#pragma unroll 1
          for (int it = 0; it < SPIN_CAP; ++it) {
            __builtin_amdgcn_s_sleep(1);
            ld8_sc1<0>(cg, lvo >> 1, rw, OFF_S + (unit + 14 * (id - eCW0)) * 2048 + ws * 512); wait_vm<0>(); pin(cg);
            if (__builtin_amdgcn_ballot_w64(cg[1] != (unsigned)tag) == 0) break;
          }
        }
        lds_st<unsigned, 0>(lds0 + L_CS + ws * 256 + (lvo >> 2), cg[0]);
      } else if constexpr (id == eZ) {
        if constexpr (pt == 1) { const unsigned a = lds0 + L_ST + ws * 128 + ((lvo >> 5) & 16); sfor<4>([&](auto J) { bo[J] = lds_ld<f32x4, 32 * J>(a); }); }
        if constexpr (pt == 2 || pt == 3) { constexpr int q = pt - 2;
          float v[8];
#pragma unroll
          for (int e = 0; e < 8; ++e) { v[e] = accA[8 * q + e] * bo[2 * q + (e >> 2)][e & 3]; pin(v[e]); }
          z[q] = u32x4{pk2(v[0], v[1]), pk2(v[2], v[3]), pk2(v[4], v[5]), pk2(v[6], v[7])};
          ah[0][0] = mfma32(z[q], tb[q], q ? ah[0][0] : zero16); pin(ah[0][0]);
          zr[q] = rot90(z[q], sgf);
        }
        if constexpr (pt == 4) { ah[0][0] = mfma32(zr[1], tb[3], mfma32(zr[0], tb[2], ah[0][0])); pin(ah[0][0]); }
        if constexpr (pt == 0) {
          const unsigned so = OFF_Z + unit * 131072 + (4 * rb + ws) * 128, vo = (lvo & 512) * 32 + ((lvo >> 2) & 127);
          sfor<8>([&](auto I) { st4_sc1(pk2(ah[0][0][2 * I], ah[0][0][2 * I + 1]), vo, rw, so + ((I >> 1) * 4 + (I & 1)) * 8192); });
        }
      } else if constexpr (id == eZF) {
        wvm<before(eZF) - pos(eZ)>();
        st_flag1(rw, OFF_C + (FZ0 + unit * 64 + rb * 4 + ws) * 8, tag);
      } else if constexpr (id == eFLZ) {
        gvo = selm(0xffffull, (FZ0 + unit * 64 + 16 * ws) * 8, (FW0 + (fi * 2341 >> 16) * 32 + 8 * ws - 16) * 8) + (lvo >> 1);
        ld_flag(fv, gvo, rw);
      } else if constexpr (id == ePZ) { wvm<pos(ePZ) - pos(eFLZ)>(); pin(fv); ST(13) poll(fv, 0xffffffull, gvo, rw, tag); ST(14)
      } else if constexpr (id == eLZ) {
        const unsigned v = lvo ^ ((lvo >> 3) & 96);
        if constexpr (EXP & 2) { dma2s<L_I>(lds0 + ws * 2048, v, v ^ 128, rd, ws * 2048); dma2s<L_W>(lds0 + ws * 2048, lvo, lvo, rd, 8192 + ws * 2048); } else {
        dma2s<L_I>(lds0 + ws * 2048, v, v ^ 128, rw, OFF_Z + fi * 8192 + ws * 2048);
        dma2s<L_W>(lds0 + ws * 2048, lvo, lvo, rw, OFF_W + fi * 8192 + ws * 2048); }
      } else if constexpr (id == eR2) {
        ld_u32_sc1(r2, (lvo & 512) * 7 + ((lvo >> 2) & 127), rw, OFF_R + (2 * rt * 28 + (ws >> 1)) * 128);
      } else if constexpr (id == eRW) {
        wvm<pos(eRW) - pos(eR2)>(); pin(r2);
        lds_st<unsigned, 0>(lds0 + L_R2 + ws * 256 + (lvo >> 2), r2);
      } else if constexpr (id == eFL1) {
        gvo = (unsigned)(FH0 + dt * 4096 + rt * 32) * 8 + ((lvo >> 1) & 255);
        hso = (2 * rt * 28 + (ws >> 1)) * 4096 + (ws & 1) * 1024;
        if constexpr (M1 > 0) __builtin_amdgcn_s_sleep(M1);
        ld_flag(fv, gvo, rw);
      } else if constexpr (id == eFL2) { gvo += 16384; await4(lds0 + L_C1); ST(20) __builtin_amdgcn_s_sleep(M2); ld_flag(fv, gvo, rw);
      } else if constexpr (id == eP1) { wvm<pos(eP1) - pos(eFL1)>(); pin(fv); ST(5) SN(19, poll(fv, 0x3fff3fffull, gvo, rw, tag)) ST(6)
      } else if constexpr (id == eP2) { wvm<pos(eP2) - pos(eFL2)>(); pin(fv); ST(8) SN(15, poll(fv, 0x3fff3fffull, gvo, rw, tag)) ST(9)
        if (b == 0 && ws == 0) st_flag1(rw, OFF_C, tag);
      } else if constexpr (id == eRES) { res();
      } else if constexpr (id == eTH) { wait_vm<pt>();
      } else if constexpr (id == eAW) { await4(lds0 + (pt ? L_C3 + 4 * pt : L_C0));
      } else if constexpr (id == eAR && pt == 2) { arrive(lds0 + L_C7);
      } else if constexpr (id == eAR) { wvm<pos(Ic) - pos(pt ? eW + 13 : eTB)>(); arrive(lds0 + (pt ? L_C2 : L_C3));
      }
    };
    auto run = [&](auto Mc) { constexpr int R = 0, mk = Mc, i0 = sidx(mk) + 1;
      [[maybe_unused]] u64 _q[10];
      STE(mk, 0)
      sfor<seglen(i0)>([&](auto K) { ev(ic<SEQ_R[i0 + K].id>{}); SB(); STE(mk, 1 + K) });
      STF(mk, seglen(i0))
    };
    SQD
    sfor<12>([&](auto Nc) { constexpr int n = Nc;
      SQ(n, 0)
      vbar<pos(mP + n) - need(n)>();
      SQ(n, 1)
      if constexpr (n == 0) { ST(1) }
      if constexpr (n == 5) { ST(2) }
      if constexpr (n == 7) { ST(3) }
      if constexpr (n == 11) { ST(4) __builtin_amdgcn_s_setprio(PRH); }
      run(ic<mP + n>{});
    });
    SQW
    sfor<28>([&](auto Sc) { constexpr int s = Sc;
      wvm<pos(mK + s) - pos(eH + s)>();
      if constexpr (s == 0) { ST(7) }
      if constexpr (s == 14) { ST(10) }
      sfor<4>([&](auto I) { constexpr int a = I & 1, c = I >> 1;
        if constexpr (!a) pin(hf[s][c]);
        ah[c][a] = mfma32(wdf[s >> 1][2 * (s & 1) + a], hf[s][c], s == 0 ? zero16 : ah[c][a]); pin(ah[c][a]); });
      SB();
      run(ic<mK + s>{});
    });
    ST(11)
    const int l = lane_id();
    put_part<SL>(lw, l, ah[0][0], ah[0][1]);
    put_part<2 * SL>(lw + 2 * SL, l, ah[1][0], ah[1][1]); pin(xres);
    finish(ic<1>{});
  }
  } else {
    constexpr int RP = 8224, L1 = 0, L2 = 65 * 1024, L3 = 140 * 1024;
    const int t = 4 * xcd + ((idx >> 1) & 3), u2 = (t & 7) < 6 ? 6 * g + (t & 7) : 24 + g;
    const int l = lane_id(), v = l & 15, gq = l >> 4;
    const unsigned lvo = (unsigned)l * 16, so = (unsigned)(128 * (t >> 1) + 32 * (w >> 1) + 16 * (t & 1)) * (DF_ * 2);
    const unsigned cvo = (unsigned)((l >> 3) * F_ * 2 + (l & 7) * 16), cso = (unsigned)((224 * (u2 & 1) + 56 * ws) * F_ * 2 + (u2 >> 1) * 128);
    srd_t r = (w & 1) ? rc : ra, rs = wn ? srd_of<2>(ka) : rg; pins(r); pins(rs);
    u32x4 in[8], wv[2][7], sel;
    unsigned jk;
    ld_flag(ep, 0u, rw);
    dma4<L1>(lds0 + w * RP, lvo, lvo, lvo, lvo, r, r, so); dma4<L1 + 4096>(lds0 + w * RP, lvo, lvo, lvo, lvo, r, r, so + 4096);
    sfor<7>([&](auto I) { ld_pl<0>(wv[0][I], cvo, rs, cso + I * (8 * F_ * 2)); });
    const int fp = 3 - (v >> 2) + (v & 3);
    unsigned p0 = lds0 + L1 + (v >> 2) * RP + (4 * gq + (v & 3)) * 448 + 8 * gq + 64 * w, p1 = lds0 + L2 + w * 8192 + fp * 256 + (((l >> 2) ^ (4 * (fp & 3))) << 4);
    unsigned p2 = lds0 + L2 + w * 7168 + 1024 + (lvo ^ (gq << 6)), vo = (unsigned)(gq * 8192 + v * 16);
    unsigned p3 = lds0 + L3 + (4 * (l & 1) + (l >> 4) + 2 * (l & 8)) * 16 + (l & 6) * 2;
#pragma unroll
    for (int i = 0; i < 4; ++i) sel[i] = selm(0x0003000300030003ull << (2 * i), 0x3f80u << ((l & 1) * 16), 0u);
    pin(p0); pin(p1); pin(p2); pin(p3); pin(vo); pin(sel);
    auto sums = [&](auto Pc) { constexpr int P = Pc;
      f32x4 c = zero;
      sfor<7>([&](auto I) { pin(wv[P][I]); c = mfma16(sel, wv[P][I], c); });
      lds_st<f32x4, L3 + P * 8192>(lds0 + w * 1024 + lvo, c);
    };
    auto total = [&](auto Pc) { constexpr int P = Pc;
      float s[2][8];
      sfor<16>([&](auto I) { s[I >> 3][I & 7] = lds_ld<float, P * 8192 + (I >> 1) * 1024 + (I & 1) * 128>(p3); });
      u32x4 o;
      sfor<2>([&](auto I) { const float y = ((s[I][0] + s[I][1]) + (s[I][2] + s[I][3])) + ((s[I][4] + s[I][5]) + (s[I][6] + s[I][7])), hi = bfhi(__float_as_uint(y)); o[2 * I] = pk2(hi, y - hi); o[2 * I + 1] = (unsigned)tag; });
      st16_sc1(o, lvo, rw, OFF_S + (u2 + 28 * P) * 1024);
    };
    ST(5)
    wait_vm<0>();
    ST(6)
    sums(ic<0>{});
    bar();
    mk_tag();
    if (w == 0) total(ic<0>{});
    ST(7)
    sfor<8>([&](auto I) { in[I] = tr_at<8 * I, 8 * I + 4 * RP>(p0); });
    { unsigned j; asm volatile("global_load_dword %0, %1, %2" : "=v"(j) : "v"(tid * 64), "s"(&TAB) : "memory"); jk = j; }
    sfor<7>([&](auto I) { ld_pl<14 * 128>(wv[1][I], cvo, rs, cso + I * (8 * F_ * 2)); });
    ST(1)
    SB();
    sfor<8>([&](auto I) { lds_st<u32x4, I * 1024>(p1, in[I]); });
    bar();
    ST(2)
    sfor<7>([&](auto I) { in[I] = l128_at<I * 1024>(p2); });
    SB();
    sfor<7>([&](auto I) { st16_sc1(in[I], vo, rw, OFF_W + (28 * w + 4 * I) * 8192 + t * 256); });
    wait_vm<7>(); pin(jk);
    sums(ic<1>{});
    bar();
    ST(3)
    if (w == 1) { total(ic<1>{}); wait_vm<1>(); } else wait_vm<0>();
    st_flag1(rw, OFF_C + (FW0 + w * 32 + t) * 8, tag);
    STAMP_END(2 + wn)
    __builtin_amdgcn_endpgm();
  }
}

extern "C" void kernel_launch(void* const* d_in, const int* in_sizes, int n_in, void* d_out, int out_size, void* d_ws, size_t ws_size,
                              hipStream_t stream) {
  (void)hipFuncSetAttribute((const void*)fwd, hipFuncAttributeMaxDynamicSharedMemorySize, 163840);
  constexpr unsigned FM = 0x00020000u, NX = S_ * D_ * 2, NW = D_ * F_ * 2, NF = S_ * DF_ * 2;
  fwd<<<256, 512, 163840, stream>>>(d_in[0], NX, FM, d_in[4], NW, FM, d_in[5], NW + 1024, FM, d_ws, OFF_END, FM,
                                    d_in[6], NW, FM, d_in[1], NF, FM, d_in[2], NF, FM, d_out, NX, FM STAMP_PASS);
}
```

```cpp
#include <hip/hip_runtime.h>
#include <hip/hip_bf16.h>
#include <stdint.h>
#include <utility>

typedef __attribute__((ext_vector_type(4))) short s16x4;
typedef __attribute__((ext_vector_type(8))) __bf16 bf16x8;
typedef __attribute__((ext_vector_type(2))) __bf16 bf16x2;
typedef __attribute__((ext_vector_type(4))) float f32x4;
typedef __attribute__((ext_vector_type(16))) float f32x16;
typedef __attribute__((ext_vector_type(2))) float f32x2;
typedef __attribute__((ext_vector_type(4))) unsigned u32x4;
typedef __attribute__((ext_vector_type(2))) unsigned u32x2;
typedef __attribute__((address_space(3))) s16x4* lds_s16x4_p;
typedef unsigned long long u64;
typedef __attribute__((address_space(1))) u64 gu64;
typedef __attribute__((address_space(3))) unsigned lu32;
#define DI __device__ __forceinline__
#define SB() __builtin_amdgcn_sched_barrier(0)
template <int N> using ic = std::integral_constant<int, N>;

constexpr int S_ = 2048, D_ = 448, F_ = 1792, DF_ = 225;

template <class Fn, int... I> DI void sfor_impl(Fn&& f, std::integer_sequence<int, I...>) { (f(ic<I>{}), ...); }
template <int N, class Fn> DI void sfor(Fn&& f) { sfor_impl(f, std::make_integer_sequence<int, N>{}); }

DI f32x4 mfma16(u32x4 a, u32x4 b, f32x4 c) {
  return __builtin_amdgcn_mfma_f32_16x16x32_bf16(__builtin_bit_cast(bf16x8, a), __builtin_bit_cast(bf16x8, b), c, 0, 0, 0);
}
DI f32x16 mfma32(u32x4 a, u32x4 b, f32x16 c) {
  return __builtin_amdgcn_mfma_f32_32x32x16_bf16(__builtin_bit_cast(bf16x8, a), __builtin_bit_cast(bf16x8, b), c, 0, 0, 0);
}
DI unsigned pk2(float lo, float hi) { bf16x2 p; p[0] = (__bf16)lo; p[1] = (__bf16)hi; return __builtin_bit_cast(unsigned, p); }
DI float bflo(unsigned u) { return __builtin_bit_cast(float, u << 16); }
DI float bfhi(unsigned u) { return __builtin_bit_cast(float, u & 0xffff0000u); }
DI float rbf(float f) { return (float)(__bf16)f; }
typedef __attribute__((address_space(3))) const char* lds_cp;
typedef __attribute__((address_space(3))) const u32x4* lds_u32x4_p;
template <int O0, int O1> DI u32x4 tr_at(unsigned a) {
  s16x4 x = __builtin_amdgcn_ds_read_tr16_b64_v4i16((lds_s16x4_p)((lds_cp)(uintptr_t)a + O0));
  s16x4 y = __builtin_amdgcn_ds_read_tr16_b64_v4i16((lds_s16x4_p)((lds_cp)(uintptr_t)a + O1));
  return __builtin_bit_cast(u32x4, __builtin_shufflevector(x, y, 0, 1, 2, 3, 4, 5, 6, 7));
}
template <int O> DI u32x4 l128_at(unsigned a) { return *(lds_u32x4_p)((lds_cp)(uintptr_t)a + O); }
DI unsigned lds_addr(const void* p) { return (unsigned)(uintptr_t)(__attribute__((address_space(3))) const char*)p; }
template <int N> DI void wait_vm() { asm volatile("s_waitcnt vmcnt(%0)" ::"n"(N) : "memory"); }
DI void bar() { asm volatile("s_waitcnt lgkmcnt(0)" ::: "memory"); __builtin_amdgcn_s_barrier(); asm volatile("" ::: "memory"); }
DI u32x4 lds128(const char* p) { return *(const u32x4*)p; }
DI int lane_id() { int r; asm volatile("v_mbcnt_lo_u32_b32 %0, -1, 0\n\tv_mbcnt_hi_u32_b32 %0, -1, %0" : "=v"(r)); return r; }
DI float vcos(float a) { return __builtin_amdgcn_cosf(a); }
DI float vsin(float a) { return __builtin_amdgcn_sinf(a); }

#define STAMP_BEGIN
#define ST(i)
#define STP(i)
#define STX(i)
#define STC(i)
#define STE(mk, i)
#define STF(mk, n)
#define SQD
#define SQ(n, k)
#define SQW
#define STAMP_END(kid)
#define STAMP_ARG
#define STAMP_PASS

#define GQD
#define GQ(s, k)
#define GQW
constexpr int SL = 16384;
constexpr int XS[7] = {3, 4, 5, 6, 7, 4, 0}, XP[7] = {0, 1, 2, 2, 3, 3, 4};
constexpr int WS[14] = {0, 1, 2, 3, 1, 5, 2, 4, 0, 3, 1, 2, 4, 0}, DS[14] = {6, 7, 8, 6, 7, 5, 6, 7, 5, 6, 4, 1, 2, 0};
constexpr int L_T1 = 8 * SL, L_W = 8 * SL, L_J = L_W + 8192, L_I = 9 * SL, L_ST = L_I + 12288, L_CS = L_ST + 512, L_R2 = L_ST + 1536, L_CT = L_ST + 2560, L_PF = L_ST + 3072;
constexpr int L_TB = 3 * SL, L_C0 = L_CT, L_C1 = L_CT + 4, L_C2 = L_CT + 8, L_C3 = L_CT + 12, L_C4 = L_CT + 16, L_C5 = L_CT + 20, L_C6 = L_CT + 24, L_C7 = L_CT + 28;
constexpr unsigned OFF_R = 7u << 20, OFF_S = 15u << 19, OFF_Z = 8u << 20, OFF_W = 10u << 20, OFF_Y = 12u << 20, OFF_C = 14u << 20, OFF_END = 16u << 20;
constexpr int FW0 = 256, FZ0 = 1024, FH0 = 2048, FY0 = 32768, FP0 = 49152;
constexpr unsigned ONES = 0x3f803f80u, NEG2 = 0x80008000u;
#define SPINC 200000
constexpr int SPIN_CAP = SPINC;
#define HFAT_ 138
#define PRC_ 3
#define PRH_ 2
#define PRN 12
constexpr int HFAT = HFAT_, PRC = PRC_, PRH = PRH_;
#define EXP 0
#define TWAT 196
#define M2 310
#define MY 405
#define TD 1
#define PRE 3
#define SQV 0
#define EPS 2
#define TS 2
#define GS 22
#define RS 22
#define FZ 0
#define SEC 3
#define UM 0
#define CHEB 1
#define G1H 0
#define HS 0
#define BM 5
#define BK 1
#define BL 7
#define PRA 99
#define PRB 99
#define SW 4
#define HW 0
#define HV 0
#define S0 69
#define HF2 0
constexpr int RA = 6;

struct Cos { double v[2048]; };
constexpr Cos mk_cos() {
  Cos c{};
  for (int k = 0; k <= 512; ++k) { const double x = k * (3.14159265358979323846 / 1024); double t = 1, s = 1;
    for (int i = 1; i < 14; ++i) { t *= -x * x / ((2 * i - 1) * (2 * i)); s += t; }
    if (k == 512) s = 0;
    c.v[k] = c.v[(2048 - k) & 2047] = s; c.v[1024 - k] = c.v[1024 + k] = -s; }
  return c;
}
constexpr Cos COS = mk_cos();
constexpr float cs(int k) { return (float)COS.v[k & 2047]; }
constexpr float sn(int k) { return (float)COS.v[(k - 512) & 2047]; }
constexpr unsigned bfr(float f) { const unsigned u = __builtin_bit_cast(unsigned, f); return (u + 0x7fff + ((u >> 16) & 1)) >> 16; }
constexpr unsigned pkc(float lo, float hi) { return bfr(lo) | bfr(hi) << 16; }
struct Tab { unsigned tb[16][64][4]; float tw[16][64][4]; };
constexpr Tab mk_tab() {
  Tab t{};
  for (int g = 0; g < 4; ++g) for (int l = 0; l < 64; ++l) for (int p = 0; p < 4; ++p) { const int a = l & 15, b = (l >> 2) & 12;
    if (g < 3) {
      const int d = 32 * (2 * a + (g >> 1)), x = (b + 16 * (p >> 1) + 2 * (p & 1)) * d + 32 * a * (g & 1), o = 2 * (g & 1);
      t.tb[4 * g][l][p] = pkc(cs(x), cs(x + d)); t.tb[4 * g + 2][l][p] = pkc(sn(x), sn(x + d));
      t.tb[4 * g + 1 + o][l][p] = t.tb[4 * g][l][p] ^ 0x80000000u; t.tb[4 * g + 3 - o][l][p] = t.tb[4 * g + 2][l][p] ^ (0x80000000u >> (8 * o));
    } else for (int u = 0; u < 2; ++u) {
      const int x = 64 * (b + p + 16 * u) * a;
      t.tb[12 + 2 * u][l][p] = pkc(cs(x), -sn(x)); t.tb[13 + 2 * u][l][p] = pkc(sn(x), cs(x));
    }
    const int n = 2 * a + 32 * (p & 1) + (p >> 1), x = n * (b / 2 + 8 * g);
    const float m = 2.0f / (448 * 2048);
    float* w = t.tw[4 * g + p][l]; w[0] = cs(x) * m; w[1] = sn(x) * m; w[2] = cs(x + n) * m; w[3] = sn(x + n) * m;
  }
  return t;
}
static __device__ __attribute__((aligned(256))) const Tab TAB = mk_tab();

enum : int { eX = 0, eW = 8, eD = 24, eDR = 40, eH = 56, eXR = 84, eTM = 92, eSM = 180, eSF = 190, eEP = 100, ePF, eTAG, eDV, eG0, eTB, eTD, eZ, eZF, eCL0, eCL1, eCW0, eCW1,
             eFLZ, ePZ, eLZ, eFL1, eP1, eR2, eRW, eFL2, eP2, eRES, eAW, eAR, eTH, ePO, eFP, ePP, mP = 210, mK = 230, mEnd = 500 };
struct Ev { int id, n; };
#define M_(m) {m, 0},
#define X_(j) {eX + j, 4},
#define W_(j, p) {eW + j + 1024 * (3 - p), 1},
#define D_(j, p) {eD + j + 1024 * (3 - p), 1},
#define W4(j) W_(j, 0) W_(j, 1) W_(j, 2) W_(j, 3)
#define D4(j) D_(j, 0) D_(j, 1) D_(j, 2) D_(j, 3)
#define E_(e, p) {e + 1024 * p, 0},
#define V_(e, p, n) {e + 1024 * p, n},
#define H_(s) {eH + s, 2},
#define T_(j, p) E_(eTM + j, p)
#define T4(j) T_(j, 0) T_(j, 1) T_(j, 2) T_(j, 3)
#define WE(j, e) W_(j, 0) E_(e, 0) W_(j, 1) E_(e, 1) W_(j, 2) E_(e, 2) W_(j, 3) E_(e, 3)
#define DE(j, e) D_(j, 0) E_(e, 0) D_(j, 1) E_(e, 1) D_(j, 2) E_(e, 2) D_(j, 3) E_(e, 3)
constexpr Ev SEQ_R[] = {{ePF, 1}, {eW + 0, 4}, {eW + 1, 4},
  M_(mP + 0) E_(eXR + 0, 0) X_(2) V_(eEP, 0, 1) WE(2, eTM + 0) X_(3)
  M_(mP + 1) E_(eXR + 1, 0) X_(4) WE(3, eTM + 1) V_(eTD, 0, TD)
  E_(eG0, 0) E_(eG0, 1)
  M_(mP + 2) E_(eXR + 2, 0) E_(eXR + 3, 0) X_(5) T4(2) X_(6) T4(3) E_(eDV, 0) E_(eTAG, 0) E_(eG0, 2) E_(eG0, 3)
  M_(mP + 3) E_(eXR + 4, 0) E_(eXR + 5, 0) WE(4, eTM + 4) WE(5, eTM + 5) D4(0)
  M_(mP + 4) E_(eXR + 6, 0) WE(6, eTM + 6) W4(7) D4(1)
  M_(mP + 5) V_(eCL0, 0, 1) E_(eZ, 1) E_(eDR + 0, 0) W_(8, 0) W_(8, 1) E_(eZ, 2) W_(8, 2) W_(8, 3) E_(eZ, 3) D_(2, 0) D_(2, 1) E_(eZ, 4) D_(2, 2) D_(2, 3) E_(eCW0, 0)
  M_(mP + 6) V_(eZ, 0, 8) E_(eDR + 1, 0) W4(9) D4(3)
  M_(mP + 7) E_(eDR + 2, 0) W4(10) D4(4) V_(eZF, 0, 1)
  M_(mP + 8) E_(eDR + 3, 0) W4(11) D4(5)
  M_(mP + 9) E_(eDR + 4, 0) W4(12) D4(6)

  M_(mP + 10) V_(eFLZ, 0, 1) V_(eCL1, 0, 1) W4(13) E_(eDR + 5, 0) D4(7)

  M_(mP + 11) V_(ePO, 0, 1) V_(eTB, 0, 4) E_(eCW1, 0) E_(eAR, 1) E_(ePZ, 0)
  V_(eFL1, 0, 1) V_(eLZ, 0, 4) E_(eDR + 6, 0) D4(8) D4(9)
  E_(eAR, 0) E_(eP1, 0) V_(eR2, 0, 1) H_(0) H_(1) H_(2) H_(3) H_(4) H_(5) H_(6)
  M_(mK + 0) V_(eFP, 0, 1) H_(7) E_(eRW, 0) M_(mK + 1) H_(8) M_(mK + 2) H_(9) M_(mK + 3) H_(10) M_(mK + 4) H_(11) E_(ePP, 0) M_(mK + 5) H_(12) M_(mK + 6) H_(13)
  M_(mK + 7) E_(eDR + 7, 0) E_(eAW, 0) D4(10) M_(mK + 8) V_(eFL2, 0, 1) D4(11) M_(mK + 9) E_(eDR + 8, 0) D4(12) M_(mK + 10) D4(13)
  M_(mK + 11) E_(eDR + 9, 0) V_(eRES, 2, (RS == 11) * 2) M_(mK + 12) E_(eP2, 0) H_(14) H_(15) H_(16) H_(17) H_(18) H_(19) E_(eAR, 2) M_(mK + 13) H_(20)
  M_(mK + 14) H_(21) H_(22) M_(mK + 15) E_(eDR + 10, 0) H_(23) H_(24) M_(mK + 16) H_(25) H_(26) M_(mK + 17) E_(eDR + 11, 0) H_(27) M_(mK + 18) V_(eRES, 1, (RS == 18) * 2) M_(mK + 19) E_(eDR + 12, 0)
  M_(mK + 20) M_(mK + 21) E_(eDR + 13, 0) M_(mK + 22) V_(eRES, 0, (RS == 22) * 2) M_(mK + 23) M_(mK + 24) M_(mK + 25) M_(mK + 26) M_(mK + 27) M_(mEnd)};
constexpr int sidx(int id) { int i = 0; while (SEQ_R[i].id != id && SEQ_R[i].id != mEnd) ++i; return i; }
constexpr int pos(int id) { int p = SEQ_R[sidx(id)].id == id ? 0 : 1000; for (int i = 0, n = sidx(id); i <= n; ++i) p += SEQ_R[i].n; return p; }
constexpr int before(int id) { return pos(id) - SEQ_R[sidx(id)].n; }
constexpr int seglen(int i0) { int n = 0; while (SEQ_R[i0 + n].id % 1024 < mP) ++n; return n; }
constexpr int need(int n) {
  int p = pos(eW + n);
  for (int j = 2; j < 7; ++j) if (XP[j] == n && pos(eX + j) > p) p = pos(eX + j);
  return n == 11 ? pos(eW + 12) : p;
}

DI unsigned pka(float lo, float hi) { unsigned r; asm volatile("v_cvt_pk_bf16_f32 %0, %1, %2" : "=v"(r) : "v"(lo), "v"(hi)); return r; }
typedef u32x4 srd_t;
typedef __attribute__((ext_vector_type(10))) unsigned u32x10;
DI f32x4 mfma4(u32x2 a, f32x4 c) { return __builtin_amdgcn_mfma_f32_4x4x4bf16_1k(__builtin_bit_cast(s16x4, a), __builtin_bit_cast(s16x4, a), c, 0, 0, 0); }
typedef __attribute__((ext_vector_type(16))) unsigned u32x16;
template <int I> DI srd_t srd_of(u32x16 a) { return __builtin_shufflevector(a, a, 4 * I, 4 * I + 1, 4 * I + 2, 4 * I + 3); }
template <int L> DI void dma2s(unsigned lw, unsigned v0, unsigned v1, srd_t r, unsigned so) {
  asm volatile("s_add_u32 m0, %0, %1\n\ts_nop 0\n\tbuffer_load_dwordx4 %2, %4, %5 offen sc1 lds\n\tbuffer_load_dwordx4 %3, %4, %5 offen offset:1024 sc1 lds"
               ::"s"(lw), "n"(L), "v"(v0), "v"(v1), "s"(r), "s"(so) : "memory", "scc");
}
template <int L> DI void dma4(unsigned lw, unsigned v0, unsigned v1, unsigned v2, unsigned v3, srd_t r0, srd_t r1, unsigned so) {
  asm volatile("s_add_u32 m0, %0, %1\n\ts_nop 0\n\tbuffer_load_dwordx4 %2, %6, %8 offen lds\n\tbuffer_load_dwordx4 %3, %7, %8 offen offset:1024 lds\n\t"
               "buffer_load_dwordx4 %4, %6, %8 offen offset:2048 lds\n\tbuffer_load_dwordx4 %5, %7, %8 offen offset:3072 lds"
               ::"s"(lw), "n"(L), "v"(v0), "v"(v1), "v"(v2), "v"(v3), "s"(r0), "s"(r1), "s"(so) : "memory", "scc");
}
template <int L, int P> DI void dma1(unsigned lw, unsigned v, srd_t r, unsigned so) {
  if constexpr (P == 3) asm volatile("s_add_u32 m0, %0, %1\n\ts_nop 0" ::"s"(lw), "n"(L) : "memory", "scc");
  asm volatile("buffer_load_dwordx4 %0, %1, %2 offen offset:%3 lds" ::"v"(v), "s"(r), "s"(so), "n"(P * 1024) : "memory");
}
template <int OFF> DI void ld_sc1(u32x4& d, unsigned voff, srd_t r, unsigned so) { asm volatile("buffer_load_dwordx4 %0, %1, %2, %3 offen offset:%4 sc1" : "=v"(d) : "v"(voff), "s"(r), "s"(so), "n"(OFF) : "memory"); }
template <int OFF> DI void ld_sc1_over(u32x4& d, unsigned voff, srd_t r, unsigned so) { asm volatile("buffer_load_dwordx4 %0, %1, %2, %3 offen offset:%4 sc1" : "+v"(d) : "v"(voff), "s"(r), "s"(so), "n"(OFF) : "memory"); }
template <int OFF> DI void ld_pl(u32x4& d, unsigned voff, srd_t r, unsigned so) { asm volatile("buffer_load_dwordx4 %0, %1, %2, %3 offen offset:%4" : "=v"(d) : "v"(voff), "s"(r), "s"(so), "n"(OFF) : "memory"); }
template <int OFF> DI void ld8_sc1(u32x2& d, unsigned voff, srd_t r, unsigned so) { asm volatile("buffer_load_dwordx2 %0, %1, %2, %3 offen offset:%4 sc1" : "=v"(d) : "v"(voff), "s"(r), "s"(so), "n"(OFF) : "memory"); }
DI void st4_sc1(unsigned v, unsigned voff, srd_t r, unsigned so) { asm volatile("buffer_store_dword %0, %1, %2, %3 offen sc1" ::"v"(v), "v"(voff), "s"(r), "s"(so) : "memory"); }
DI void ld_u32_sc1(unsigned& d, unsigned voff, srd_t r, unsigned so) { asm volatile("buffer_load_dword %0, %1, %2, %3 offen sc1" : "=v"(d) : "v"(voff), "s"(r), "s"(so) : "memory"); }
DI void ld_flag(u64& d, unsigned voff, srd_t r) { asm volatile("buffer_load_dwordx2 %0, %1, %2, %3 offen sc1" : "=v"(d) : "v"(voff), "s"(r), "s"(OFF_C) : "memory"); }
template <int OFF = 0> DI void st16_sc1(u32x4 v, unsigned voff, srd_t r, unsigned so) {
  asm volatile("buffer_store_dwordx4 %0, %1, %2, %3 offen offset:%4 sc1\n\ts_nop 1" ::"v"(v), "v"(voff), "s"(r), "s"(so), "n"(OFF) : "memory");
}
DI void st_flag1(srd_t r, unsigned so, u64 tag) {
  asm volatile("s_mov_b64 exec, 1\n\tbuffer_store_dwordx2 %0, off, %1, %2 sc1\n\ts_mov_b64 exec, -1" ::"v"(tag), "s"(r), "s"(so) : "memory");
}
template <int M> DI void st_flags(unsigned voff, srd_t r, unsigned so, u64 tag) {
  asm volatile("s_mov_b64 exec, %4\n\tbuffer_store_dwordx2 %0, %1, %2, %3 offen sc1\n\ts_mov_b64 exec, -1" ::"v"(tag), "v"(voff), "s"(r), "s"(so), "n"(M) : "memory");
}
template <class T> DI void pin(T& a) { asm volatile("" : "+v"(a)); }
template <class T> DI void pins(T& a) { asm volatile("" : "+s"(a)); }
template <class T> DI void fresh(T& a) { asm volatile("" : "=v"(a)); }
template <bool K> DI unsigned vmov(unsigned a) { unsigned r; if constexpr (K) asm volatile("v_mov_b32 %0, %1" : "=v"(r) : "v"(a)); else asm volatile("v_mov_b32 %0, 0" : "=v"(r)); return r; }
DI unsigned nb(unsigned a) { return __builtin_amdgcn_mov_dpp(a, 0xb1, 0xf, 0xf, true); }
DI float nb(float a) { return __uint_as_float(nb(__float_as_uint(a))); }
DI u32x4 rot90(u32x4 a, unsigned sg) { u32x4 r = {nb(a[0]) ^ sg, nb(a[1]) ^ sg, nb(a[2]) ^ sg, nb(a[3]) ^ sg}; pin(r); return r; }
DI unsigned selm(u64 m, unsigned a, unsigned b) { unsigned r; asm("v_cndmask_b32_e64 %0, %2, %1, %3" : "=v"(r) : "v"(a), "v"(b), "s"(m)); return r; }
template <int N> DI void vbar() {
  static_assert(N >= 0, "wait");
  asm volatile("s_waitcnt vmcnt(%0) lgkmcnt(0)\n\ts_barrier" ::"n"(N > 63 ? 63 : N) : "memory");
}
template <int N> DI void wvm() { static_assert(N >= 0, "wait"); wait_vm<(N > 63 ? 63 : N)>(); }
#define SLP 8
DI int poll(u64& fv, u64 mask, unsigned voff, srd_t sb, u64 tag) {
  int it = 0;
  if ((~__builtin_amdgcn_ballot_w64(fv == tag) & mask) != 0) {
#pragma unroll 1
    for (it = 1; it < SPIN_CAP; ++it) {
      __builtin_amdgcn_s_sleep(SLP);
      ld_flag(fv, voff, sb); wait_vm<0>(); pin(fv);
      if ((~__builtin_amdgcn_ballot_w64(fv == tag) & mask) == 0) break;
    }
  }
  asm volatile("" ::: "memory");
  return it;
}
#define SN(i, n) (void)(n);
typedef __attribute__((address_space(3))) char* lds_p;
template <class T, int O> DI T lds_ld(unsigned a) { return *(__attribute__((address_space(3))) const T*)((lds_cp)(uintptr_t)a + O); }
template <class T, int O> DI void lds_st(unsigned a, T v) { *(__attribute__((address_space(3))) T*)((lds_p)(uintptr_t)a + O) = v; }
DI void arrive(unsigned ct) { asm volatile("s_mov_b64 exec, 1\n\tds_add_u32 %0, %1\n\ts_mov_b64 exec, -1" ::"v"(ct), "v"(1u) : "memory"); }
template <int N = 4> DI void await4(unsigned ct) {
#pragma unroll 1
  for (int it = 0; it < SPIN_CAP * 8; ++it) {
    unsigned c;
    asm volatile("ds_read_b32 %0, %1\n\ts_waitcnt lgkmcnt(0)" : "=v"(c) : "v"(ct) : "memory");
    if (__builtin_amdgcn_readfirstlane(c) >= N) break;
    __builtin_amdgcn_s_sleep(1);
  }
}
template <int D> DI void pace(unsigned ct) {
  unsigned c; u64 n;
  asm volatile("ds_read_b32 %0, %2\n\ts_memrealtime %1\n\ts_waitcnt lgkmcnt(0)" : "=v"(c), "=s"(n) : "v"(ct) : "memory");
  int r = min((int)(__builtin_amdgcn_readfirstlane(c) + D - (unsigned)n), 200) * 2;
#pragma unroll 1
  for (; r > 0; r -= 7) __builtin_amdgcn_s_sleep(1);
}
DI void check4(unsigned c, unsigned ct) {
  unsigned s, n;
  asm volatile("s_mov_b32 %2, %4\n1:\n\tv_readfirstlane_b32 %1, %0\n\ts_cmp_ge_u32 %1, 4\n\ts_cbranch_scc1 2f\n\tds_read_b32 %0, %3\n\ts_sub_u32 %2, %2, 1\n\ts_waitcnt lgkmcnt(0)\n\ts_cbranch_scc0 1b\n2:"
               : "+v"(c), "=&s"(s), "=&s"(n) : "v"(ct), "n"(SPIN_CAP * 8) : "memory", "scc");
}
template <int N> DI void trig(float a, float d, float k, float* c, float* s) {
  c[0] = vcos(a); s[0] = vsin(a); c[1] = vcos(a + d); s[1] = vsin(a + d);
#pragma unroll
  for (int j = 2; j < N; ++j) { c[j] = __builtin_fmaf(k, c[j - 1], -c[j - 2]); s[j] = __builtin_fmaf(k, s[j - 1], -s[j - 2]); }
}
DI void part_at(unsigned* p, unsigned a, unsigned lvo) { sfor<8>([&](auto K) { p[K] = a + (lvo ^ (32 * K)); pin(p[K]); }); }
template <int O, int S, int K0 = 0, int N = 8> DI void put_part(const unsigned* a, const f32x16& c0, const f32x16& c1) {
  sfor<N>([&](auto K) { constexpr int k = K0 + K; const f32x16& c = k < 4 ? c0 : c1;
    lds_st<f32x4, O + (k >> 2) * S + (k & 3) * 1024>(a[k], f32x4{c[4 * (k & 3)], c[4 * (k & 3) + 1], c[4 * (k & 3) + 2], c[4 * (k & 3) + 3]});
  });
}
#define BUF(n) const void* n##p, unsigned n##b, unsigned n##f
typedef __attribute__((ext_vector_type(8))) unsigned u32x8;
template <int I> DI srd_t srd_of(u32x8 a) { return __builtin_shufflevector(a, a, 4 * I, 4 * I + 1, 4 * I + 2, 4 * I + 3); }
template <int P> DI void dmap(unsigned v, srd_t r, unsigned so) { asm volatile("buffer_load_dwordx4 %0, %1, %2 offen offset:%3 lds" ::"v"(v), "s"(r), "s"(so), "n"(P * 1024) : "memory"); }
constexpr unsigned CH = 64 * F_ * 2;
__global__ __launch_bounds__(512, 2) void fwd(BUF(x), BUF(y), BUF(g), BUF(u), BUF(w), BUF(d), BUF(a), BUF(c), BUF(o), BUF(z) STAMP_ARG) {
  extern __shared__ __attribute__((aligned(16))) char smem[];
  u32x16 ka;
  u32x8 kf;
  unsigned b = blockIdx.x, tid = threadIdx.x, fv0, fs0;
  int w, ws, idx, rb, unit, col;
  auto kp = __builtin_amdgcn_kernarg_segment_ptr();
  const unsigned lds0 = lds_addr(smem);
  static_assert(XS[0] == 3 && XS[1] == 4 && WS[0] == 0 && WS[1] == 1 && SL == 16384, "first loads");
  STAMP_BEGIN
  {
    u64 pc; unsigned t0, t1, t2, t3, t4, t5, t6, t7;
    asm volatile("v_readfirstlane_b32 %[w], %[tid]\n\ts_lshr_b32 %[idx], %[b], 3\n\ts_lshr_b32 %[w], %[w], 6\n\ts_and_b32 %[t1], %[w], 4\n\ts_lshl_b32 %[t1], %[t1], 3\n\t"
                 "s_load_dwordx8 %[kf], %[kp], %[t1]\n\ts_load_dwordx16 %[ka], %[kp], 64\n\t"
                 "s_getpc_b64 %[pc]\n.Lpf_here:\n\ts_mov_b32 m0, %[lpf]\n\ts_lshl_b32 %[t0], %[idx], 11\n\t"
                 "v_lshl_add_u32 v200, %[tid], 2, %[t0]\n\tv_min_u32 v200, .Lpf_end-.Lpf_here, v200\n\tglobal_load_lds_dword v200, %[pc]\n\t"
                 "s_and_b32 %[ws], %[w], 3\n\ts_and_b32 %[rb], %[idx], 7\n\ts_and_b32 %[t2], %[b], 1\n\ts_lshl3_add_u32 %[rb], %[t2], %[rb]\n\ts_lshl2_add_u32 %[t6], %[rb], %[ws]\n\t"
                 "s_lshl_b32 %[t7], %[ws], 6\n\ts_xor_b32 %[so], %[b], %[idx]\n\ts_and_b32 %[so], %[so], 8\n\t"
                 "s_lshr_b32 %[t3], %[b], 6\n\ts_cmpk_ge_u32 %[b], 192\n\ts_cselect_b32 %[t2], 2, 1\n\ts_cselect_b32 %[col], 2, 0\n\ts_cselect_b32 %[t3], 12, %[t3]\n\ts_cselect_b32 %[t4], 1, 3\n\t"
                 "s_and_b32 %[un], %[b], 7\n\ts_lshr_b32 %[un], %[un], %[t2]\n\ts_mul_i32 %[un], %[un], %[t4]\n\ts_add_i32 %[un], %[t3], %[un]\n\ts_lshl_b32 %[t0], %[un], 3\n\t"
                 "s_lshr_b32 %[t2], %[b], 2\n\ts_xor_b32 %[t2], %[t2], %[b]\n\ts_and_b32 %[col], %[col], %[t2]\n\t"
                 "s_cmp_lg_u32 %[t1], 0\n\ts_cselect_b32 %[t3], 2, 4\n\ts_cselect_b32 %[t4], 4, 3\n\ts_cselect_b32 %[t5], 2, 6\n\ts_cselect_b32 %[t2], -4, 12\n\t"
                 "s_cselect_b32 %[t6], %[t7], %[t6]\n\ts_cselect_b32 %[t0], %[t0], 0\n\ts_cselect_b32 %[t7], %[ch], 64\n\t"
                 "s_mul_i32 %[so], %[so], %[t7]\n\ts_add_i32 %[t2], %[t2], %[w]\n\ts_lshl_b32 m0, %[t2], 12\n\t"
                 "v_bfe_u32 v201, %[tid], 3, 3\n\tv_lshrrev_b32 v202, %[t3], %[tid]\n\tv_lshlrev_b32 v201, %[t5], v201\n\tv_bitop3_b32 v202, v202, %[tid], %[t4] bitop3:0x6c\n\t"
                 "v_or_b32 v201, %[t6], v201\n\tv_and_b32 v202, 7, v202\n\tv_mad_u32_u24 v201, v201, 56, %[t0]\n\tv_add_lshl_u32 v201, v201, v202, 4\n\ts_waitcnt lgkmcnt(0)"
                 : [kf] "=&s"(kf), [ka] "=&s"(ka), [so] "=&s"(fs0), [pc] "=&s"(pc), [w] "=&s"(w), [ws] "=&s"(ws), [idx] "=&s"(idx), [rb] "=&s"(rb), [un] "=&s"(unit), [col] "=&s"(col),
                   [t0] "=&s"(t0), [t1] "=&s"(t1), [t2] "=&s"(t2), [t3] "=&s"(t3), [t4] "=&s"(t4), [t5] "=&s"(t5), [t6] "=&s"(t6), [t7] "=&s"(t7)
                 : [kp] "s"(kp), [lpf] "n"(L_PF), [ch] "n"(CH / 2), [b] "s"(b), [tid] "v"(tid) : "memory", "scc", "v200", "v201", "v202");
    asm volatile("" : "={v201}"(fv0));
  }
  SB();
  const srd_t rw = srd_of<0>(ka), rd = srd_of<1>(ka), ra = srd_of<2>(ka), rc = srd_of<3>(ka);
  const f32x4 zero = {0.f, 0.f, 0.f, 0.f};
  const f32x16 zero16 = {};
  u64 ep, fv, tag;
  auto mk_tag = [&]() __attribute__((always_inline)) { pin(ep);
    tag = (((u64)__builtin_amdgcn_readfirstlane((unsigned)(ep >> 32)) << 32) | (unsigned)__builtin_amdgcn_readfirstlane((unsigned)ep)) + 1; };

  if (!col) {
  const srd_t rg = srd_of<0>(kf);
  STX(50)
  dmap<0>(fv0, rg, fs0);
  SB();
  pins(w); pin(fv0);
  const int wn = w >> 2;
  srd_t ru = srd_of<1>(kf);
  const unsigned fk = wn ? 8 * F_ * 2 - 2048 : 1024 * D_ * 2 - 2048;
  unsigned fv2 = fv0 + fk;
  dmap<2>(fv2, rg, fs0);
  SB();
  { const u64 p = ((u64)ru[1] << 32 | ru[0]) - 1024; ru[0] = (unsigned)p; ru[1] = (unsigned)(p >> 32); }
  unsigned fv1 = fv0 ^ (wn ? 0 : 64), fs1 = fs0 + (wn ? 0 : 512 * D_ * 2), fv3 = fv1 + fk;
  dmap<1>(fv1, ru, fs1); dmap<3>(fv3, ru, fs1);
  u32x8 kx;
  asm volatile("s_load_dwordx8 %0, %2, 128" : "=&s"(kx), "+s"(b) : "s"(kp) : "memory");
  pin(tid);
  STX(51)
  SB();
  pins(idx); pins(rb); pins(unit); pins(ws);
  const int xcd = b & 7, g = xcd >> 1, i3 = (b + 64) >> 8, r0 = ((b ^ idx) >> 1) & 4;
  const unsigned lw = lds0 + ws * 4096;
  auto kof = [&](int st) { int k = st + r0; return k >= 7 ? k - 7 : k; };
  const float cf_ = (float)(16 * unit + 1 + ((tid >> 1) & 15)) * (1.0f / 448), k2 = 2.f * vcos(cf_);
  const float a0 = (float)(((tid >> 2) & ~7u) + 3) * cf_ + (float)(tid & 1) * 0.25f;
  const unsigned sg = selm(0x3333333333333333ull, NEG2, 0u), ta_ = lds0 + L_T1 + tid * 16;
  auto t1gen = [&](auto S) {
    const float x = S ? __builtin_fmaf(cf_, 128.f, a0) : a0, c3 = vcos(x), c4 = vcos(x + cf_);
    const float c2 = __builtin_fmaf(k2, c3, -c4), c5 = __builtin_fmaf(k2, c4, -c3), c1 = __builtin_fmaf(k2, c2, -c3), c6 = __builtin_fmaf(k2, c5, -c4);
    const u32x4 o = {pk2(__builtin_fmaf(k2, c1, -c2), c1), pk2(c2, c3), pk2(c4, c5), pk2(c6, __builtin_fmaf(k2, c6, -c5))};
    lds_st<u32x4, S * 8192>(ta_, o); lds_st<u32x4, S * 8192 + 14336>(ta_, o ^ sg);
  };
  auto second = [&]() { const unsigned st = wn ? CH : 128;
    asm volatile("s_add_u32 m0, m0, %0" ::"n"(SL) : "memory", "scc");
    const unsigned s0 = fs0 + st, s1 = fs1 + st;
    asm volatile("s_nop 0" ::"s"(s0), "s"(s1));
    dmap<0>(fv0, rg, s0); dmap<1>(fv1, ru, s1); dmap<2>(fv2, rg, s0); dmap<3>(fv3, ru, s1); };
  if constexpr (SEC == 0) { second(); SB(); }
  t1gen(ic<0>{});
  SB();
  if constexpr (SEC == 1) second();
  STX(52)
  SB();
  lds_st<u32x4, L_CT>(lds0 + (tid & 16), u32x4{0u, 0u, 0u, 0u});
  if (w < 6) t1gen(ic<1>{});
  SB();
  if constexpr (SEC == 2) second();
  STX(53)
  SB();
  srd_t ro, rx;
#define XM 1
  const int rt = 4 * (XM == 0 ? xcd : XM == 1 ? g + 4 * (~xcd & 1) : g + 4 * (xcd & 1)) + ((idx >> i3) & 3), dt = min(idx >> 2, 6);
  const int fi = 16 * unit + rb;
  unsigned lvo, wo, xo[4], gvo, hso, ao, sgf;
  u32x4 z[2], zr[2];
  f32x4 bo[4];
  lvo = (tid & 63) * 16; pin(lvo);
  xo[0] = lw + (tid & 31) * 128 + ((((tid >> 5) & 1) ^ ((tid >> 1) & 7)) * 16); pin(xo[0]);
#pragma unroll
  for (int i = 1; i < 4; ++i) { xo[i] = xo[0] ^ (32 * i); pin(xo[i]); }
  auto xread = [&](auto J, u32x4* d) { constexpr int j = J;
    sfor<4>([&](auto I) { if constexpr (j == 1 || j == 6) { xo[I] += j == 1 ? 4 * SL : -4 * SL; pin(xo[I]); } d[I] = l128_at<(XS[j] & 3) * SL>(xo[I]); }); };

  if (wn == 0) {
    ld_flag(ep, 0u, rw);
    auto yst = [&](auto Y, u32x4 o) { st16_sc1(o, (lvo >> 8) * (2 * 57344) + (lvo & 255), rw, OFF_Y + (8 * ws + Y) * 57344 + fi * 256); };
    { const int l = lane_id();
      wo = lds0 + (l >> 5) * 2048 + ((l & 15) >> 2) * 128 + ((l >> 3) & 1) * 64 + ((l >> 4) & 1) * 32 + (l & 3) * 8; pin(wo);
    }
    unsigned on = selm(0xf0f00f0ff0f00f0full, ONES, 0u); pin(on);
    u32x4 xf[28], wf[RA], eo;
    f32x4 sm[2] = {zero, zero}, sq[2] = {zero, zero};
    f32x16 acc[2][4];
    hso = ((4 * rb + ws) * 28 + unit) * 4096;
    float u0, u1, rr;
    unsigned nm;
    u32x2 q2;
    auto stats = [&](auto Tc) { constexpr int T = Tc, n = T / 16, k = T % 16 - 2, S = T - S0;
      constexpr int j0 = n < 2 ? n : 2 * n - 2, j = n < 5 && k == BK ? j0 : (n == 2 || n == 3) && k == BL ? j0 + 1 : -1;
      if constexpr (j >= 0) {
        sfor<4>([&](auto I) { sm[I & 1] = mfma16(u32x4{on, on, on, on}, xf[4 * j + I], sm[I & 1]); pin(sm[I & 1]); });
        sfor<8>([&](auto I) { const u32x4 x = xf[4 * j + (I >> 1)]; sq[I & 1] = mfma4((I & 1) ? x.hi : x.lo, sq[I & 1]); pin(sq[I & 1]); });
      }
      if constexpr (S == 0) { sfor<4>([&](auto I) { float p = sq[0][+I] + sq[1][+I]; pin(p); sq[0][+I] = p; }); pin(sq[0]); }
      if constexpr (S == 1) { u0 = __uint_as_float(selm(0xaaaaaaaaaaaaaaaaull, __float_as_uint(sq[0][1]), __float_as_uint(sq[0][0])));
        u1 = __uint_as_float(selm(0xaaaaaaaaaaaaaaaaull, __float_as_uint(sq[0][3]), __float_as_uint(sq[0][2]))); rr = sm[0][0] + sm[1][0]; pin(u0); pin(u1); pin(rr); }
      if constexpr (S == 2) { const unsigned qu = selm(0xccccccccccccccccull, __float_as_uint(u1), __float_as_uint(u0)); q2 = __builtin_amdgcn_permlane32_swap(qu, qu, false, false);
        u0 = rr * (-1.0f / D_); pin(q2); pin(u0); }
      if constexpr (S == 3) { nm = pk2(u0, u0); u1 = __builtin_fmaf(rr, u0, __uint_as_float(q2[0]) + __uint_as_float(q2[1])); pin(nm); pin(u1); }
      if constexpr (S == 4) { u1 = rbf(fmaxf(u1, 0.f) * (1.0f / D_)); pin(u1); }
      if constexpr (S == 5) { u1 = rbf(u1 + 1e-5f); gvo = lds0 + L_ST + ws * 128 + ((lvo >> 2) & 127); pin(u1); pin(gvo); }
      if constexpr (S == 6) { u1 = __builtin_amdgcn_sqrtf(u1); pin(u1); }
      if constexpr (S == 7) { u1 = __builtin_amdgcn_rcpf(rbf(u1)); pin(u1); }
      if constexpr (S == 8) { lds_st<float, 0>(gvo, u1); rr = u1 * u1; pin(rr); }
    };
    auto epi = [&](auto Pc, auto Kc) { constexpr int P = Pc, k = Kc, Q = k >> 4, e = (k >> 1) & 7;
      if constexpr (!(k & 1)) asm volatile("v_max_i32 %0, 0, %2\n\tv_max_i32 %1, 0, %3" : "=&v"(u0), "=v"(u1) : "v"(acc[P][2 * Q][2 * e]), "v"(acc[P][2 * Q][2 * e + 1]));
      else { unsigned o;
        asm volatile("v_mul_f32 %1, %1, %3\n\tv_mul_f32 %2, %2, %4\n\tv_cvt_pk_bf16_f32 %0, %1, %2" : "=v"(o), "+v"(u0), "+v"(u1) : "v"(acc[P][2 * Q + 1][2 * e]), "v"(acc[P][2 * Q + 1][2 * e + 1]));
        eo[e & 3] = o;
        if constexpr ((e & 3) == 3) st16_sc1<Q * 2048 + (e >> 2) * 1024>(eo, lvo, rw, hso);
      }
    };
    auto hflag = [&](int p) { st_flags<0x7f>(lvo << 11, rw, OFF_C + (FH0 + p * 2048 + (4 * rb + ws) * 16 + unit) * 8, tag); };
    unsigned a0, a1, cs;
    f32x4 tw[4];
    u32x10 nv;
    SQD
    sfor<232 + RA>([&](auto Tc) { constexpr int T = Tc, M = T - RA, tu = T % 116, mu = M % 116;
      if constexpr (T < 232 && tu % 16 == 0 && tu < 100) { constexpr int n = tu / 16 + 7 * (T / 116);
        SQ(n, 0)
        if constexpr (n == 0) { if constexpr (SEC == 3) second(); vbar<5>(); pins(kx); }
        else if constexpr (n == 1) asm volatile("s_waitcnt vmcnt(0)\n\ts_barrier" ::: "memory");
        else if constexpr (n == 5) asm volatile("s_waitcnt lgkmcnt(%0)\n\ts_barrier" ::"n"(2 * (71 - S0)) : "memory");
        else if constexpr (n == 13) check4(cs, lds0 + L_C2);
        else if constexpr (n < 12) asm volatile("s_barrier" ::: "memory");
        SQ(n, 1)
        if constexpr (n == 0) { ST(1) }
        if constexpr (n == 5) { ST(2) }
        if constexpr (n == 7) { ST(3) }
        if constexpr (n == PRN || n == PRA) __builtin_amdgcn_s_setprio(PRC);
        if constexpr (n == PRB) __builtin_amdgcn_s_setprio(0);
      }
      if constexpr (T < 232 && ((tu % 16 == 0 && tu < 96) || tu == 100)) { pin(wo); a0 = wo + WS[tu / 16 + 7 * (T / 116)] * SL; a1 = a0 ^ 64; pin(a0); pin(a1); }
      if constexpr (M >= 0) { constexpr int P = M / 116, n = mu / 16 > 6 ? 6 : mu / 16, i = mu < 96 ? mu % 16 : mu - 100, q = i < 0 ? i + 4 : 2 * (i >> 3) + (i & 1);
        if constexpr (i < 0) { constexpr int o = q == 0 ? 2 : q == 1 ? 6 : q == 2 ? 0 : 4; acc[P][q] = mfma32(eo, __builtin_shufflevector(nv, nv, o, o + 1, o + 2, o + 3), acc[P][q]); }
        else acc[P][q] = mfma32(wf[M % RA], xf[4 * n + ((i >> 1) & 3)], (n == 0 && (i & 6) == 0) ? zero16 : acc[P][q]);
        pin(acc[P][q]);
      }
      SB();
      sfor<7>([&](auto J) { if constexpr (T == 16 * XP[J] + (J > 0 && XP[J] == XP[J > 0 ? J - 1 : 0] ? 4 : 0)) xread(J, xf + 4 * J); });
      if constexpr (T < 232) { constexpr int i = tu < 96 ? tu % 16 : tu - 100, o = ((i >> 1) & 3) * 4096 + (i & 1) * 1024;
        if constexpr (i == -4) { pin(lvo); eo = l128_at<0>(lvo + (lds0 + L_CS)); }
        if constexpr (i >= 0) wf[T % RA] = tr_at<o, o + 512>(i >= 8 ? a1 : a0);
      }
      SB();
      stats(Tc);
      if constexpr (T == 26) mk_tag();
      if constexpr (T == 205 || T == 232) { asm volatile("" ::: "memory"); cs = lds_ld<unsigned, T == 205 ? L_C2 : L_C3>(lds0); asm volatile("" ::: "memory"); }
      if constexpr (EPS == 1 && M >= 109 && M < 141) epi(ic<0>{}, ic<M - 109>{});
      if constexpr (EPS == 2 && M >= 109 && M < 125) { epi(ic<0>{}, ic<2 * M - 218>{}); epi(ic<0>{}, ic<2 * M - 217>{}); }
      if constexpr (M == 117) st4_sc1(__float_as_uint(rr), (lvo >> 2) & 127, rw, OFF_R + (hso >> 5));
      if constexpr (M >= 225) { epi(ic<1>{}, ic<2 * M - 450>{}); epi(ic<1>{}, ic<2 * M - 449>{}); }
      if constexpr (M == HFAT) { wait_vm<0>(); hflag(0); hso += 14 * 4096; STX(29) }
      if constexpr (M >= TWAT && M < TWAT + 8 && !(M & 1)) asm volatile("global_load_dwordx4 %0, %1, %2 offset:%3" : "=v"(tw[(M - TWAT) / 2]) : "v"(lvo), "s"(TAB.tw[4 * ws]), "n"((M - TWAT) * 512) : "memory");
      if constexpr (M > 0 && mu == 90) fresh(nv);
      if constexpr (M > 0 && mu >= 90 && mu < 94) { sfor<10>([&](auto I) { if constexpr (I / 3 == mu - 90) nv[+I] = vmov<I == 2 || I == 7>(nm); }); pin(nv); }
      SB();
    });
    arrive(lds0 + L_C0);
    if constexpr (PRE == 1) __builtin_amdgcn_s_setprio(1);
    check4(cs, lds0 + L_C3);
    {
      u32x4 tb[4][4], fw[4];
      { const int l = lane_id();
        const unsigned a = lds0 + L_I + (l >> 2) * 256 + ((ws ^ ((l >> 2) & 7)) << 5) + (l & 3) * 8, c = lvo + lds0, d = lds0 + L_W + ws * 128 + (lvo & 768) * 2 + ((lvo >> 1) & 112);
        z[0] = tr_at<0, 4096>(a); z[1] = tr_at<0, 4096>(a ^ 128);
        sfor<8>([&](auto I) { tb[I >> 2][I & 3] = l128_at<L_TB + I * 1024>(c); });
        sfor<4>([&](auto I) { fw[I] = l128_at<I * 2048>(d); });
        sgf = (unsigned)(l & 1) * NEG2; pin(sgf); }
      SB();
      sfor<18>([&](auto I) { epi(ic<1>{}, ic<14 + I>{}); });
      ST(4)
      if constexpr (PRE == 2) __builtin_amdgcn_s_setprio(1);
      SQW
      SB();
      auto step = [&](auto Bc, auto Hc) { constexpr int B = Bc, H = Hc;
        sfor<4>([&](auto I) { const u32x4 t = tb[B ? (I & 2) : (I >> 1)][2 * H + (I & 1)], y = H ? zr[I >> 1] : z[I >> 1];
          bo[I] = B ? mfma16(y, t, H ? bo[I] : zero) : mfma16(t, y, H ? bo[I] : zero); pin(bo[I]); });
        if constexpr (!H) { zr[0] = rot90(z[0], sgf); zr[1] = rot90(z[1], sgf); }
        SB(); };
      step(ic<0>{}, ic<0>{}); step(ic<0>{}, ic<1>{});
      sfor<8>([&](auto I) { tb[2 + (I >> 2)][I & 3] = l128_at<L_TB + 8192 + I * 1024>(lvo + lds0); });
      sgf ^= NEG2; pin(sgf);
      SB();
      if constexpr (HF2 == 2) { wait_vm<0>(); hflag(1); ST(6) }
      sfor<4>([&](auto Kc) { constexpr int k = Kc, t = k >> 1, i = 2 * (k & 1);
        float x1 = bo[t][i] + bo[2 + t][i], x2 = bo[t][i] - bo[2 + t][i], y1 = bo[t][i + 1] + bo[2 + t][i + 1], y2 = bo[t][i + 1] - bo[2 + t][i + 1];
        unsigned a1 = fw[t][i], a2 = fw[2 + t][i], b1 = fw[t][i + 1], b2 = fw[2 + t][i + 1], p, q, c, d, sg = sgf;
#define NB " quad_perm:[1,0,3,2] row_mask:0xf bank_mask:0xf\n\t"
        asm volatile("v_lshlrev_b32 %0, 16, %4\n\tv_lshlrev_b32 %1, 16, %6\n\tv_bitop3_b32 %4, %12, %13, %4 bitop3:0x48\n\tv_bitop3_b32 %6, %12, %13, %6 bitop3:0x48\n\t"
                     "v_mul_f32 %0, %8, %0\n\tv_mul_f32 %1, %10, %1\n\tv_lshlrev_b32 %2, 16, %5\n\tv_lshlrev_b32 %3, 16, %7\n\t"
                     "v_fmac_f32_dpp %0, %8, %4" NB "v_fmac_f32_dpp %1, %10, %6" NB "v_bitop3_b32 %5, %12, %13, %5 bitop3:0x48\n\tv_bitop3_b32 %7, %12, %13, %7 bitop3:0x48\n\t"
                     "v_fma_f32 %2, %9, %2, %0\n\tv_fma_f32 %3, %11, %3, %1\n\tv_fmac_f32_dpp %2, %9, %5" NB "v_fmac_f32_dpp %3, %11, %7" NB
                     "v_fma_f32 %4, %0, 2.0, -%2\n\tv_fma_f32 %6, %1, 2.0, -%3\n\tv_cvt_pk_bf16_f32 %0, %2, %3\n\tv_cvt_pk_bf16_f32 %1, %4, %6"
                     : "=&v"(p), "=&v"(q), "=&v"(c), "=&v"(d), "+v"(a1), "+v"(a2), "+v"(b1), "+v"(b2) : "v"(x1), "v"(x2), "v"(y1), "v"(y2), "v"(sg), "s"(0xffff0000u));
        z[0][k] = p; z[1][k] = q;
        SB(); });
      if constexpr (HF2 == 1) { wait_vm<0>(); hflag(1); ST(6) }
      asm volatile("s_nop 1");
      step(ic<1>{}, ic<0>{}); step(ic<1>{}, ic<1>{});
      if constexpr (HF2 == 3) { wait_vm<0>(); hflag(1); ST(6) }
      const int l = lane_id();
      unsigned e = lds0 + L_J + (l & 15) * 256 + (((2 * ws + (l >> 5)) ^ (l & 7)) << 4) + ((l >> 1) & 8); pin(e);
      wait_vm<4>(); sfor<4>([&](auto I) { pin(tw[I]); });
      sfor<4>([&](auto Kc) { constexpr int k = Kc;
        float v[4];
#pragma unroll
        for (int u = 0; u < 4; u += 2) { const float re = bo[k][u], im = bo[k][u + 1], c = tw[k][u], s = tw[k][u + 1];
          v[u] = __builtin_fmaf(-im, s, re * c); v[u + 1] = __builtin_fmaf(re, s, im * c); }
        pin(v[0]); pin(v[1]); pin(v[2]); pin(v[3]);
        lds_st<u32x2, (k & 1) * 4096 + (k >> 1) * 128>(e, u32x2{pk2(v[0], v[1]), pk2(v[2], v[3])}); });
      arrive(lds0 + L_C1);
      ST(5)
      if constexpr (HF2 == 0) { wait_vm<0>(); hflag(1); ST(6) }
      e = lds0 + L_J + ws * 2048 + (l & 15) * 128 + (((l >> 4) ^ ((l >> 1) & 7)) << 4); pin(e);
      await4(lds0 + L_C1);
      z[0] = l128_at<0>(e); z[1] = l128_at<0>(e ^ 64);
      sfor<2>([&](auto H) { zr[H] = z[H] ^ u32x4{0u, NEG2, 0u, NEG2};
        sfor<4>([&](auto I) { bo[I] = mfma16(I < 2 ? z[H] : zr[H], tb[3][2 * H + (I & 1)], H ? bo[I] : zero); }); });
      sfor<2>([&](auto Y) {
        u32x4 o = {pk2(bo[0][2 * Y], bo[1][2 * Y]), pk2(bo[0][2 * Y + 1], bo[1][2 * Y + 1]), pk2(bo[2][2 * Y], bo[3][2 * Y]), pk2(bo[2][2 * Y + 1], bo[3][2 * Y + 1])};
        yst(Y, o); });
      ST(7)
      if constexpr (PRE == 3) __builtin_amdgcn_s_setprio(1);
      SB();
    }
    {
      const int l = lane_id();
      u32x4 t2[7][2];
      u32x4 y[14];
      const float f0 = (float)(56 * ws + 1 + 28 * (l >> 5));
      float dd[2], ab[2], k2[2];
#pragma unroll
      for (int t = 0; t < 2; ++t) { dd[t] = (float)(64 * dt + 32 * t + (l & 31)) * (-1.0f / 448); ab[t] = __builtin_amdgcn_fractf(f0 * dd[t]); k2[t] = 2.f * vcos(dd[t]); }
      sfor<2>([&](auto Kc) { constexpr int k = Kc;
#pragma unroll
        for (int t = 0; t < 2; ++t) {
          float c[14], s[14];
          trig<14>((float)(14 * k) * dd[t] + ab[t], dd[t], k2[t], c, s);
          if constexpr (k == 1) { const float m = f0 > 196.f ? 0.5f : 1.f; c[13] *= m; s[13] *= m; }
#pragma unroll
          for (int e = 0; e < 14; ++e) { const int x = 14 * k + e;
            unsigned v = e < 2 ? pk2(c[e], s[e]) : pka(c[e], s[e]); if (e < 2) pin(v); t2[x >> 2][t][2 * (x & 1) + ((x >> 1) & 1)] = v; }
        }
        SB();
        if constexpr (k == 0) {
          wait_vm<0>(); st_flags<0xff>(lvo << 7, rw, OFF_C + (FY0 + ws * 2048 + fi) * 8, tag);
          ST(13)
          gvo = (unsigned)(FY0 + rt * 256 + 56 * ws) * 8 + (lvo >> 1);
        }
        SB();
      });
      ST(20)
      pace<MY>(lds0 + L_C5);
      ST(21)
      ld_flag(fv, gvo, rw); wait_vm<0>(); pin(fv);
      ST(8)
      SN(15, poll(fv, ~0ull >> 8, gvo, rw, tag))
      f32x16 ac[2][2];
      const unsigned yv = (l >> 5) * (7168 - 512) + lvo;
      ST(9)
      sfor<14>([&](auto I) { ld_sc1<(I & 7) * 512>(y[I], yv, rw, OFF_Y + rt * 57344 + ws * 14336 + (I >> 3) * 4096); });
      SB();
      unsigned pa[8];
      part_at(pa, lds0 + (ws ? 4 + ws : 3) * SL, lvo);
      await4(lds0 + L_C7);
      sfor<7>([&](auto I) { constexpr int i = I;
        wait_vm<12 - 2 * i>();
        if constexpr (i == 0) { ST(10) }
        pin(y[2 * i]); pin(y[2 * i + 1]);
        unsigned p[8];
        sfor<8>([&](auto K) { p[K] = y[2 * i + (K >> 2)][K & 3]; });
        asm volatile("s_nop 1\n\tv_permlane16_swap_b32 %0, %2\n\tv_permlane16_swap_b32 %1, %3\n\tv_permlane16_swap_b32 %4, %6\n\tv_permlane16_swap_b32 %5, %7\n\ts_nop 0\n\t"
                     "v_swap_b32 %1, %4\n\tv_swap_b32 %3, %6\n\ts_nop 1"
                     : "+v"(p[0]), "+v"(p[1]), "+v"(p[2]), "+v"(p[3]), "+v"(p[4]), "+v"(p[5]), "+v"(p[6]), "+v"(p[7]));
        y[2 * i] = u32x4{p[0], p[1], p[2], p[3]}; y[2 * i + 1] = u32x4{p[4], p[5], p[6], p[7]}; pin(y[2 * i]); pin(y[2 * i + 1]);
        sfor<(i < TS ? 4 : 2)>([&](auto K) { ac[K >> 1][K & 1] = mfma32(t2[i][K & 1], y[2 * i + (K >> 1)], i ? ac[K >> 1][K & 1] : zero16); pin(ac[K >> 1][K & 1]); });
        SB();
      });
      constexpr int NR = 14 - 2 * TS;
      sfor<NR>([&](auto Jc) { constexpr int j = Jc, i = TS + j / 2, k0 = j < 2 ? 0 : (j - 2) * 8 / (NR - 2), k1 = j < 2 ? 0 : (j - 1) * 8 / (NR - 2);
        ac[1][j & 1] = mfma32(t2[i][j & 1], y[2 * i + 1], i ? ac[1][j & 1] : zero16); pin(ac[1][j & 1]);
        SB();
        put_part<0, 4096, k0, k1 - k0>(pa, ac[0][0], ac[0][1]);
        SB();
      });
      ST(11)
      arrive(lds0 + L_C2);
      put_part<8192, 4096>(pa, ac[1][0], ac[1][1]);
    }
    arrive(lds0 + L_C6);
    asm volatile("s_waitcnt lgkmcnt(0)" ::: "memory");
    STAMP_END(0)
    __builtin_amdgcn_endpgm();
  } else {
    __builtin_amdgcn_s_setprio(2);
#define RT 3
    const int rot = RT == 0 ? (unsigned)(dt + 2 * (rt & 3)) % 7 : RT == 1 ? 0 : RT == 2 ? ((dt ^ rt) & 1) * 4 : RT == 3 ? 2 * (rt & 3) : RT == 4 ? dt : (unsigned)xcd % 7;
    unsigned urot = (unsigned)(0xca86420ca86420ull >> (4 * rot)); pins(urot);
    auto uof = [&](int i) { return 14 * (i / 7) + ((urot >> (4 * (i % 7))) & 15); };
    unsigned xvo[4], wvo = fv0, wv1 = fv2;
    xvo[0] = (unsigned)(4 * rb + ws + ((tid & 56) << 3)) * (D_ * 2) + ((tid ^ ((tid >> 4) & 3)) & 7) * 16;
    xvo[1] = (xvo[0] ^ 64) + (512 * D_ * 2 - 1024); xvo[2] = xvo[0] + (1024 * D_ * 2 - 2048); xvo[3] = xvo[1] + (1024 * D_ * 2 - 2048);
    sfor<4>([&](auto I) { pin(xvo[I]); });
    pin(wvo); pin(wv1);
    unsigned dv[4], r2, ovo;
    u32x4 xres[2];
    u32x2 cg;
    u32x4 wdf[14][4], hf[28][2], tb[4], xa[2][4], ta[2][4];
    f32x16 accA, ah[2][2];
    float gd, gx;
    auto ev = [&](auto Ic) { constexpr int id = Ic % 1024, pt = Ic / 1024;
      if constexpr (id >= eX && id < eX + 7) { constexpr int j = id - eX;
        dma4<XS[j] * SL>(lw, xvo[0], xvo[1], xvo[2], xvo[3], rx, rx, kof(j) * 128);
      } else if constexpr (id >= eW && id < eW + 14) { constexpr int j = id - eW;
        dma1<WS[j] * SL, pt>(lw, pt < 2 ? wvo : wv1, (pt & 1) ? ru : rg, kof(j % 7) * CH + (j >= 7 ? 14 * 128 : 0));
      } else if constexpr (id >= eD && id < eD + 14) { constexpr int i = id - eD;
        if constexpr (i == 9 && pt == 3) asm volatile("s_waitcnt lgkmcnt(0)" ::: "memory");
        dma1<DS[i] * SL, pt>(lw, dv[pt], rd, uof(i) * (64 * D_ * 2));
      } else if constexpr (id >= eDR && id < eDR + 14) { constexpr int i = id - eDR;
        wvm<pos(id) - pos(eD + i)>();
        if constexpr (i == 3 || i == 11) { ao -= (i == 3 ? 1 : 4) * SL; wo -= (i == 3 ? 1 : 4) * SL; pin(ao); pin(wo); }
        sfor<4>([&](auto K) { constexpr int o = (DS[i] - (i < 3 ? 5 : i < 11 ? 4 : 0)) * SL + (K >> 1) * 2048; wdf[i][K] = tr_at<o, o + 1024>((K & 1) ? wo : ao); });
      } else if constexpr (id >= eH && id < eH + 28) { constexpr int s = id - eH;
        const unsigned so = hso + uof(s >> 1) * 4096;
        sfor<2>([&](auto C) { constexpr int c = C;
          if constexpr (s < 7) ld_sc1<(s & 1) * 2048>(hf[s][c], lvo, rw, so + c * 28 * 4096);
          else {
            if constexpr (s < 21) hf[s][c] = hf[s - 7][c];
            else hf[s][c] = wdf[(s - 14) >> 1][2 * (s & 1) + c];
            ld_sc1_over<(s & 1) * 2048>(hf[s][c], lvo, rw, so + c * 28 * 4096);
          }
        });
      } else if constexpr (id >= eXR && id < eXR + 7) { constexpr int j = id - eXR;
        unsigned to = lvo + (lds0 + L_T1 + kof(j) * 4096); pin(to);
        xread(ic<j>{}, xa[j & 1]);
        sfor<4>([&](auto I) { ta[j & 1][I] = l128_at<I * 1024>(to); });
      } else if constexpr (id >= eTM && id < eTM + 7) { constexpr int j = id - eTM;
        accA = mfma32(xa[j & 1][pt], ta[j & 1][pt], j + pt ? accA : zero16); pin(accA);
      } else if constexpr (id == eEP) { ld_flag(ep, 0u, rw);
      } else if constexpr (id == eTAG) { wvm<pos(eTAG) - pos(eEP)>(); mk_tag();
      } else if constexpr (id == eDV) {
        const int l = lane_id();
        dv[0] = (unsigned)((64 * (ws >> 1) + 16 * (ws & 1) + (l >> 3)) * D_ * 2 + dt * 128 + (((l & 7) ^ ((l >> 2) & 4)) * 16));
#pragma unroll
        for (int i = 1; i < 4; ++i) { dv[i] = dv[0] + ((8 * (i & 1) + 16 * (i & 2)) * D_ * 2 - 1024 * i); pin(dv[i]); }
        ao = lw + 5 * SL + (4 * (l >> 5) + ((l & 15) >> 2)) * 128 + ((l >> 3) & 1) * 64 + ((l >> 4) & 1) * 32 + (l & 3) * 8;
        wo = ao ^ 64; pin(ao); pin(wo);
      } else if constexpr (id == eG0) {
        if constexpr (pt == 0) { const int l = lane_id(); sgf = (unsigned)(l & 1) * NEG2; gd = (float)(l & 31) * (1.0f / 32); gx = (float)(8 * (l & 32) + 4 * rb + ws) * (1.0f / 64) * gd; pin(sgf); pin(gd); pin(gx);
          sfor<4>([&](auto I) { fresh(tb[I]); }); }
        const float x = (float)(8 * (pt >> 1) + 2 * (pt & 1)) * gd + gx, y = x + gd;
        tb[0][pt] = pk2(vcos(x), vcos(y)); tb[2][pt] = pk2(vsin(x), vsin(y)); tb[1][pt] = tb[0][pt] ^ sgf; tb[3][pt] = tb[2][pt] ^ sgf;
        sfor<4>([&](auto I) { pin(tb[I]); });
      } else if constexpr (id == eTD) {
        if constexpr (TD) asm volatile("s_mov_b32 m0, %0\n\ts_mov_b64 exec, 0x7f\n\tbuffer_load_dword %1, %2, %3 offen lds\n\ts_mov_b64 exec, -1" ::"n"(L_PF), "v"(lvo * 8), "s"(rd), "s"((896 + 4 * fi + ws) * (D_ * 2)) : "memory");
      } else if constexpr (id == eTB) {
        asm volatile("s_add_u32 m0, %0, %1\n\ts_nop 0\n\tglobal_load_lds_dwordx4 %2, %3\n\tglobal_load_lds_dwordx4 %2, %3 offset:1024\n\t"
                     "global_load_lds_dwordx4 %2, %3 offset:2048\n\tglobal_load_lds_dwordx4 %2, %3 offset:3072" ::"s"(lw), "n"(L_TB), "v"(lvo), "s"(TAB.tb[4 * ws]) : "memory", "scc");
      } else if constexpr (id == eCL0 || id == eCL1) {
        ld8_sc1<0>(cg, lvo >> 1, rw, OFF_S + (unit + 14 * (id - eCL0)) * 2048 + ws * 512);
      } else if constexpr (id == eCW0 || id == eCW1) {
        wvm<pos(id) - pos(id - 2)>(); pin(cg);
        int it = 0;
        if (__builtin_amdgcn_ballot_w64(cg[1] != (unsigned)tag) != 0) {
#pragma unroll 1
          for (it = 1; it < SPIN_CAP; ++it) {
            __builtin_amdgcn_s_sleep(SLP);
            ld8_sc1<0>(cg, lvo >> 1, rw, OFF_S + (unit + 14 * (id - eCW0)) * 2048 + ws * 512); wait_vm<0>(); pin(cg);
            if (__builtin_amdgcn_ballot_w64(cg[1] != (unsigned)tag) == 0) break;
          }
        }
        SN(22 + id - eCW0, it)
        lds_st<unsigned, 0>(lds0 + L_CS + ws * 256 + (lvo >> 2), cg[0]);
      } else if constexpr (id == eZ) {
        if constexpr (pt == 1) { const unsigned a = lds0 + L_ST + ws * 128 + ((lvo >> 5) & 16); sfor<4>([&](auto J) { bo[J] = lds_ld<f32x4, 32 * J>(a); }); }
        if constexpr (pt == 2 || pt == 3) { constexpr int q = pt - 2;
          float v[8];
#pragma unroll
          for (int e = 0; e < 8; ++e) { v[e] = accA[8 * q + e] * bo[2 * q + (e >> 2)][e & 3]; pin(v[e]); }
          z[q] = u32x4{pk2(v[0], v[1]), pk2(v[2], v[3]), pk2(v[4], v[5]), pk2(v[6], v[7])};
          ah[0][0] = mfma32(z[q], tb[q], q ? ah[0][0] : zero16); pin(ah[0][0]);
          zr[q] = rot90(z[q], sgf);
        }
        if constexpr (pt == 4) { ah[0][0] = mfma32(zr[1], tb[3], mfma32(zr[0], tb[2], ah[0][0])); pin(ah[0][0]); }
        if constexpr (pt == 0) {
          const unsigned so = OFF_Z + unit * 131072 + (4 * rb + ws) * 128, vo = (lvo & 512) * 32 + ((lvo >> 2) & 127);
          sfor<8>([&](auto I) { st4_sc1(pk2(ah[0][0][2 * I], ah[0][0][2 * I + 1]), vo, rw, so + ((I >> 1) * 4 + (I & 1)) * 8192); });
        }
      } else if constexpr (id == eZF) {
        wvm<before(eZF) - pos(eZ)>();
        st_flag1(rw, OFF_C + (FZ0 + unit * 64 + rb * 4 + ws) * 8, tag);
        ST(29)
      } else if constexpr (id == ePO) {
        u64 n;
        asm volatile("s_memrealtime %0\n\ts_waitcnt lgkmcnt(0)" : "=s"(n) :: "memory");
        st_flag1(rw, OFF_C + (FP0 + ws * 256 + xcd * 32 + idx) * 8, n << 32 | (unsigned)tag);
      } else if constexpr (id == eFP) {
        ld8_sc1<0>(cg, lvo >> 1, rw, OFF_C + (FP0 + g * 256 + ws * 64) * 8);
      } else if constexpr (id == ePP) {
        wvm<pos(ePP) - pos(eFP)>(); pin(cg);
        asm volatile("ds_max_u32 %0, %1" ::"v"(lds0 + L_C5), "v"(cg[0] == (unsigned)tag ? cg[1] : 0u) : "memory");
      } else if constexpr (id == eFLZ) {
        gvo = selm(0xffffull, (FZ0 + unit * 64 + 16 * ws) * 8, (FW0 + (fi * 2341 >> 16) * 32 + 8 * ws - 16) * 8) + (lvo >> 1);
        ld_flag(fv, gvo, rw);
      } else if constexpr (id == ePZ) { wvm<pos(ePZ) - pos(eFLZ)>(); pin(fv); ST(13) SN(21, poll(fv, 0xffffffull, gvo, rw, tag)) ST(14)
      } else if constexpr (id == eLZ) {
        const unsigned v = lvo ^ ((lvo >> 3) & 96);
        if constexpr (EXP & 2) { dma2s<L_I>(lds0 + ws * 2048, v, v ^ 128, rd, ws * 2048); dma2s<L_W>(lds0 + ws * 2048, lvo, lvo, rd, 8192 + ws * 2048); } else {
        dma2s<L_I>(lds0 + ws * 2048, v, v ^ 128, rw, OFF_Z + fi * 8192 + ws * 2048);
        dma2s<L_W>(lds0 + ws * 2048, lvo, lvo, rw, OFF_W + fi * 8192 + ws * 2048); }
      } else if constexpr (id == eR2) {
        ld_u32_sc1(r2, (lvo & 512) * 7 + ((lvo >> 2) & 127), rw, OFF_R + (2 * rt * 28 + (ws >> 1)) * 128);
      } else if constexpr (id == eRW) {
        wvm<pos(eRW) - pos(eR2)>(); pin(r2);
        lds_st<unsigned, 0>(lds0 + L_R2 + ws * 256 + (lvo >> 2), r2);
      } else if constexpr (id == eFL1) {
        gvo = (unsigned)(FH0 + dt * 4096 + rt * 32) * 8 + ((lvo >> 1) & 255);
        hso = (2 * rt * 28 + (ws >> 1)) * 4096 + (ws & 1) * 1024;
        ld_flag(fv, gvo, rw);
      } else if constexpr (id == eFL2) { gvo += 16384; ST(20) pace<M2>(lds0 + L_C5); ST(25) ld_flag(fv, gvo, rw);
      } else if constexpr (id == eP1) { wvm<pos(eP1) - pos(eFL1)>(); pin(fv); ST(5) SN(19, poll(fv, 0x3fff3fffull, gvo, rw, tag)) ST(6)
      } else if constexpr (id == eP2) { wvm<pos(eP2) - pos(eFL2)>(); pin(fv); ST(8) SN(15, poll(fv, 0x3fff3fffull, gvo, rw, tag)) ST(9)
        if (b == 0 && ws == 0) st_flag1(rw, OFF_C, tag);
      } else if constexpr (id == eRES && pt != (RS == 22 ? 0 : RS == 18 ? 1 : 2)) {
      } else if constexpr (id == eRES) {
        const int l = lane_id();
        ovo = (unsigned)((64 * (8 * ws + (l >> 3)) + 2 * rt) * D_ + dt * 64 + (l & 7) * 8) * 2;
        ld_pl<0>(xres[0], ovo, rx, 0); ld_pl<D_ * 2>(xres[1], ovo, rx, 0);
      } else if constexpr (id == eTH) { wait_vm<pt>();
      } else if constexpr (id == eAW) { await4(lds0 + (pt ? L_C3 + 4 * pt : L_C0));
      } else if constexpr (id == eAR && pt == 2) { arrive(lds0 + L_C7);
      } else if constexpr (id == eAR) { wvm<pos(Ic) - pos(pt ? eW + 13 : eLZ)>(); arrive(lds0 + (pt ? L_C2 : L_C3));
      }
    };
    auto run = [&](auto Mc) { constexpr int R = 0, mk = Mc, i0 = sidx(mk) + 1;
      [[maybe_unused]] u64 _q[10];
      STE(mk, 0)
      sfor<seglen(i0)>([&](auto K) { ev(ic<SEQ_R[i0 + K].id>{}); SB(); STE(mk, 1 + K) });
      STF(mk, seglen(i0))
    };
    if constexpr (HV >= 3) { sfor<4>([&](auto I) { ev(ic<eG0 + 1024 * I>{}); }); ev(ic<eDV>{}); SB(); }
    SQD
    sfor<12>([&](auto Nc) { constexpr int n = Nc;
      SQ(n, 0)
      if constexpr (SEC == 3 && n == 0) second();
      vbar<pos(mP + n) - need(n)>();
      if constexpr (n == 0) { pins(kx); ro = srd_of<0>(kx); rx = srd_of<1>(kx); }
      SQ(n, 1)
      if constexpr (n == 0) { ST(1) }
      if constexpr (n == 5) { ST(2) }
      if constexpr (n == 7) { ST(3) }
      if constexpr (n == 11) { ST(4) __builtin_amdgcn_s_setprio(PRH); }
      run(ic<mP + n>{});
    });
    SQW
    GQD
    sfor<28>([&](auto Sc) { constexpr int s = Sc;
      GQ(s, 0)
      wvm<pos(mK + s) - pos(eH + s)>();
      GQ(s, 1)
      if constexpr (s == 0) { ST(7) }
      if constexpr (s == 14) { ST(10) }
      if constexpr (s >= GS) pin(hf[s][1]);
      sfor<(s < GS ? 4 : 2)>([&](auto I) { constexpr int a = I & 1, c = I >> 1;
        if constexpr (!a) pin(hf[s][c]);
        ah[c][a] = mfma32(wdf[s >> 1][2 * (s & 1) + a], hf[s][c], s == 0 ? zero16 : ah[c][a]); pin(ah[c][a]); });
      SB();
      run(ic<mK + s>{});
    });
    unsigned pa[8];
    part_at(pa, lw, lvo);
    SB();
    constexpr int NG = 56 - 2 * GS;
    sfor<NG>([&](auto Jc) { constexpr int j = Jc, s = GS + j / 2, k0 = j < 2 ? 0 : (j - 2) * 8 / (NG - 2), k1 = j < 2 ? 0 : (j - 1) * 8 / (NG - 2);
      ah[1][j & 1] = mfma32(wdf[s >> 1][2 * (s & 1) + (j & 1)], hf[s][1], ah[1][j & 1]); pin(ah[1][j & 1]);
      SB();
      put_part<0, SL, k0, k1 - k0>(pa, ah[0][0], ah[0][1]);
      SB();
    });
    if constexpr (NG == 0) put_part<0, SL>(pa, ah[0][0], ah[0][1]);
    ST(11)
    GQW
    const int l = lane_id(), ch = l & 7;
    sfor<4>([&](auto K) { pa[4 + K] += SL; });
    put_part<2 * SL, SL>(pa, ah[1][0], ah[1][1]);
    arrive(lds0 + L_C4);
    unsigned qo = lds0 + ((8 * ws + (l >> 3)) ^ (2 * ch)) * 16, qc = qo + 5 * SL + ch * 1024, qb = qc - 2 * SL; pin(qc); pin(qb);
    f32x4 a[2][2];
    ST(23)
    await4(lds0 + L_C4);
    ST(24)
    sfor<2>([&](auto R) {
      const unsigned qh = qo + 2 * R * SL + (ch >> 2) * (SL << R) + (ch & 3) * 1024;
      const float r = lds_ld<float, 0>(lds0 + L_R2 + R * 128 + ws * 32 + (l >> 3) * 4);
      sfor<2>([&](auto K) { a[R][K] = ((lds_ld<f32x4, K * 512>(qh) + lds_ld<f32x4, K * 512 + 4096>(qh)) + (lds_ld<f32x4, K * 512 + 8192>(qh) + lds_ld<f32x4, K * 512 + 12288>(qh))) * r; }); });
    ST(26)
    wait_vm<0>(); pin(xres[0]); pin(xres[1]);
    sfor<4>([&](auto I) { constexpr int R = I >> 1, K = I & 1;
      a[R][K] += f32x4{bflo(xres[R][2 * K]), bfhi(xres[R][2 * K]), bflo(xres[R][2 * K + 1]), bfhi(xres[R][2 * K + 1])}; pin(a[R][K]); });
    ST(22)
    sfor<2>([&](auto R) {
      if constexpr (R) await4(lds0 + L_C6); else await4<8>(lds0 + L_C2);
      if constexpr (R) { ST(12) }
      f32x4 c[2][4];
      sfor<8>([&](auto I) { constexpr int o = R * 8192 + (I & 4) * 128, p = I & 3; c[I >> 2][p] = lds_ld<f32x4, o + (p ? p - 1 : 0) * SL>(p ? qc : qb); });
      SB();
      sfor<2>([&](auto K) { a[R][K] += (c[K][0] + c[K][1]) + (c[K][2] + c[K][3]); });
      st16_sc1<R * D_ * 2>(u32x4{pk2(a[R][0][0], a[R][0][1]), pk2(a[R][0][2], a[R][0][3]), pk2(a[R][1][0], a[R][1][1]), pk2(a[R][1][2], a[R][1][3])}, ovo, ro, 0); });
    asm volatile(".Lpf_end:");
    STAMP_END(1)
    __builtin_amdgcn_endpgm();
  }
  } else {
    constexpr int RP = 8224, L1 = 0, L2 = 65 * 1024, L3 = 140 * 1024;
    pins(w);
    const int wn = w >> 2;
    srd_t rs;
    asm volatile("s_load_dwordx4 %0, %2, %3 offset:32" : "=&s"(rs), "+s"(b) : "s"(kp), "s"(wn * 16) : "memory");
    pin(tid);
    pins(idx); pins(ws);
    const int xcd = b & 7, g = xcd >> 1;
    const int t = 4 * xcd + ((idx >> 1) & 3), u2 = (t & 7) < 6 ? (UM ? 2 * g + 4 * (t & 6) + (t & 1) : 6 * g + (t & 7)) : 24 + g;
    const int l = lane_id(), v = l & 15, gq = l >> 4;
    const unsigned lvo = (unsigned)l * 16, so = (unsigned)(128 * (t >> 1) + 32 * (w >> 1) + 16 * (t & 1)) * (DF_ * 2);
    const unsigned cvo = (unsigned)((l >> 3) * F_ * 2 + (l & 7) * 16), cso = (unsigned)((224 * (u2 & 1) + 56 * ws) * F_ * 2 + (u2 >> 1) * 128);
    srd_t r = (w & 1) ? rc : ra; pins(r);
    u32x4 in[8], wv[2][7], sel;
    unsigned jk;
    ld_flag(ep, 0u, rw);
    asm volatile("s_waitcnt lgkmcnt(0)" : "+s"(rs));
    sfor<7>([&](auto I) { ld_pl<0>(wv[0][I], cvo, rs, cso + I * (8 * F_ * 2)); });
    asm volatile("s_barrier" ::: "memory");
    auto rows = [&]() { dma4<L1>(lds0 + w * RP, lvo, lvo, lvo, lvo, r, r, so); dma4<L1 + 4096>(lds0 + w * RP, lvo, lvo, lvo, lvo, r, r, so + 4096); };
    if (w < SW) rows();
    const int fp = 3 - (v >> 2) + (v & 3);
    unsigned p0 = lds0 + L1 + (v >> 2) * RP + (4 * gq + (v & 3)) * 448 + 8 * gq + 64 * w, p1 = lds0 + L2 + w * 8192 + fp * 256 + (((l >> 2) ^ (4 * (fp & 3))) << 4);
    unsigned p2 = lds0 + L2 + w * 7168 + 1024 + (lvo ^ (gq << 6)), vo = (unsigned)(gq * 8192 + v * 16);
    unsigned p3 = lds0 + L3 + (4 * (l & 1) + (l >> 4) + 2 * (l & 8)) * 16 + (l & 6) * 2;
#pragma unroll
    for (int i = 0; i < 4; ++i) sel[i] = selm(0x0003000300030003ull << (2 * i), 0x3f80u << ((l & 1) * 16), 0u);
    pin(p0); pin(p1); pin(p2); pin(p3); pin(vo); pin(sel);
    auto sums = [&](auto Pc) { constexpr int P = Pc;
      f32x4 c = zero;
      sfor<7>([&](auto I) { pin(wv[P][I]); c = mfma16(sel, wv[P][I], c); });
      lds_st<f32x4, L3 + P * 8192>(lds0 + w * 1024 + lvo, c);
    };
    auto total = [&](auto Pc) { constexpr int P = Pc;
      float s[2][8];
      sfor<16>([&](auto I) { s[I >> 3][I & 7] = lds_ld<float, P * 8192 + (I >> 1) * 1024 + (I & 1) * 128>(p3); });
      u32x4 o;
      sfor<2>([&](auto I) { const float y = ((s[I][0] + s[I][1]) + (s[I][2] + s[I][3])) + ((s[I][4] + s[I][5]) + (s[I][6] + s[I][7])), hi = bfhi(__float_as_uint(y)); o[2 * I] = pk2(hi, y - hi); o[2 * I + 1] = (unsigned)tag; });
      st16_sc1(o, lvo, rw, OFF_S + (u2 + 28 * P) * 1024);
    };
    ST(5)
    if (w < SW) wait_vm<8>(); else wait_vm<0>();
    ST(6)
    sums(ic<0>{});
    bar();
    mk_tag();
    if (w == 7) total(ic<0>{});
    if (w >= SW) rows();
    wait_vm<0>();
    bar();
    ST(7)
    sfor<8>([&](auto I) { in[I] = tr_at<8 * I, 8 * I + 4 * RP>(p0); });
    { unsigned j; asm volatile("global_load_dword %0, %1, %2" : "=v"(j) : "v"(tid * 64), "s"(&TAB) : "memory"); jk = j; }
    sfor<7>([&](auto I) { ld_pl<14 * 128>(wv[1][I], cvo, rs, cso + I * (8 * F_ * 2)); });
    ST(1)
    SB();
    sfor<8>([&](auto I) { lds_st<u32x4, I * 1024>(p1, in[I]); });
    bar();
    ST(2)
    sfor<7>([&](auto I) { in[I] = l128_at<I * 1024>(p2); });
    SB();
    sfor<7>([&](auto I) { st16_sc1(in[I], vo, rw, OFF_W + (28 * w + 4 * I) * 8192 + t * 256); });
    wait_vm<7>(); pin(jk);
    sums(ic<1>{});
    bar();
    ST(3)
    if (w == 1) { total(ic<1>{}); wait_vm<1>(); } else wait_vm<0>();
    st_flag1(rw, OFF_C + (FW0 + w * 32 + t) * 8, tag);
    STAMP_END(2 + wn)
    __builtin_amdgcn_endpgm();
  }
}

extern "C" void kernel_launch(void* const* d_in, const int* in_sizes, int n_in, void* d_out, int out_size, void* d_ws, size_t ws_size,
                              hipStream_t stream) {
  (void)hipFuncSetAttribute((const void*)fwd, hipFuncAttributeMaxDynamicSharedMemorySize, 163840);
  constexpr unsigned FM = 0x00020000u, NX = S_ * D_ * 2, NW = D_ * F_ * 2, NF = S_ * DF_ * 2;
  fwd<<<256, 512, 163840, stream>>>(d_in[0], NX, FM, d_in[0], NX + 1024, FM, d_in[4], NW, FM, d_in[5], NW + 1024, FM, d_ws, OFF_END, FM,
                                    d_in[6], NW, FM, d_in[1], NF, FM, d_in[2], NF, FM, d_out, NX, FM, d_in[0], NX, FM STAMP_PASS);
}
```
